```python
import torch

def _softmax(x, axis=-1):
    return torch.softmax(x, dim=axis)
def _gelu(x):
    return torch.nn.functional.gelu(x, approximate="tanh")
def _silu(x):
    return torch.nn.functional.silu(x)
def _sigmoid(x):
    return torch.sigmoid(x)
def _layernorm(x, axis=-1, eps=1e-5):
    m = x.mean(dim=axis, keepdim=True)
    v = x.var(dim=axis, keepdim=True, unbiased=False)
    return (x - m) / torch.sqrt(v + eps)
def _scatter_add(tgt, idx, src, axis=0):
    return tgt.clone().index_add_(axis, idx, src)
def _segment_sum(src, seg, n):
    out = torch.zeros((n,) + src.shape[1:], dtype=src.dtype)
    return out.index_add_(0, seg, src)
def _pad(x, pw):
    flat = tuple(v for pair in reversed(pw) for v in pair)
    return torch.nn.functional.pad(x, flat)

kernel_name = 'gated_mix__grouped_kv_mqa__sliding_block__prompt'

def setup_inputs():

    _w = (lambda a: (lambda t: t.to(torch.bfloat16) if t.is_floating_point() else t)(torch.as_tensor(a)))
    def _A_setup():
        B, S, D, H = 1, 2048, 1024, 32
        Dh = D // H
        return {
            "x": _w((lambda *s: torch.randn(*s, dtype=torch.bfloat16) * 0.05)(B, S, D)),
            "wq": _w((lambda *s: torch.randn(*s, dtype=torch.bfloat16) * 0.8)(D, H * Dh)),
            "wk": _w((lambda *s: torch.randn(*s, dtype=torch.bfloat16) * 0.8)(D, Dh)),
            "wv": _w((lambda *s: torch.randn(*s, dtype=torch.bfloat16) * 0.2)(D, Dh)),
            "wo": _w((lambda *s: torch.randn(*s, dtype=torch.bfloat16) * 0.2)(H * Dh, D)),
        }
    _A_in = _A_setup()

    def _B_setup():
        B, S, D = 1, 2048, 1024
        return {
            "x": _w((lambda *s: torch.randn(*s, dtype=torch.bfloat16) * 0.05)(B, S, D)),
            "wqkv": _w((lambda *s: torch.randn(*s, dtype=torch.bfloat16) * 0.8)(D, 3 * D)),
            "wo": _w((lambda *s: torch.randn(*s, dtype=torch.bfloat16) * 0.05)(D, D)),
        }
    _B_in = _B_setup()

    return {
        "A_x": _A_in["x"],
        "A_wq": _A_in["wq"],
        "A_wk": _A_in["wk"],
        "A_wv": _A_in["wv"],
        "A_wo": _A_in["wo"],
        "B_x": _B_in["x"],
        "B_wqkv": _B_in["wqkv"],
        "B_wo": _B_in["wo"],
        "gate_w": _w((lambda *s: torch.randn(*s, dtype=torch.bfloat16) * 0.05)(1024) * 16.0),
    }

def reference(A_x, A_wq, A_wk, A_wv, A_wo, B_x, B_wqkv, B_wo, gate_w):
    def _A_ref(x, wq, wk, wv, wo):
        B, S, D = x.shape
        H = 32
        Dh = D // H
        q = (lambda x, *d: x.permute(*d))((x @ wq).reshape(B, S, H, Dh), 0, 2, 1, 3)
        k = x @ wk
        v = x @ wv
        scores = torch.einsum("bhqd,bkd->bhqk", q, k) * (Dh ** -0.5)
        p = _softmax(scores, axis=-1)
        o = torch.einsum("bhqk,bkd->bhqd", p, v)
        return (lambda x, *d: x.permute(*d))(o, 0, 2, 1, 3).reshape(B, S, H * Dh) @ wo

    def _B_ref(x, wqkv, wo):
        B, S, D = x.shape
        H = 32; Dh = D // H; W = 256
        qkv = (lambda x, *d: x.permute(*d))((x @ wqkv).reshape(B, S, 3, H, Dh), 2, 0, 3, 1, 4)
        q, k, v = qkv[0], qkv[1], qkv[2]
        scores = torch.einsum("bhqd,bhkd->bhqk", q, k) * (Dh ** -0.5)
        idx = torch.arange(S)
        mask = (idx[:, None] - idx[None, :] >= 0) & (idx[:, None] - idx[None, :] < W)
        scores = torch.where(mask[None, None], scores, -1e9)
        p = _softmax(scores, axis=-1)
        o = torch.einsum("bhqk,bhkd->bhqd", p, v)
        return (lambda x, *d: x.permute(*d))(o, 0, 2, 1, 3).reshape(B, S, D) @ wo

    x = A_x
    g = _sigmoid((x * gate_w).sum(-1, keepdims=True))
    a = _A_ref(x, A_wq, A_wk, A_wv, A_wo)
    b = _B_ref(x, B_wqkv, B_wo)
    return g * a + (1 - g) * b
```

```cpp
#include <hip/hip_runtime.h>
#include <hip/hip_bf16.h>
#include <stdint.h>

typedef float f32x16 __attribute__((ext_vector_type(16)));
typedef float f32x4 __attribute__((ext_vector_type(4)));
typedef float f32x2 __attribute__((ext_vector_type(2)));
typedef short bf16x8 __attribute__((ext_vector_type(8)));
typedef short s16x4 __attribute__((ext_vector_type(4)));
typedef __bf16 bf16x2 __attribute__((ext_vector_type(2)));
typedef unsigned u32x4 __attribute__((ext_vector_type(4)));
typedef unsigned u32x2 __attribute__((ext_vector_type(2)));
typedef __attribute__((address_space(3))) const char* lds_cptr;
typedef __attribute__((address_space(3))) char* lds_ptr;

#define DI __device__ __forceinline__
#define SBAR() __builtin_amdgcn_sched_barrier(0)
#define PIN(x) asm volatile("" : "+v"(x))
#define MFMA32(a, b, c) __builtin_amdgcn_mfma_f32_32x32x16_bf16(a, b, c, 0, 0, 0)
#define MFMA16(a, b, c) __builtin_amdgcn_mfma_f32_16x16x32_bf16(a, b, c, 0, 0, 0)

constexpr int SEQ = 2048, DM = 1024;
constexpr float QSCALE = 0.17677669529663687f * 1.4426950408889634f;

constexpr size_t WS_QA = 0;
constexpr size_t WS_KVA = 4u << 20;
constexpr size_t WS_G = WS_KVA + (256u << 10);
constexpr size_t WS_QB = 5u << 20;
constexpr size_t WS_KVB = 9u << 20;
constexpr size_t WS_O = 17u << 20;

DI unsigned rfl(unsigned x) { return __builtin_amdgcn_readfirstlane(x); }
DI unsigned cvtpk(float lo, float hi) {
  f32x2 v = {lo, hi};
  bf16x2 b = __builtin_convertvector(v, bf16x2);
  return __builtin_bit_cast(unsigned, b);
}
DI bf16x8 pack8(const f32x16& a, int o) {
  u32x4 r = {cvtpk(a[o], a[o + 1]), cvtpk(a[o + 2], a[o + 3]), cvtpk(a[o + 4], a[o + 5]), cvtpk(a[o + 6], a[o + 7])};
  return __builtin_bit_cast(bf16x8, r);
}
DI bf16x8 pack8s(const f32x16& a, int o, float s) {
  u32x4 r = {cvtpk(a[o] * s, a[o + 1] * s), cvtpk(a[o + 2] * s, a[o + 3] * s), cvtpk(a[o + 4] * s, a[o + 5] * s),
             cvtpk(a[o + 6] * s, a[o + 7] * s)};
  return __builtin_bit_cast(bf16x8, r);
}
template <int OFF>
DI void glds16(unsigned voff, const void* sbase, unsigned lds_base) {
  unsigned sv;
  asm volatile("s_mov_b32 %0, m0\n\ts_mov_b32 m0, %3\n\ts_nop 0\n\tglobal_load_lds_dwordx4 %1, %2 offset:%4\n\ts_mov_b32 m0, %0"
               : "=&s"(sv) : "v"(voff), "s"(sbase), "s"(lds_base), "i"(OFF) : "memory");
}
DI bf16x8 lds_rd128(lds_cptr p) { return *(const __attribute__((address_space(3))) bf16x8*)p; }
DI s16x4 lds_tr(lds_cptr p) {
  typedef short v4i16_t __attribute__((ext_vector_type(4)));
  return __builtin_bit_cast(s16x4, __builtin_amdgcn_ds_read_tr16_b64_v4i16((__attribute__((address_space(3))) v4i16_t*)p));
}
DI bf16x8 cat8(s16x4 lo, s16x4 hi) { return (bf16x8){lo[0], lo[1], lo[2], lo[3], hi[0], hi[1], hi[2], hi[3]}; }
DI float bf2f(unsigned short b) { return __uint_as_float((unsigned)b << 16); }

constexpr int P_SLOT = 53248;
constexpr int P_XOFF = 0, P_WOFF = 16384, P_KVOFF = 49152;
constexpr int P_LDS = 3 * P_SLOT;

struct PFrag { bf16x8 x[2]; bf16x8 w[2]; };

template <int MODE>
DI void proj_body(const char* __restrict__ xg, const char* __restrict__ wg, int ldw_bytes, const char* __restrict__ wkv,
                  char* __restrict__ ws, int mb, int nb, int ni, char* lds, u32x4 gx0, u32x4 gx1, u32x4 gw0, u32x4 gw1,
                  int grow) {
  constexpr bool SIDE = MODE >= 2;
  constexpr int NP = SIDE ? 7 : 6;
  const int tid = threadIdx.x, lane = tid & 63;
  const int w = rfl(tid >> 6);
  const int wm = w & 1, wn = w >> 1;
  const int r = lane & 31, h = lane >> 5;
  const unsigned lds0 = (unsigned)(uintptr_t)lds;
  unsigned xo[2], wo[4];
#pragma unroll
  for (int e = 0; e < 2; ++e) {
    const int row = 16 * w + 8 * e + (lane >> 3);
    const int c = (lane & 7) ^ (4 * e + (lane >> 4));
    xo[e] = row * 2048 + c * 16;
  }
#pragma unroll
  for (int e = 0; e < 4; ++e) {
    const int krow = 8 * w + 2 * e + (lane >> 5);
    const int c = (lane & 31) ^ ((krow & 3) << 2);
    wo[e] = krow * ldw_bytes + c * 16;
  }
  const unsigned kvo = w * 512 + (lane & 31) * 16;
  const char* xsrc = xg + (size_t)mb * 128 * 2048;
  auto dma = [&](int st, int slot) {
    const unsigned lb = lds0 + slot * P_SLOT;
    const char* xs = xsrc + st * 128;
    const char* wsrc = wg + (size_t)st * 64 * ldw_bytes;
    glds16<0>(xo[0], xs, lb + P_XOFF + w * 2048);
    glds16<0>(xo[1], xs, lb + P_XOFF + w * 2048 + 1024);
    glds16<0>(wo[0], wsrc, lb + P_WOFF + w * 4096);
    glds16<0>(wo[1], wsrc, lb + P_WOFF + w * 4096 + 1024);
    glds16<0>(wo[2], wsrc, lb + P_WOFF + w * 4096 + 2048);
    glds16<0>(wo[3], wsrc, lb + P_WOFF + w * 4096 + 3072);
    if constexpr (SIDE) {
      if (lane < 32) glds16<0>(kvo, wkv + st * 4096, lb + P_KVOFF + w * 512);
    }
  };
  const int t3 = (r >> 1) & 7;
  const int xa0 = P_XOFF + (wm * 64 + r) * 128 + ((h ^ t3) << 4);
  const int g16 = lane >> 4, q4 = (lane & 15) >> 2, p4 = lane & 3;
  const int wc = wn * 8 + 2 * (g16 & 1) + (p4 >> 1);
  const int wa0 = P_WOFF + (8 * h + q4) * 512 + ((wc ^ (q4 << 2)) << 4) + 8 * (p4 & 1);
  const int ss = w >> 1;
  const int sxa = P_XOFF + (32 * (2 * (ni & 1) + (w & 1)) + r) * 128 + (((2 * ss + h) ^ t3) << 4);
  const int swa = P_KVOFF + (16 * ss + 8 * h + q4) * 64 + 32 * (g16 & 1) + 8 * p4;

  auto rd = [&](PFrag& f, int slot, int s) {
    lds_cptr b = (lds_cptr)lds + slot * P_SLOT;
    f.x[0] = lds_rd128(b + (xa0 ^ (s << 5)));
    f.x[1] = lds_rd128(b + (xa0 ^ (s << 5)) + 4096);
#pragma unroll
    for (int j = 0; j < 2; ++j) {
      lds_cptr p = b + (wa0 ^ (j << 6)) + s * 8192;
      f.w[j] = cat8(lds_tr(p), lds_tr(p + 2048));
    }
  };
  f32x16 acc[2][2] = {};
  f32x16 sacc = {};
  auto mma = [&](const PFrag& f) {
#pragma unroll
    for (int i = 0; i < 2; ++i)
#pragma unroll
      for (int j = 0; j < 2; ++j) {
        if constexpr (MODE == 1) acc[i][j] = MFMA32(f.x[i], f.w[j], acc[i][j]);
        else acc[i][j] = MFMA32(f.w[j], f.x[i], acc[i][j]);
      }
  };
  auto side = [&](int slot) {
    if constexpr (SIDE) {
      lds_cptr b = (lds_cptr)lds + slot * P_SLOT;
      bf16x8 xf = lds_rd128(b + sxa);
      bf16x8 wf = cat8(lds_tr(b + swa), lds_tr(b + swa + 256));
      if constexpr (MODE == 2) sacc = MFMA32(wf, xf, sacc);
      else sacc = MFMA32(xf, wf, sacc);
    }
  };

  dma(0, 0); dma(1, 1); dma(2, 2);
  if constexpr (SIDE) asm volatile("s_waitcnt vmcnt(14)\n\ts_barrier" : "+v"(gx0), "+v"(gx1), "+v"(gw0), "+v"(gw1)::"memory");
  else asm volatile("s_waitcnt vmcnt(12)\n\ts_barrier" : "+v"(gx0), "+v"(gx1), "+v"(gw0), "+v"(gw1)::"memory");
  {
    float z = 0.f;
#pragma unroll
    for (int i = 0; i < 4; ++i) {
      z += __uint_as_float(gx0[i] << 16) * __uint_as_float(gw0[i] << 16) + __uint_as_float(gx0[i] & 0xffff0000u) * __uint_as_float(gw0[i] & 0xffff0000u);
      z += __uint_as_float(gx1[i] << 16) * __uint_as_float(gw1[i] << 16) + __uint_as_float(gx1[i] & 0xffff0000u) * __uint_as_float(gw1[i] & 0xffff0000u);
    }
#pragma unroll
    for (int o = 32; o >= 1; o >>= 1) z += __shfl_xor(z, o);
    if (lane == 0) ((float*)(ws + WS_G))[grow] = 1.f / (1.f + __expf(-z));
  }
  PFrag f0, f1;
  rd(f0, 0, 0);
  int slot = 0;
  for (int it = 0; it < 16; ++it) {
    const int nslot = slot == 2 ? 0 : slot + 1;
    rd(f1, slot, 1); side(slot); mma(f0);
    rd(f0, slot, 2); mma(f1);
    rd(f1, slot, 3); mma(f0);
    if (it < 14) {
      if constexpr (SIDE) asm volatile("s_waitcnt vmcnt(7) lgkmcnt(0)\n\ts_barrier" ::: "memory");
      else asm volatile("s_waitcnt vmcnt(6) lgkmcnt(0)\n\ts_barrier" ::: "memory");
    } else {
      asm volatile("s_waitcnt vmcnt(0) lgkmcnt(0)\n\ts_barrier" ::: "memory");
    }
    if (it < 13) dma(it + 3, slot);
    if (it < 15) rd(f0, nslot, 0);
    mma(f1);
    slot = nslot;
  }
#pragma unroll
  for (int i = 0; i < 2; ++i)
#pragma unroll
    for (int j = 0; j < 2; ++j) {
      const int mt = mb * 4 + wm * 2 + i;
      const int hd = (nb & 3) * 8 + wn * 2 + j;
      char* dst;
      float sc = 1.f;
      if constexpr (MODE >= 2) { dst = ws + WS_QA + (size_t)(mt * 32 + hd) * 2048; sc = QSCALE; }
      else if constexpr (MODE == 1) dst = ws + WS_KVB + (size_t)(hd * 64 + mt) * 4096 + 2048;
      else {
        if (nb < 4) { dst = ws + WS_QB + (size_t)(hd * 64 + mt) * 2048; sc = QSCALE; }
        else dst = ws + WS_KVB + (size_t)(hd * 64 + mt) * 4096;
      }
      *(bf16x8*)(dst + lane * 16) = pack8s(acc[i][j], 0, sc);
      *(bf16x8*)(dst + 1024 + lane * 16) = pack8s(acc[i][j], 8, sc);
    }
  if constexpr (SIDE) {
    __syncthreads();
    float* red = (float*)lds;
#pragma unroll
    for (int g = 0; g < 4; ++g)
      *(f32x4*)(red + (w * 4 + g) * 256 + lane * 4) = (f32x4){sacc[4 * g], sacc[4 * g + 1], sacc[4 * g + 2], sacc[4 * g + 3]};
    __syncthreads();
    if (w < 2) {
      f32x16 t;
#pragma unroll
      for (int g = 0; g < 4; ++g) {
        f32x4 a = *(const f32x4*)(red + ((w + 0) * 4 + g) * 256 + lane * 4);
        f32x4 b = *(const f32x4*)(red + ((w + 2) * 4 + g) * 256 + lane * 4);
        f32x4 c = *(const f32x4*)(red + ((w + 4) * 4 + g) * 256 + lane * 4);
        f32x4 d = *(const f32x4*)(red + ((w + 6) * 4 + g) * 256 + lane * 4);
#pragma unroll
        for (int e = 0; e < 4; ++e) t[4 * g + e] = (a[e] + b[e]) + (c[e] + d[e]);
      }
      const int mt = mb * 4 + 2 * (ni & 1) + w;
      char* dst = ws + WS_KVA + (size_t)mt * 4096 + (MODE == 3 ? 2048 : 0);
      *(bf16x8*)(dst + lane * 16) = pack8(t, 0);
      *(bf16x8*)(dst + 1024 + lane * 16) = pack8(t, 8);
    }
  }
}

__global__ __launch_bounds__(512, 2) void k_proj(const char* __restrict__ xa, const char* __restrict__ wq,
                                                 const char* __restrict__ wk, const char* __restrict__ wv,
                                                 const char* __restrict__ wqkv,
                                                 const char* __restrict__ gw, char* __restrict__ ws) {
  extern __shared__ __attribute__((aligned(16))) char lds[];
  const int b = blockIdx.x, xcd = b & 7, idx = b >> 3;
  const int mb = (xcd & 1) * 8 + (idx & 7), ni = idx >> 3, nb = (xcd >> 1) * 4 + ni;
  const int lane = threadIdx.x & 63, grow = b * 8 + (threadIdx.x >> 6);
  u32x4 gx0, gx1, gw0, gw1;
  {
    const char* px = xa + (size_t)grow * 2048 + lane * 32;
    const char* pw = gw + lane * 32;
    asm volatile("global_load_dwordx4 %0, %4, off\n\tglobal_load_dwordx4 %1, %4, off offset:16\n\t"
                 "global_load_dwordx4 %2, %5, off\n\tglobal_load_dwordx4 %3, %5, off offset:16"
                 : "=&v"(gx0), "=&v"(gx1), "=&v"(gw0), "=&v"(gw1) : "v"(px), "v"(pw) : "memory");
  }
  if (nb < 8) proj_body<0>(xa, wqkv + nb * 512, 6144, nullptr, ws, mb, nb, ni, lds, gx0, gx1, gw0, gw1, grow);
  else if (nb < 12) proj_body<1>(xa, wqkv + nb * 512, 6144, nullptr, ws, mb, nb, ni, lds, gx0, gx1, gw0, gw1, grow);
  else if (ni < 2) proj_body<2>(xa, wq + ni * 512, 2048, wk, ws, mb, nb, ni, lds, gx0, gx1, gw0, gw1, grow);
  else proj_body<3>(xa, wq + ni * 512, 2048, wv, ws, mb, nb, ni, lds, gx0, gx1, gw0, gw1, grow);
}

constexpr int A_SLOT = 16384, A_NSLOT = 4, A_BOFF = A_SLOT * A_NSLOT;
constexpr int A_LDS = A_BOFF + 65536;

DI int crow(int r, int h) { return (r & 3) + 8 * (r >> 2) + 4 * h; }
DI float swap_half(float x) {
  auto rr = __builtin_amdgcn_permlane32_swap(__float_as_uint(x), __float_as_uint(x), false, false);
  return __uint_as_float((threadIdx.x & 32) ? rr[0] : rr[1]);
}
DI void exp16(f32x16& s) {
#pragma unroll
  for (int i = 0; i < 16; ++i) s[i] = __builtin_amdgcn_exp2f(s[i]);
}

template <bool MASKED>
DI void attn_exact(const char* __restrict__ kv, int t0, int t1, int qt, const bf16x8& q0, const bf16x8& q1, const bf16x8& ones,
                   f32x16& o, float& l) {
  const int lane = threadIdx.x & 63, r = lane & 31, h = lane >> 5;
  float m = -3.0e38f;
  for (int t = t0; t < t1; ++t) {
    const char* p = kv + (size_t)t * 4096 + lane * 16;
    bf16x8 k0 = *(const bf16x8*)p, k1 = *(const bf16x8*)(p + 1024);
    f32x16 s = {};
    s = MFMA32(k0, q0, s); s = MFMA32(k1, q1, s);
#pragma unroll
    for (int i = 0; i < 16; ++i) {
      bool ok = true;
      if (MASKED) { const int c = crow(i, h); ok = (t == qt) ? (c <= r) : ((t == qt - 8) ? (c > r) : true); }
      m = fmaxf(m, ok ? s[i] : -3.0e38f);
    }
  }
  m = fmaxf(m, swap_half(m));
  o = (f32x16){};
  f32x4 la = {};
  for (int t = t0; t < t1; ++t) {
    const char* p = kv + (size_t)t * 4096 + lane * 16;
    bf16x8 k0 = *(const bf16x8*)p, k1 = *(const bf16x8*)(p + 1024);
    bf16x8 v0 = *(const bf16x8*)(p + 2048), v1 = *(const bf16x8*)(p + 3072);
    f32x16 s = {};
    s = MFMA32(k0, q0, s); s = MFMA32(k1, q1, s);
#pragma unroll
    for (int i = 0; i < 16; ++i) {
      bool ok = true;
      if (MASKED) { const int c = crow(i, h); ok = (t == qt) ? (c <= r) : ((t == qt - 8) ? (c > r) : true); }
      s[i] = ok ? __builtin_amdgcn_exp2f(s[i] - m) : 0.f;
    }
    bf16x8 pa = pack8(s, 0), pb = pack8(s, 8);
    o = MFMA32(v0, pa, o); o = MFMA32(v1, pb, o);
    la = MFMA16(ones, pa, la); la = MFMA16(ones, pb, la);
  }
  l = la[0];
}
DI bool ref_unsafe(const f32x16& o, float l) {
  float z = 0.f;
#pragma unroll
  for (int i = 0; i < 16; ++i) z = __builtin_fmaf(o[i], 0.f, z);
  const bool ok = (l >= 5.421010862e-20f) && (l <= 1.8446744e19f) && (z == 0.f);
  return __any(!ok);
}

__global__ __launch_bounds__(512, 2) void k_attn(char* __restrict__ ws) {
  extern __shared__ __attribute__((aligned(16))) char lds[];
  const int b = blockIdx.x, tid = threadIdx.x, lane = tid & 63;
  const int w = rfl(tid >> 6);
  const int r = lane & 31, h = lane >> 5;
  const unsigned lds0 = (unsigned)(uintptr_t)lds;
  const int qt = b >> 2, pr = w & 3, half = w >> 2;
  const int hA = 8 * (b & 3) + 2 * pr;
  const int hB = (b & 7) + 8 * (b >> 6), rb = (b >> 3) & 7;
  const char* kva = ws + WS_KVA;
  const char* kvb = ws + WS_KVB + (size_t)hB * 64 * 4096;
  bf16x8 ones;
  {
    const short v = ((((lane >> 4) & 1) == (((lane & 15) >> 2) & 1)) ? (short)0x3f80 : (short)0);
    ones = (bf16x8){v, v, v, v, v, v, v, v};
  }
  const unsigned lo16 = lane * 16;
  auto dmaA = [&](int c) {
    const char* src = kva + (size_t)(half * 32 + 2 * c) * 4096 + pr * 2048;
    const unsigned dst = lds0 + (c & 3) * A_SLOT + w * 2048;
    glds16<0>(lo16, src, dst);
    glds16<1024>(lo16, src, dst);
  };
  auto dmaB = [&](int i) {
    const int gt = rb * 8 - 8 + 2 * w + (i >> 2);
    if (gt >= 0) glds16<0>(lo16, kvb + (size_t)gt * 4096 + (i & 3) * 1024, lds0 + A_BOFF + (2 * w + (i >> 2)) * 4096 + (i & 3) * 1024);
  };
  bf16x8 q[2][2];
#pragma unroll
  for (int u = 0; u < 2; ++u)
#pragma unroll
    for (int s = 0; s < 2; ++s) q[u][s] = *(const bf16x8*)(ws + WS_QA + (size_t)(qt * 32 + hA + u) * 2048 + s * 1024 + lane * 16);
  const int qtB = rb * 8 + w;
  bf16x8 qb[2];
#pragma unroll
  for (int s = 0; s < 2; ++s) qb[s] = *(const bf16x8*)(ws + WS_QB + (size_t)(hB * 64 + qtB) * 2048 + s * 1024 + lane * 16);
  const float gA = ((const float*)(ws + WS_G))[qt * 32 + r];
  const float gB = 1.f - ((const float*)(ws + WS_G))[qtB * 32 + r];
  dmaA(0); dmaA(1); dmaA(2);

  f32x16 o0 = {}, o1 = {}, s0, s1;
  f32x4 l0 = {}, l1 = {};
  const f32x16 zero = {};
  bf16x8 p0a, p0b, p1a, p1b, kf0, kf1, vf0, vf1;
  lds_cptr kvbase = (lds_cptr)lds + half * 8192 + lane * 16;
  asm volatile("s_waitcnt vmcnt(4)\n\ts_barrier" ::: "memory");
  kf0 = lds_rd128(kvbase); kf1 = lds_rd128(kvbase + 1024);
  s0 = MFMA32(kf0, q[0][0], zero); s0 = MFMA32(kf1, q[0][1], s0);
  p1a = (bf16x8){}; p1b = (bf16x8){};
  vf0 = (bf16x8){}; vf1 = (bf16x8){};
  for (int c = 0; c < 16; ++c) {
#pragma unroll
    for (int tt = 0; tt < 2; ++tt) {
      lds_cptr cur = kvbase + (c & 3) * A_SLOT + tt * 4096;
      if (tt == 1) {
        if (c < 14) asm volatile("s_waitcnt vmcnt(2)\n\ts_barrier" ::: "memory");
        else asm volatile("s_waitcnt vmcnt(0)\n\ts_barrier" ::: "memory");
        if (c < 8) dmaB(c);
        if (c + 3 < 16) dmaA(c + 3);
      }
      lds_cptr nxt = tt == 0 ? cur + 4096 : kvbase + ((c + 1) & 3) * A_SLOT;
      s1 = MFMA32(kf0, q[1][0], zero); s1 = MFMA32(kf1, q[1][1], s1);
      o1 = MFMA32(vf0, p1a, o1); o1 = MFMA32(vf1, p1b, o1);
      l1 = MFMA16(ones, p1a, l1); l1 = MFMA16(ones, p1b, l1);
      vf0 = lds_rd128(cur + 2048); vf1 = lds_rd128(cur + 3072);
      if (tt == 0 || c < 15) { kf0 = lds_rd128(nxt); kf1 = lds_rd128(nxt + 1024); }
      exp16(s0);
      p0a = pack8(s0, 0); p0b = pack8(s0, 8);
      s0 = MFMA32(kf0, q[0][0], zero); s0 = MFMA32(kf1, q[0][1], s0);
      o0 = MFMA32(vf0, p0a, o0); o0 = MFMA32(vf1, p0b, o0);
      l0 = MFMA16(ones, p0a, l0); l0 = MFMA16(ones, p0b, l0);
      exp16(s1);
      p1a = pack8(s1, 0); p1b = pack8(s1, 8);
    }
  }
  o1 = MFMA32(vf0, p1a, o1); o1 = MFMA32(vf1, p1b, o1);
  l1 = MFMA16(ones, p1a, l1); l1 = MFMA16(ones, p1b, l1);
  asm volatile("s_waitcnt vmcnt(0) lgkmcnt(0)\n\ts_barrier" ::: "memory");
  {
    float* xb = (float*)lds + w * (5 * 256);
    const f32x16& os = half ? o0 : o1;
    const float ls = half ? l0[0] : l1[0];
#pragma unroll
    for (int g = 0; g < 4; ++g) *(f32x4*)(xb + g * 256 + lane * 4) = (f32x4){os[4 * g], os[4 * g + 1], os[4 * g + 2], os[4 * g + 3]};
    xb[4 * 256 + lane] = ls;
  }
  __syncthreads();
  {
    const float* xb = (const float*)lds + (w ^ 4) * (5 * 256);
    f32x16 o = half ? o1 : o0;
    float l = half ? l1[0] : l0[0];
#pragma unroll
    for (int g = 0; g < 4; ++g) {
      f32x4 v = *(const f32x4*)(xb + g * 256 + lane * 4);
#pragma unroll
      for (int e = 0; e < 4; ++e) o[4 * g + e] += v[e];
    }
    l += xb[4 * 256 + lane];
    if (__builtin_expect(ref_unsafe(o, l), 0)) {
      if (half) attn_exact<false>(kva, 0, 64, 0, q[1][0], q[1][1], ones, o, l);
      else attn_exact<false>(kva, 0, 64, 0, q[0][0], q[0][1], ones, o, l);
    }
    const float sc = gA / l;
    char* dst = ws + WS_O + (size_t)(qt * 64 + hA + half) * 2048 + lane * 16;
    *(bf16x8*)dst = pack8s(o, 0, sc);
    *(bf16x8*)(dst + 1024) = pack8s(o, 8, sc);
  }
  {
    f32x16 mF, mD;
#pragma unroll
    for (int i = 0; i < 16; ++i) { const int c = crow(i, h); mF[i] = c > r ? 0.f : -1.0e30f; mD[i] = c <= r ? 0.f : -1.0e30f; }
    lds_cptr bb = (lds_cptr)lds + A_BOFF + w * 4096 + lane * 16;
    f32x16 o = {};
    f32x4 la = {};
    const int j0 = rb == 0 ? 8 - w : 0;
    auto tile = [&](int j, const f32x16& cin) {
      lds_cptr p = bb + j * 4096;
      bf16x8 k0 = lds_rd128(p), k1 = lds_rd128(p + 1024), v0 = lds_rd128(p + 2048), v1 = lds_rd128(p + 3072);
      f32x16 s = MFMA32(k0, qb[0], cin);
      s = MFMA32(k1, qb[1], s);
      exp16(s);
      bf16x8 pa = pack8(s, 0), pb = pack8(s, 8);
      o = MFMA32(v0, pa, o); o = MFMA32(v1, pb, o);
      la = MFMA16(ones, pa, la); la = MFMA16(ones, pb, la);
    };
    if (j0 == 0) tile(0, mF);
    for (int j = j0 < 1 ? 1 : j0; j < 8; ++j) tile(j, zero);
    tile(8, mD);
    float l = la[0];
    if (__builtin_expect(ref_unsafe(o, l), 0)) attn_exact<true>(kvb, qtB >= 8 ? qtB - 8 : 0, qtB + 1, qtB, qb[0], qb[1], ones, o, l);
    const float sc = gB / l;
    char* dst = ws + WS_O + (size_t)(qtB * 64 + 32 + hB) * 2048 + lane * 16;
    *(bf16x8*)dst = pack8s(o, 0, sc);
    *(bf16x8*)(dst + 1024) = pack8s(o, 8, sc);
  }
}

constexpr int O_SLOT = 49152;
constexpr int O_WOFF = 32768;
constexpr int O_LDS = 3 * O_SLOT;

__global__ __launch_bounds__(512, 2) void k_out(const char* __restrict__ woa, const char* __restrict__ wob,
                                                const char* __restrict__ ws, char* __restrict__ out) {
  extern __shared__ __attribute__((aligned(16))) char lds[];
  const int b = blockIdx.x, xcd = b & 7, idx = b >> 3;
  const int mb = (xcd & 3) * 4 + (idx & 3), nb = (xcd >> 2) * 8 + (idx >> 2);
  const int tid = threadIdx.x, lane = tid & 63;
  const int w = rfl(tid >> 6);
  const int wm = w & 1, kq = w >> 1;
  const int r = lane & 31, h = lane >> 5;
  const unsigned lds0 = (unsigned)(uintptr_t)lds;
  const unsigned lo16 = lane * 16;
  const char* osrc = ws + WS_O + ((size_t)(mb * 4 + wm * 2) * 64 + kq * 16) * 2048;
  const char* wsrc = (kq < 2 ? woa : wob) + (size_t)((kq & 1) * 512 + wm * 16) * 2048 + nb * 128;
  unsigned wo[2];
#pragma unroll
  for (int e = 0; e < 2; ++e) {
    const int row = 8 * e + (lane >> 3);
    const int c = (lane & 7) ^ (((row >> 1) & 1) << 2);
    wo[e] = row * 2048 + c * 16;
  }
  auto dma = [&](int st, int slot) {
    const unsigned lb = lds0 + slot * O_SLOT;
    const char* o0 = osrc + st * 2048;
    glds16<0>(lo16, o0, lb + w * 4096);
    glds16<1024>(lo16, o0, lb + w * 4096);
    glds16<0>(lo16, o0 + 64 * 2048, lb + w * 4096 + 2048);
    glds16<1024>(lo16, o0 + 64 * 2048, lb + w * 4096 + 2048);
    const char* w0 = wsrc + (size_t)st * 32 * 2048;
    glds16<0>(wo[0], w0, lb + O_WOFF + kq * 4096 + wm * 2048);
    glds16<0>(wo[1], w0, lb + O_WOFF + kq * 4096 + wm * 2048 + 1024);
  };
  const int g16 = lane >> 4, q4 = (lane & 15) >> 2, p4 = lane & 3;
  const int wc = 2 * (g16 & 1) + (p4 >> 1);
  const int wa0 = O_WOFF + kq * 4096 + (4 * h + q4) * 128 + ((wc ^ (((q4 >> 1) & 1) << 2)) << 4) + 8 * (p4 & 1);
  const int oa0 = w * 4096 + lane * 16;
  struct OFrag { bf16x8 a[2]; bf16x8 w[2]; };
  auto rd = [&](OFrag& f, int slot, int s) {
    lds_cptr bp = (lds_cptr)lds + slot * O_SLOT;
    f.a[0] = lds_rd128(bp + oa0 + s * 1024);
    f.a[1] = lds_rd128(bp + oa0 + 2048 + s * 1024);
#pragma unroll
    for (int j = 0; j < 2; ++j) {
      lds_cptr p = bp + (wa0 ^ (j << 6)) + s * 2048;
      f.w[j] = cat8(lds_tr(p), lds_tr(p + 1024));
    }
  };
  f32x16 acc[2][2] = {};
  auto mma = [&](const OFrag& f) {
#pragma unroll
    for (int i = 0; i < 2; ++i)
#pragma unroll
      for (int j = 0; j < 2; ++j) acc[i][j] = MFMA32(f.w[j], f.a[i], acc[i][j]);
  };
  dma(0, 0); dma(1, 1); dma(2, 2);
  asm volatile("s_waitcnt vmcnt(12)\n\ts_barrier" ::: "memory");
  OFrag f0, f1;
  rd(f0, 0, 0);
  int slot = 0;
  for (int it = 0; it < 16; ++it) {
    const int nslot = slot == 2 ? 0 : slot + 1;
    rd(f1, slot, 1); mma(f0);
    if (it < 14) asm volatile("s_waitcnt vmcnt(6) lgkmcnt(0)\n\ts_barrier" ::: "memory");
    else asm volatile("s_waitcnt vmcnt(0) lgkmcnt(0)\n\ts_barrier" ::: "memory");
    if (it < 13) dma(it + 3, slot);
    if (it < 15) rd(f0, nslot, 0);
    mma(f1);
    slot = nslot;
  }
  __syncthreads();
  float* red = (float*)lds;
#pragma unroll
  for (int t = 0; t < 4; ++t) {
    if (t != kq) {
      const f32x16& a = acc[t >> 1][t & 1];
      float* p = red + ((wm * 4 + t) * 4 + kq) * 1024 + lane * 4;
#pragma unroll
      for (int g = 0; g < 4; ++g) *(f32x4*)(p + g * 256) = (f32x4){a[4 * g], a[4 * g + 1], a[4 * g + 2], a[4 * g + 3]};
    }
  }
  __syncthreads();
  f32x16 t = kq == 0 ? acc[0][0] : (kq == 1 ? acc[0][1] : (kq == 2 ? acc[1][0] : acc[1][1]));
#pragma unroll
  for (int s = 0; s < 4; ++s) {
    if (s != kq) {
      const float* p = red + ((wm * 4 + kq) * 4 + s) * 1024 + lane * 4;
#pragma unroll
      for (int g = 0; g < 4; ++g) {
        f32x4 v = *(const f32x4*)(p + g * 256);
#pragma unroll
        for (int e = 0; e < 4; ++e) t[4 * g + e] += v[e];
      }
    }
  }
  const int row = mb * 128 + wm * 64 + (kq >> 1) * 32 + r;
  char* dst = out + (size_t)row * 2048 + (nb * 64 + (kq & 1) * 32) * 2;
#pragma unroll
  for (int g = 0; g < 4; g += 2) {
    u32x2 a = {cvtpk(t[4 * g], t[4 * g + 1]), cvtpk(t[4 * g + 2], t[4 * g + 3])};
    u32x2 c = {cvtpk(t[4 * g + 4], t[4 * g + 5]), cvtpk(t[4 * g + 6], t[4 * g + 7])};
    auto x = __builtin_amdgcn_permlane32_swap(a[0], c[0], false, false);
    auto y = __builtin_amdgcn_permlane32_swap(a[1], c[1], false, false);
    *(u32x4*)(dst + g * 16 + h * 16) = (u32x4){x[0], y[0], x[1], y[1]};
  }
}

extern "C" void kernel_launch(void* const* d_in, const int* in_sizes, int n_in, void* d_out, int out_size, void* d_ws,
                              size_t ws_size, hipStream_t stream) {
  const char* xa = (const char*)d_in[0];
  const char* wq = (const char*)d_in[1];
  const char* wk = (const char*)d_in[2];
  const char* wv = (const char*)d_in[3];
  const char* woa = (const char*)d_in[4];
  const char* wqkv = (const char*)d_in[6];
  const char* wob = (const char*)d_in[7];
  const char* gw = (const char*)d_in[8];
  char* ws = (char*)d_ws;
  (void)hipFuncSetAttribute((const void*)k_proj, hipFuncAttributeMaxDynamicSharedMemorySize, P_LDS);
  (void)hipFuncSetAttribute((const void*)k_attn, hipFuncAttributeMaxDynamicSharedMemorySize, A_LDS);
  (void)hipFuncSetAttribute((const void*)k_out, hipFuncAttributeMaxDynamicSharedMemorySize, O_LDS);
  k_proj<<<256, 512, P_LDS, stream>>>(xa, wq, wk, wv, wqkv, gw, ws);
  k_attn<<<256, 512, A_LDS, stream>>>(ws);
  k_out<<<256, 512, O_LDS, stream>>>(woa, wob, ws, (char*)d_out);
}
```

```cpp
#include <hip/hip_runtime.h>
#include <hip/hip_bf16.h>
#include <stdint.h>

typedef float f32x16 __attribute__((ext_vector_type(16)));
typedef float f32x4 __attribute__((ext_vector_type(4)));
typedef float f32x2 __attribute__((ext_vector_type(2)));
typedef short bf16x8 __attribute__((ext_vector_type(8)));
typedef short s16x4 __attribute__((ext_vector_type(4)));
typedef __bf16 bf16x2 __attribute__((ext_vector_type(2)));
typedef unsigned u32x4 __attribute__((ext_vector_type(4)));
typedef unsigned u32x2 __attribute__((ext_vector_type(2)));
typedef __attribute__((address_space(3))) const char* lds_cptr;
typedef __attribute__((address_space(3))) char* lds_ptr;

#define DI __device__ __forceinline__
#define SBAR() __builtin_amdgcn_sched_barrier(0)
#define PIN(x) asm volatile("" : "+v"(x))
#define MFMA32(a, b, c) __builtin_amdgcn_mfma_f32_32x32x16_bf16(a, b, c, 0, 0, 0)
#define MFMA16(a, b, c) __builtin_amdgcn_mfma_f32_16x16x32_bf16(a, b, c, 0, 0, 0)

constexpr int SEQ = 2048, DM = 1024;
constexpr float QSCALE = 0.17677669529663687f * 1.4426950408889634f;

constexpr size_t WS_QA = 0;
constexpr size_t WS_KVA = 4u << 20;
constexpr size_t WS_G = WS_KVA + (256u << 10);
constexpr size_t WS_CNT = WS_G + 8192;
constexpr size_t WS_QB = 5u << 20;
constexpr size_t WS_KVB = 9u << 20;
constexpr size_t WS_O = 17u << 20;

#define TRACE(i) do {} while (0)
#define TRACE_DECL
#define TRACE_OUT(wsp) do {} while (0)
#define STAMP_DECL
#define STAMP(i) do {} while (0)
#define STAMP_OUT(k, wsp) do {} while (0)
DI unsigned rfl(unsigned x) { return __builtin_amdgcn_readfirstlane(x); }
DI unsigned cvtpk(float lo, float hi) {
  f32x2 v = {lo, hi};
  bf16x2 b = __builtin_convertvector(v, bf16x2);
  return __builtin_bit_cast(unsigned, b);
}
DI bf16x8 pack8(const f32x16& a, int o) {
  u32x4 r = {cvtpk(a[o], a[o + 1]), cvtpk(a[o + 2], a[o + 3]), cvtpk(a[o + 4], a[o + 5]), cvtpk(a[o + 6], a[o + 7])};
  return __builtin_bit_cast(bf16x8, r);
}
DI bf16x8 pack8s(const f32x16& a, int o, float s) {
  u32x4 r = {cvtpk(a[o] * s, a[o + 1] * s), cvtpk(a[o + 2] * s, a[o + 3] * s), cvtpk(a[o + 4] * s, a[o + 5] * s),
             cvtpk(a[o + 6] * s, a[o + 7] * s)};
  return __builtin_bit_cast(bf16x8, r);
}
template <int OFF>
DI void glds16(unsigned voff, const void* sbase, unsigned lds_base) {
  asm volatile("s_mov_b32 m0, %2\n\ts_nop 0\n\tglobal_load_lds_dwordx4 %0, %1 offset:%3"
               :: "v"(voff), "s"(sbase), "s"(lds_base), "i"(OFF) : "memory");
}
DI bf16x8 lds_rd128(lds_cptr p) { return *(const __attribute__((address_space(3))) bf16x8*)p; }
DI s16x4 lds_tr(lds_cptr p) {
  typedef short v4i16_t __attribute__((ext_vector_type(4)));
  return __builtin_bit_cast(s16x4, __builtin_amdgcn_ds_read_tr16_b64_v4i16((__attribute__((address_space(3))) v4i16_t*)p));
}
DI bf16x8 cat8(s16x4 lo, s16x4 hi) { return (bf16x8){lo[0], lo[1], lo[2], lo[3], hi[0], hi[1], hi[2], hi[3]}; }
DI float bf2f(unsigned short b) { return __uint_as_float((unsigned)b << 16); }

constexpr int P_SLOT = 53248;
constexpr int P_XOFF = 0, P_WOFF = 16384, P_KVOFF = 49152;
constexpr int P_LDS = 3 * P_SLOT;

struct PFrag { bf16x8 x[2]; bf16x8 w[2]; };

template <int MODE>
DI void proj_body(const char* __restrict__ xg, const char* __restrict__ wg, int ldw_bytes, const char* __restrict__ wkv,
                  char* __restrict__ ws, int mb, int nb, int ni, char* lds, const char* __restrict__ xa,
                  const char* __restrict__ gw) {
  STAMP_DECL STAMP(0);
  constexpr bool SIDE = MODE >= 2;
  constexpr int NP = SIDE ? 7 : 6;
  const int tid = threadIdx.x, lane = tid & 63;
  const int w = rfl(tid >> 6);
  const int wm = w & 1, wn = w >> 1;
  const int r = lane & 31, h = lane >> 5;
  const unsigned lds0 = (unsigned)(uintptr_t)lds;
  const int grow = blockIdx.x * 8 + w;
  u32x4 gx0, gx1, gw0, gw1;
  asm volatile("global_load_dwordx4 %0, %4, %5\n\tglobal_load_dwordx4 %1, %4, %5 offset:16\n\t"
               "global_load_dwordx4 %2, %4, %6\n\tglobal_load_dwordx4 %3, %4, %6 offset:16"
               : "=&v"(gx0), "=&v"(gx1), "=&v"(gw0), "=&v"(gw1) : "v"(lane * 32), "s"(xa + (size_t)grow * 2048), "s"(gw) : "memory");
  unsigned xo[2], wo[4];
#pragma unroll
  for (int e = 0; e < 2; ++e) {
    const int row = 16 * w + 8 * e + (lane >> 3);
    const int c = (lane & 7) ^ (4 * e + (lane >> 4));
    xo[e] = row * 2048 + c * 16;
  }
#pragma unroll
  for (int e = 0; e < 4; ++e) {
    const int krow = 8 * w + 2 * e + (lane >> 5);
    const int c = (lane & 31) ^ ((krow & 3) << 2);
    wo[e] = krow * ldw_bytes + c * 16;
  }
  const unsigned kvo = w * 512 + (lane & 31) * 16;
  const char* xsrc = xg + (size_t)mb * 128 * 2048;
  auto piece = [&](int pc, int st, int slot) {
    const unsigned lb = lds0 + slot * P_SLOT;
    if (pc < 2) glds16<0>(xo[pc], xsrc + st * 128, lb + P_XOFF + w * 2048 + pc * 1024);
    else if (pc < 6) glds16<0>(wo[pc - 2], wg + (size_t)st * 64 * ldw_bytes, lb + P_WOFF + w * 4096 + (pc - 2) * 1024);
    else if constexpr (SIDE) {
      if (lane < 32) glds16<0>(kvo, wkv + st * 4096, lb + P_KVOFF + w * 512);
    }
  };
  auto dma = [&](int st, int slot) {
#pragma unroll
    for (int pc = 0; pc < NP; ++pc) piece(pc, st, slot);
  };
  const int t3 = (r >> 1) & 7;
  const int xa0 = P_XOFF + (wm * 64 + r) * 128 + ((h ^ t3) << 4);
  const int g16 = lane >> 4, q4 = (lane & 15) >> 2, p4 = lane & 3;
  const int wc = wn * 8 + 2 * (g16 & 1) + (p4 >> 1);
  const int wa0 = P_WOFF + (8 * h + q4) * 512 + ((wc ^ (q4 << 2)) << 4) + 8 * (p4 & 1);
  const int ss = w >> 1;
  const int sxa = P_XOFF + (32 * (2 * (ni & 1) + (w & 1)) + r) * 128 + (((2 * ss + h) ^ t3) << 4);
  const int swa = P_KVOFF + (16 * ss + 8 * h + q4) * 64 + 32 * (g16 & 1) + 8 * p4;

  auto rdx = [&](PFrag& f, int i, int slot, int s) { f.x[i] = lds_rd128((lds_cptr)lds + slot * P_SLOT + (xa0 ^ (s << 5)) + i * 4096); };
  auto rdw = [&](PFrag& f, int j, int slot, int s) {
    lds_cptr p = (lds_cptr)lds + slot * P_SLOT + (wa0 ^ (j << 6)) + s * 8192;
    f.w[j] = cat8(lds_tr(p), lds_tr(p + 2048));
  };
  f32x16 acc[2][2] = {};
  f32x16 sacc = {};
  auto mm = [&](const PFrag& f, int i, int j) {
    if constexpr (MODE == 1) acc[i][j] = MFMA32(f.x[i], f.w[j], acc[i][j]);
    else acc[i][j] = MFMA32(f.w[j], f.x[i], acc[i][j]);
  };
  auto step = [&](const PFrag& c, PFrag& n, int rslot, int rs, bool rd, int pa, int pb, int dst, int dslot, bool dm) {
    mm(c, 0, 0); SBAR();
    if (rd) { rdw(n, 0, rslot, rs); rdx(n, 0, rslot, rs); }
    SBAR();
    mm(c, 0, 1); SBAR();
    if (rd) { rdw(n, 1, rslot, rs); rdx(n, 1, rslot, rs); }
    SBAR();
    mm(c, 1, 0); SBAR();
    if (dm && pa >= 0) piece(pa, dst, dslot);
    SBAR();
    mm(c, 1, 1); SBAR();
    if (dm && pb >= 0) piece(pb, dst, dslot);
    SBAR();
  };
  bf16x8 sxf, swf;
  auto side_rd = [&](int slot) {
    if constexpr (SIDE) {
      lds_cptr b = (lds_cptr)lds + slot * P_SLOT;
      sxf = lds_rd128(b + sxa);
      swf = cat8(lds_tr(b + swa), lds_tr(b + swa + 256));
    }
  };
  auto side_mm = [&]() {
    if constexpr (MODE == 2) sacc = MFMA32(swf, sxf, sacc);
    if constexpr (MODE == 3) sacc = MFMA32(sxf, swf, sacc);
  };

  STAMP(5);
  dma(0, 0);
  asm volatile("s_barrier" ::: "memory");
  STAMP(6);
  dma(1, 1); dma(2, 2);
  STAMP(7);
  if constexpr (SIDE) asm volatile("s_waitcnt vmcnt(14)\n\ts_barrier" : "+v"(gx0), "+v"(gx1), "+v"(gw0), "+v"(gw1)::"memory");
  else asm volatile("s_waitcnt vmcnt(12)\n\ts_barrier" : "+v"(gx0), "+v"(gx1), "+v"(gw0), "+v"(gw1)::"memory");
  {
    float z = 0.f;
#pragma unroll
    for (int i = 0; i < 4; ++i) {
      z += __uint_as_float(gx0[i] << 16) * __uint_as_float(gw0[i] << 16) + __uint_as_float(gx0[i] & 0xffff0000u) * __uint_as_float(gw0[i] & 0xffff0000u);
      z += __uint_as_float(gx1[i] << 16) * __uint_as_float(gw1[i] << 16) + __uint_as_float(gx1[i] & 0xffff0000u) * __uint_as_float(gw1[i] & 0xffff0000u);
    }
#pragma unroll
    for (int o = 32; o >= 1; o >>= 1) z += __shfl_xor(z, o);
    if (lane == 0) ((float*)(ws + WS_G))[grow] = 1.f / (1.f + __expf(-z));
  }
  STAMP(1);
  PFrag f0, f1;
  rdw(f0, 0, 0, 0); rdx(f0, 0, 0, 0); rdw(f0, 1, 0, 0); rdx(f0, 1, 0, 0);
  int slot = 0, pslot = 0;
  for (int it = 0; it < 16; ++it) {
    const int nslot = slot == 2 ? 0 : slot + 1;
    const bool dmp = it >= 1 && it <= 13;
    side_rd(slot); SBAR();
    step(f0, f1, slot, 1, true, 2, 3, it + 2, pslot, dmp);
    side_mm(); SBAR();
    step(f1, f0, slot, 2, true, 4, 5, it + 2, pslot, dmp);
    step(f0, f1, slot, 3, true, SIDE ? 6 : -1, -1, it + 2, pslot, dmp);
    if (it < 14) {
      if constexpr (SIDE) asm volatile("s_waitcnt vmcnt(7) lgkmcnt(0)\n\ts_barrier" ::: "memory");
      else asm volatile("s_waitcnt vmcnt(6) lgkmcnt(0)\n\ts_barrier" ::: "memory");
    } else {
      asm volatile("s_waitcnt vmcnt(0) lgkmcnt(0)\n\ts_barrier" ::: "memory");
    }
    SBAR();
    step(f1, f0, nslot, 0, it < 15, 0, 1, it + 3, slot, it < 13);
    pslot = slot;
    slot = nslot;
  }
  STAMP(2);
#pragma unroll
  for (int i = 0; i < 2; ++i)
#pragma unroll
    for (int j = 0; j < 2; ++j) {
      const int mt = mb * 4 + wm * 2 + i;
      const int hd = (nb & 3) * 8 + wn * 2 + j;
      char* dst;
      float sc = 1.f;
      if constexpr (MODE >= 2) { dst = ws + WS_QA + (size_t)(mt * 32 + hd) * 2048; sc = QSCALE; }
      else if constexpr (MODE == 1) dst = ws + WS_KVB + (size_t)(hd * 64 + mt) * 4096 + 2048;
      else {
        if (nb < 4) { dst = ws + WS_QB + (size_t)(hd * 64 + mt) * 2048; sc = QSCALE; }
        else dst = ws + WS_KVB + (size_t)(hd * 64 + mt) * 4096;
      }
      *(bf16x8*)(dst + lane * 16) = pack8s(acc[i][j], 0, sc);
      *(bf16x8*)(dst + 1024 + lane * 16) = pack8s(acc[i][j], 8, sc);
    }
  if constexpr (SIDE) {
    __syncthreads();
    float* red = (float*)lds;
#pragma unroll
    for (int g = 0; g < 4; ++g)
      *(f32x4*)(red + (w * 4 + g) * 256 + lane * 4) = (f32x4){sacc[4 * g], sacc[4 * g + 1], sacc[4 * g + 2], sacc[4 * g + 3]};
    __syncthreads();
    if (w < 2) {
      f32x16 t;
#pragma unroll
      for (int g = 0; g < 4; ++g) {
        f32x4 a = *(const f32x4*)(red + ((w + 0) * 4 + g) * 256 + lane * 4);
        f32x4 b = *(const f32x4*)(red + ((w + 2) * 4 + g) * 256 + lane * 4);
        f32x4 c = *(const f32x4*)(red + ((w + 4) * 4 + g) * 256 + lane * 4);
        f32x4 d = *(const f32x4*)(red + ((w + 6) * 4 + g) * 256 + lane * 4);
#pragma unroll
        for (int e = 0; e < 4; ++e) t[4 * g + e] = (a[e] + b[e]) + (c[e] + d[e]);
      }
      const int mt = mb * 4 + 2 * (ni & 1) + w;
      char* dst = ws + WS_KVA + (size_t)mt * 4096 + (MODE == 3 ? 2048 : 0);
      *(bf16x8*)(dst + lane * 16) = pack8(t, 0);
      *(bf16x8*)(dst + 1024 + lane * 16) = pack8(t, 8);
    }
  }
  STAMP(3);
  STAMP(4);
  STAMP_OUT(0, ws);
}

__global__ __launch_bounds__(512, 2) void k_proj(const char* __restrict__ xa, const char* __restrict__ wq,
                                                 const char* __restrict__ wk, const char* __restrict__ wv,
                                                 const char* __restrict__ wqkv,
                                                 const char* __restrict__ gw, char* __restrict__ ws) {
  extern __shared__ __attribute__((aligned(16))) char lds[];
  const int b = blockIdx.x, xcd = b & 7, idx = b >> 3;
  const int mb = (xcd & 1) * 8 + (idx & 7), ni = idx >> 3, nb = (xcd >> 1) * 4 + ni;
  if (b == 0 && threadIdx.x < 32) *(unsigned*)(ws + WS_CNT + threadIdx.x * 256) = 0u;
  if (nb < 8) proj_body<0>(xa, wqkv + nb * 512, 6144, nullptr, ws, mb, nb, ni, lds, xa, gw);
  else if (nb < 12) proj_body<1>(xa, wqkv + nb * 512, 6144, nullptr, ws, mb, nb, ni, lds, xa, gw);
  else if (ni < 2) proj_body<2>(xa, wq + ni * 512, 2048, wk, ws, mb, nb, ni, lds, xa, gw);
  else proj_body<3>(xa, wq + ni * 512, 2048, wv, ws, mb, nb, ni, lds, xa, gw);
}

constexpr int SL = 32768;
constexpr int R_LDS = 5 * SL;
constexpr int XCH_OFF = 0;
constexpr int WO_OFF = 2 * SL;
typedef __attribute__((address_space(1))) unsigned gu32;

DI int crow(int r, int h) { return (r & 3) + 8 * (r >> 2) + 4 * h; }
DI float swap_half(float x) {
  auto rr = __builtin_amdgcn_permlane32_swap(__float_as_uint(x), __float_as_uint(x), false, false);
  return __uint_as_float((threadIdx.x & 32) ? rr[0] : rr[1]);
}
DI void exp16(f32x16& s) {
#pragma unroll
  for (int i = 0; i < 16; ++i) s[i] = __builtin_amdgcn_exp2f(s[i]);
}
template <int OFF>
DI void gload16(bf16x8& d, unsigned voff, const void* sbase) {
  asm volatile("global_load_dwordx4 %0, %1, %2 offset:%3" : "=v"(d) : "v"(voff), "s"(sbase), "i"(OFF) : "memory");
}
template <int OFF>
DI void gload16_sc1(bf16x8& d, unsigned voff, const void* sbase) {
  asm volatile("global_load_dwordx4 %0, %1, %2 offset:%3 sc1" : "=v"(d) : "v"(voff), "s"(sbase), "i"(OFF) : "memory");
}
template <int OFF>
DI void gstore16_sc1(unsigned voff, void* sbase, const bf16x8& d) {
  asm volatile("global_store_dwordx4 %0, %1, %2 offset:%3 sc1\n\ts_nop 1" :: "v"(voff), "v"(d), "s"(sbase), "i"(OFF) : "memory");
}
DI void gload4(float& d, unsigned voff, const void* sbase) {
  asm volatile("global_load_dword %0, %1, %2" : "=v"(d) : "v"(voff), "s"(sbase) : "memory");
}
DI void poll_issue(unsigned& d, const void* sbase) {
  asm volatile("global_load_dword %0, %1, %2 sc1" : "+v"(d) : "v"(0u), "s"(sbase) : "memory");
}
DI void poll_finish(unsigned seen, char* cnt, char* tmo, unsigned want) {
  if (__builtin_expect(rfl(seen) < want, 0)) {
    for (unsigned spins = 0; spins < (1u << 22); ++spins) {
      if (rfl(__hip_atomic_load((gu32*)cnt, __ATOMIC_RELAXED, __HIP_MEMORY_SCOPE_AGENT)) >= want) return;
      __builtin_amdgcn_s_sleep(2);
    }
    __hip_atomic_store((gu32*)tmo, 1u, __ATOMIC_RELAXED, __HIP_MEMORY_SCOPE_AGENT);
  }
}
DI void signal(char* cnt) { (void)__hip_atomic_fetch_add((gu32*)cnt, 1u, __ATOMIC_RELAXED, __HIP_MEMORY_SCOPE_AGENT); }
DI void setprio_dyn(int p) {
  if (p == 0) __builtin_amdgcn_s_setprio(0);
  else if (p == 1) __builtin_amdgcn_s_setprio(1);
  else if (p == 2) __builtin_amdgcn_s_setprio(2);
  else __builtin_amdgcn_s_setprio(3);
}

#define AE(S, i) S[i] = __builtin_amdgcn_exp2f(S[i])
#define AC(P, S, i) P[(i) & 3] = cvtpk(S[2 * (i)], S[2 * (i) + 1])
#define ABC(x) __builtin_bit_cast(bf16x8, x)
#define APHASE(QK, PV, SX, PXa, PXb, SY, CINY, PYa, PYb, QY0, QY1, OY, LY, KF0, KF1, VF0, VF1, R1, R2) do { \
  if (QK) { SY = MFMA32(KF0, QY0, CINY); PIN(SY); } SBAR(); \
  AE(SX, 0); AE(SX, 1); AE(SX, 2); AE(SX, 3); PIN(SX); SBAR(); \
  if (QK) { SY = MFMA32(KF1, QY1, SY); PIN(SY); } SBAR(); \
  R1; \
  AC(PXa, SX, 0); AC(PXa, SX, 1); AE(SX, 4); AE(SX, 5); AE(SX, 6); PIN(SX); PIN(PXa); SBAR(); \
  if (PV) { OY = MFMA32(VF0, ABC(PYa), OY); PIN(OY); } SBAR(); \
  AE(SX, 7); AC(PXa, SX, 2); AE(SX, 8); AE(SX, 9); AC(PXa, SX, 3); PIN(SX); PIN(PXa); SBAR(); \
  if (PV) { LY = MFMA16(ones, ABC(PYa), LY); PIN(LY); } SBAR(); \
  R2; \
  AE(SX, 10); AE(SX, 11); PIN(SX); SBAR(); \
  if (PV) { OY = MFMA32(VF1, ABC(PYb), OY); PIN(OY); } SBAR(); \
  AC(PXb, SX, 4); AC(PXb, SX, 5); AE(SX, 12); AE(SX, 13); AE(SX, 14); PIN(SX); PIN(PXb); SBAR(); \
  if (PV) { LY = MFMA16(ones, ABC(PYb), LY); PIN(LY); } SBAR(); \
  AE(SX, 15); AC(PXb, SX, 6); AC(PXb, SX, 7); PIN(PXb); SBAR(); \
} while (0)

template <bool MASKED>
DI void attn_exact(const char* __restrict__ kv, int t0, int t1, int qt, const bf16x8& q0, const bf16x8& q1, const bf16x8& ones,
                   f32x16& o, float& l) {
  const int lane = threadIdx.x & 63, r = lane & 31, h = lane >> 5;
  float m = -3.0e38f;
  for (int t = t0; t < t1; ++t) {
    const char* p = kv + (size_t)t * 4096 + lane * 16;
    bf16x8 k0 = *(const bf16x8*)p, k1 = *(const bf16x8*)(p + 1024);
    f32x16 s = {};
    s = MFMA32(k0, q0, s); s = MFMA32(k1, q1, s);
#pragma unroll
    for (int i = 0; i < 16; ++i) {
      bool ok = true;
      if (MASKED) { const int c = crow(i, h); ok = (t == qt) ? (c <= r) : ((t == qt - 8) ? (c > r) : true); }
      m = fmaxf(m, ok ? s[i] : -3.0e38f);
    }
  }
  m = fmaxf(m, swap_half(m));
  o = (f32x16){};
  f32x4 la = {};
  for (int t = t0; t < t1; ++t) {
    const char* p = kv + (size_t)t * 4096 + lane * 16;
    bf16x8 k0 = *(const bf16x8*)p, k1 = *(const bf16x8*)(p + 1024);
    bf16x8 v0 = *(const bf16x8*)(p + 2048), v1 = *(const bf16x8*)(p + 3072);
    f32x16 s = {};
    s = MFMA32(k0, q0, s); s = MFMA32(k1, q1, s);
#pragma unroll
    for (int i = 0; i < 16; ++i) {
      bool ok = true;
      if (MASKED) { const int c = crow(i, h); ok = (t == qt) ? (c <= r) : ((t == qt - 8) ? (c > r) : true); }
      s[i] = ok ? __builtin_amdgcn_exp2f(s[i] - m) : 0.f;
    }
    bf16x8 pa = pack8(s, 0), pb = pack8(s, 8);
    o = MFMA32(v0, pa, o); o = MFMA32(v1, pb, o);
    la = MFMA16(ones, pa, la); la = MFMA16(ones, pb, la);
  }
  l = la[0];
}
DI bool ref_unsafe(const f32x16& o, float l) {
  float z = 0.f;
#pragma unroll
  for (int i = 0; i < 16; ++i) z = __builtin_fmaf(o[i], 0.f, z);
  const bool ok = (l >= 5.421010862e-20f) && (l <= 1.8446744e19f) && (z == 0.f);
  return __any(!ok);
}

__global__ __launch_bounds__(512, 2) void k_rest(const char* __restrict__ woa, const char* __restrict__ wob, char* __restrict__ ws,
                                                 char* __restrict__ out) {
  extern __shared__ __attribute__((aligned(16))) char lds[];
  STAMP_DECL STAMP(0); TRACE_DECL
  const int b = blockIdx.x, tid = threadIdx.x, lane = tid & 63;
  const int w = rfl(tid >> 6);
  const int r = lane & 31, h = lane >> 5;
  const unsigned lds0 = (unsigned)(uintptr_t)lds;
  const unsigned lo16 = lane * 16;
  const int qt = b >> 2, pr = w & 3, half = w >> 2;
  const int hA = 8 * (b & 3) + 2 * pr;
  const int hB = (b & 7) + 8 * (b >> 6), rb = (b >> 3) & 7;
  const int qtB = rb * 8 + w;
  const int xcd = b & 7, idx = b >> 3;
  const int mb = (xcd & 3) * 4 + (idx & 3), nb = (xcd >> 2) * 8 + (idx >> 2);
  const char* kva = ws + WS_KVA;
  const char* kvb = ws + WS_KVB + (size_t)hB * 64 * 4096;
  char* cntA_mine = ws + WS_CNT + (qt >> 2) * 256;
  char* cntB_mine = ws + WS_CNT + (16 + rb) * 256;
  char* cntA_need = ws + WS_CNT + mb * 256;
  char* cntB_need = ws + WS_CNT + (16 + (mb >> 1)) * 256;
  char* tmo = ws + WS_CNT + 24 * 256;
  bf16x8 ones;
  {
    const short v = ((((lane >> 4) & 1) == (((lane & 15) >> 2) & 1)) ? (short)0x3f80 : (short)0);
    ones = (bf16x8){v, v, v, v, v, v, v, v};
  }
  auto pieceA = [&](int k, int sl, int i) {
    const char* src = kva + (size_t)(half * 32 + 4 * k + pr) * 4096;
    const unsigned dst = lds0 + sl * SL + w * 4096;
    if (i == 0) glds16<0>(lo16, src, dst);
    else if (i == 1) glds16<1024>(lo16, src, dst);
    else if (i == 2) glds16<2048>(lo16, src, dst);
    else glds16<3072>(lo16, src, dst);
  };
  bf16x8 qb[2], q[2][2];
  float gA, gB;
  {
    const char* pa = ws + WS_QA + (size_t)(qt * 32 + hA) * 2048;
    gload16<0>(q[0][0], lo16, pa); gload16<1024>(q[0][1], lo16, pa);
    gload16<2048>(q[1][0], lo16, pa); gload16<3072>(q[1][1], lo16, pa);
    gload4(gA, r * 4, ws + WS_G + qt * 128);
  }
#pragma unroll
  for (int i = 0; i < 4; ++i) pieceA(0, 2, i);
  auto pieceB = [&](int n) {
    int gt = rb * 8 - 8 + 2 * w + (n >> 2);
    gt = gt < 0 ? 0 : gt;
    const char* src = kvb + (size_t)gt * 4096;
    const unsigned dst = lds0 + (2 * w + (n >> 2)) * 4096;
    if ((n & 3) == 0) glds16<0>(lo16, src, dst);
    else if ((n & 3) == 1) glds16<1024>(lo16, src, dst);
    else if ((n & 3) == 2) glds16<2048>(lo16, src, dst);
    else glds16<3072>(lo16, src, dst);
  };
  const char* pqb = ws + WS_QB + (size_t)(hB * 64 + qtB) * 2048;
  unsigned wo[2];
#pragma unroll
  for (int e = 0; e < 2; ++e) {
    const int row = 16 * w + 8 * e + (lane >> 3);
    const int c = (lane & 7) ^ (((row >> 1) & 1) << 2);
    wo[e] = row * 2048 + c * 16;
  }
  auto pieceW = [&](int st, int e) {
    const char* src = (st < 8 ? wob : woa) + (size_t)(st & 7) * 128 * 2048 + nb * 128;
    glds16<0>(wo[e], src, lds0 + WO_OFF + (st % 6) * 16384 + w * 2048 + e * 1024);
  };
  const f32x16 zero = {};
  f32x16 o0 = {}, o1 = {}, s0, s1;
  f32x4 l0 = {}, l1 = {};
  u32x4 p0a, p0b, p1a, p1b;
  bf16x8 ka0, ka1, kb0, kb1, va0, va1, vb0, vb1;
  lds_cptr kvbase = (lds_cptr)lds + half * 16384 + lane * 16;
  unsigned seenB = 0;
#define APAIR(pp, SL0, SL1, FIRST, LAST, PA, PB, MID, D0, D1, D2, D3) do { \
    lds_cptr cur = kvbase + (SL0) * SL + ((pp) & 1) * 8192; PIN(cur);     \
    lds_cptr nxt = (SL0) == (SL1) && !((pp) & 1) ? cur + 8192 : kvbase + (SL1) * SL; \
    __builtin_amdgcn_s_setprio(PA); \
    if (FIRST) { ka0 = lds_rd128(cur); ka1 = lds_rd128(cur + 1024); s0 = MFMA32(ka0, q[0][0], zero); s0 = MFMA32(ka1, q[0][1], s0); } \
    SBAR(); \
    APHASE(1, !(FIRST), s0, p0a, p0b, s1, zero, p1a, p1b, q[1][0], q[1][1], o1, l1, ka0, ka1, vb0, vb1, \
           (kb0 = lds_rd128(cur + 4096), kb1 = lds_rd128(cur + 5120)), (va0 = lds_rd128(cur + 2048), va1 = lds_rd128(cur + 3072))); \
    D0; SBAR(); \
    APHASE(1, 1, s1, p1a, p1b, s0, zero, p0a, p0b, q[0][0], q[0][1], o0, l0, kb0, kb1, va0, va1, (void)0, (void)0); \
    D1; SBAR(); \
    MID; \
    __builtin_amdgcn_s_setprio(PB); SBAR(); \
    APHASE(1, 1, s0, p0a, p0b, s1, zero, p1a, p1b, q[1][0], q[1][1], o1, l1, kb0, kb1, va0, va1, \
           if (!(LAST)) (ka0 = lds_rd128(nxt), ka1 = lds_rd128(nxt + 1024)), (vb0 = lds_rd128(cur + 4096 + 2048), vb1 = lds_rd128(cur + 4096 + 3072))); \
    D2; SBAR(); \
    APHASE(!(LAST), 1, s1, p1a, p1b, s0, zero, p0a, p0b, q[0][0], q[0][1], o0, l0, ka0, ka1, vb0, vb1, (void)0, (void)0); \
    D3; SBAR(); \
    if (LAST) { \
      o1 = MFMA32(vb0, ABC(p1a), o1); o1 = MFMA32(vb1, ABC(p1b), o1); \
      l1 = MFMA16(ones, ABC(p1a), l1); l1 = MFMA16(ones, ABC(p1b), l1); \
    } \
  } while (0)
#define NOP (void)0
#define BAR_V0 asm volatile("s_waitcnt vmcnt(0)\n\ts_barrier" ::: "memory")
#define DA(k, sl, i) pieceA(k, sl, i)
#define DB(n) pieceB(n)
#define DW(st, e) pieceW(st, e)
#define BAR_V(n) asm volatile("s_waitcnt vmcnt(" #n ")\n\ts_barrier" ::: "memory")
  asm volatile("s_waitcnt vmcnt(0)\n\ts_barrier" : "+v"(q[0][0]), "+v"(q[0][1]), "+v"(q[1][0]), "+v"(q[1][1]), "+v"(gA)::"memory");
  STAMP(1);
  APAIR(0, 2, 2, 1, 0, 3, 2, NOP, DA(1, 3, 0), DA(1, 3, 1), DA(1, 3, 2), DA(1, 3, 3));
  APAIR(1, 2, 3, 0, 0, 0, 3, BAR_V(2), DA(2, 4, 0), DA(2, 4, 1), DA(2, 4, 2), DA(2, 4, 3));
  APAIR(2, 3, 3, 0, 0, 2, 1, NOP, DB(0), DB(1), DB(2), DB(3));
  APAIR(3, 3, 4, 0, 0, 0, 3, BAR_V(6), DB(4), DB(5), DB(6), DB(7));
  APAIR(4, 4, 4, 0, 0, 2, 1, NOP, gload16<0>(qb[0], lo16, pqb), gload16<1024>(qb[1], lo16, pqb), gload4(gB, r * 4, ws + WS_G + qtB * 128), NOP);
  APAIR(5, 4, 4, 0, 1, 1, 0, NOP, NOP, NOP, NOP, NOP);
  asm volatile("s_waitcnt vmcnt(0) lgkmcnt(0)\n\ts_barrier" : "+v"(qb[0]), "+v"(qb[1]), "+v"(gB)::"memory");
  STAMP(2);
  {
    f32x16 o = {};
    f32x4 la = {};
    lds_cptr bb = (lds_cptr)lds + w * 4096 + lane * 16;
    f32x16 mD;
#pragma unroll
    for (int i = 0; i < 16; ++i) mD[i] = crow(i, h) <= r ? 0.f : -1.0e30f;
    if (rb != 0) {
      f32x16 sa, sb;
#pragma unroll
      for (int i = 0; i < 16; ++i) sa[i] = -1.0e30f - mD[i];
      u32x4 pa0 = {}, pa1 = {}, pb0 = {}, pb1 = {};
      bf16x8 k0, k1, k2, k3, v0, v1, v2, v3;
      k0 = lds_rd128(bb); k1 = lds_rd128(bb + 1024);
      k2 = lds_rd128(bb + 4096); k3 = lds_rd128(bb + 4096 + 1024);
      sa = MFMA32(k0, qb[0], sa); sa = MFMA32(k1, qb[1], sa);
      SBAR();
#define BRDK(KA, KB, j) (KA = lds_rd128(bb + (j) * 4096), KB = lds_rd128(bb + (j) * 4096 + 1024))
#define BRDV(VA, VB, j) (VA = lds_rd128(bb + (j) * 4096 + 2048), VB = lds_rd128(bb + (j) * 4096 + 3072))
#define B_DMA(n) do { pieceA(3 + (n) / 4, 2 + (n) / 4, (n) & 3); SBAR(); } while (0)
      __builtin_amdgcn_s_setprio(3);
      APHASE(1, 0, sa, pa0, pa1, sb, zero, pb0, pb1, qb[0], qb[1], o, la, k2, k3, v2, v3, BRDK(k0, k1, 2), BRDV(v0, v1, 0));
      B_DMA(0); B_DMA(1);
      APHASE(1, 1, sb, pb0, pb1, sa, zero, pa0, pa1, qb[0], qb[1], o, la, k0, k1, v0, v1, BRDK(k2, k3, 3), BRDV(v2, v3, 1));
      B_DMA(2); B_DMA(3);
      __builtin_amdgcn_s_setprio(2);
      APHASE(1, 1, sa, pa0, pa1, sb, zero, pb0, pb1, qb[0], qb[1], o, la, k2, k3, v2, v3, BRDK(k0, k1, 4), BRDV(v0, v1, 2));
      B_DMA(4); B_DMA(5);
      APHASE(1, 1, sb, pb0, pb1, sa, zero, pa0, pa1, qb[0], qb[1], o, la, k0, k1, v0, v1, BRDK(k2, k3, 5), BRDV(v2, v3, 3));
      B_DMA(6); B_DMA(7);
      __builtin_amdgcn_s_setprio(1);
      APHASE(1, 1, sa, pa0, pa1, sb, zero, pb0, pb1, qb[0], qb[1], o, la, k2, k3, v2, v3, BRDK(k0, k1, 6), BRDV(v0, v1, 4));
      B_DMA(8); B_DMA(9);
      APHASE(1, 1, sb, pb0, pb1, sa, zero, pa0, pa1, qb[0], qb[1], o, la, k0, k1, v0, v1, BRDK(k2, k3, 7), BRDV(v2, v3, 5));
      B_DMA(10); B_DMA(11);
      __builtin_amdgcn_s_setprio(0);
      APHASE(1, 1, sa, pa0, pa1, sb, zero, pb0, pb1, qb[0], qb[1], o, la, k2, k3, v2, v3, BRDK(k0, k1, 8), BRDV(v0, v1, 6));
      APHASE(1, 1, sb, pb0, pb1, sa, mD, pa0, pa1, qb[0], qb[1], o, la, k0, k1, v0, v1, (void)0, BRDV(v2, v3, 7));
      APHASE(0, 1, sa, pa0, pa1, sb, zero, pb0, pb1, qb[0], qb[1], o, la, k2, k3, v2, v3, (void)0, BRDV(v0, v1, 8));
      o = MFMA32(v0, ABC(pa0), o); o = MFMA32(v1, ABC(pa1), o);
      la = MFMA16(ones, ABC(pa0), la); la = MFMA16(ones, ABC(pa1), la);
    } else {
#pragma unroll
      for (int n = 0; n < 12; ++n) B_DMA(n);
      auto tile = [&](int j, const f32x16& cin) {
        lds_cptr p = bb + j * 4096;
        bf16x8 k0 = lds_rd128(p), k1 = lds_rd128(p + 1024), v0 = lds_rd128(p + 2048), v1 = lds_rd128(p + 3072);
        f32x16 s = MFMA32(k0, qb[0], cin);
        s = MFMA32(k1, qb[1], s);
        exp16(s);
        bf16x8 pa = pack8(s, 0), pb = pack8(s, 8);
        o = MFMA32(v0, pa, o); o = MFMA32(v1, pb, o);
        la = MFMA16(ones, pa, la); la = MFMA16(ones, pb, la);
      };
      for (int j = 8 - w; j < 8; ++j) tile(j, zero);
      tile(8, mD);
    }
    float l = la[0];
    if (__builtin_expect(ref_unsafe(o, l), 0)) attn_exact<true>(kvb, qtB >= 8 ? qtB - 8 : 0, qtB + 1, qtB, qb[0], qb[1], ones, o, l);
    const float sc = (1.f - gB) * __builtin_amdgcn_rcpf(l);
    char* dst = ws + WS_O + (size_t)(qtB * 64 + 32 + hB) * 2048;
    gstore16_sc1<0>(lo16, dst, pack8s(o, 0, sc));
    gstore16_sc1<1024>(lo16, dst, pack8s(o, 8, sc));
  }
  asm volatile("s_waitcnt vmcnt(2) lgkmcnt(0)\n\ts_barrier" ::: "memory");
  STAMP(3);
  const int wm = w & 3, kh = w >> 2;
  const char* obase = ws + WS_O + (size_t)(mb * 4 + wm) * 64 * 2048 + kh * 4096;
  bf16x8 of[5][4];
  auto loadO1 = [&](int st, int i, bf16x8& f) {
    const char* src = obase + (size_t)(st < 8 ? 32 + 4 * st : 4 * (st - 8)) * 2048;
    if (i == 0) gload16_sc1<0>(f, lo16, src);
    else if (i == 1) gload16_sc1<1024>(f, lo16, src);
    else if (i == 2) gload16_sc1<2048>(f, lo16, src);
    else gload16_sc1<3072>(f, lo16, src);
  };
#define LO(st, i) loadO1(st, i, of[st][i])
  auto loadO = [&](int st, bf16x8 (&f)[4]) {
    const char* src = obase + (size_t)(st < 8 ? 32 + 4 * st : 4 * (st - 8)) * 2048;
    gload16_sc1<0>(f[0], lo16, src); gload16_sc1<1024>(f[1], lo16, src);
    gload16_sc1<2048>(f[2], lo16, src); gload16_sc1<3072>(f[3], lo16, src);
  };
  APAIR(6, 2, 2, 1, 0, 3, 3, NOP, DA(6, 0, 0), DA(6, 0, 1), DA(6, 0, 2), DA(6, 0, 3));
  APAIR(7, 2, 3, 0, 0, 3, 2, NOP, DA(7, 1, 0), DA(7, 1, 1), DA(7, 1, 2), DA(7, 1, 3));
  unsigned warm = 0;
  {
    const int R = 768 + (idx & 3) * 320 + w * 40 + (lane < 40 ? lane : 39);
    const char* pw = (R < 1024 ? wob + (size_t)R * 2048 : woa + (size_t)(R - 1024) * 2048) + nb * 128;
    asm volatile("global_load_dword %0, %1, off" : "+v"(warm) : "v"(pw) : "memory");
  }
  APAIR(8, 3, 3, 0, 0, 2, 2, NOP, NOP, NOP, NOP, NOP);
  APAIR(9, 3, 4, 0, 0, 1, 1, NOP, NOP, NOP, NOP, NOP);
  APAIR(10, 4, 4, 0, 0, 1, 0, NOP, NOP, NOP, NOP, NOP);
  APAIR(11, 4, 0, 0, 0, 0, 3, { asm volatile("s_waitcnt vmcnt(0)\n\ts_barrier" : "+v"(warm)::"memory"); if (tid == 0) signal(cntB_mine);   }, NOP, NOP, NOP, NOP);
  APAIR(12, 0, 0, 0, 0, 3, 2, NOP, DW(0, 0), DW(0, 1), DW(1, 0), DW(1, 1));
  if (w == 0) poll_issue(seenB, cntB_need);
  APAIR(13, 0, 1, 0, 0, 2, 1, NOP, DW(2, 0), DW(2, 1), DW(3, 0), DW(3, 1));
  APAIR(14, 1, 1, 0, 0, 0, 3, { asm volatile("s_waitcnt vmcnt(0)" : "+v"(seenB)::"memory"); if (w == 0) poll_finish(seenB, cntB_need, tmo, 32u); asm volatile("s_barrier" ::: "memory"); },
        NOP, NOP, (LO(0, 0), LO(0, 1), DW(4, 0)), (LO(0, 2), LO(0, 3), DW(4, 1)));
  APAIR(15, 1, 1, 0, 1, 2, 1, NOP, (LO(1, 0), LO(1, 1), DW(5, 0)), (LO(1, 2), LO(1, 3), DW(5, 1)), (LO(2, 0), LO(2, 1)), (LO(2, 2), LO(2, 3)));
  STAMP(4);

  loadO(3, of[3]);
  asm volatile("s_waitcnt lgkmcnt(0)\n\ts_barrier" ::: "memory");
  {
    float* xb = (float*)(lds + XCH_OFF) + w * (5 * 256);
    const f32x16& os = half ? o0 : o1;
    const float ls = half ? l0[0] : l1[0];
#pragma unroll
    for (int g = 0; g < 4; ++g) *(f32x4*)(xb + g * 256 + lane * 4) = (f32x4){os[4 * g], os[4 * g + 1], os[4 * g + 2], os[4 * g + 3]};
    xb[4 * 256 + lane] = ls;
  }
  asm volatile("s_waitcnt lgkmcnt(0)\n\ts_barrier" ::: "memory");
  {
    const float* xb = (const float*)(lds + XCH_OFF) + (w ^ 4) * (5 * 256);
    f32x16 o = half ? o1 : o0;
    float l = half ? l1[0] : l0[0];
#pragma unroll
    for (int g = 0; g < 4; ++g) {
      f32x4 v = *(const f32x4*)(xb + g * 256 + lane * 4);
#pragma unroll
      for (int e = 0; e < 4; ++e) o[4 * g + e] += v[e];
    }
    l += xb[4 * 256 + lane];
    if (__builtin_expect(ref_unsafe(o, l), 0)) {
      if (half) attn_exact<false>(kva, 0, 64, 0, q[1][0], q[1][1], ones, o, l);
      else attn_exact<false>(kva, 0, 64, 0, q[0][0], q[0][1], ones, o, l);
    }
    const float sc = gA * __builtin_amdgcn_rcpf(l);
    char* dst = ws + WS_O + (size_t)(qt * 64 + hA + half) * 2048;
    gstore16_sc1<0>(lo16, dst, pack8s(o, 0, sc));
    gstore16_sc1<1024>(lo16, dst, pack8s(o, 8, sc));
  }
  STAMP(5);
  {
    const int g16 = lane >> 4, q4 = (lane & 15) >> 2, p4 = lane & 3;
    const int wc = 2 * (g16 & 1) + (p4 >> 1);
    const int wa0 = (kh * 64 + 4 * h + q4) * 128 + ((wc ^ (((q4 >> 1) & 1) << 2)) << 4) + 8 * (p4 & 1);
    lds_cptr wbase[2][2] = {{(lds_cptr)lds + WO_OFF + wa0, (lds_cptr)lds + WO_OFF + (wa0 ^ 64)},
                            {(lds_cptr)lds + WO_OFF + 49152 + wa0, (lds_cptr)lds + WO_OFF + 49152 + (wa0 ^ 64)}};
    PIN(wbase[0][0]); PIN(wbase[0][1]); PIN(wbase[1][0]); PIN(wbase[1][1]);
    f32x16 acc[2] = {};
    bf16x8 wf[4][2];
    unsigned seenA = 0;
    auto rdw = [&](bf16x8 (&f)[2], int st, int ks) {
#pragma unroll
      for (int j = 0; j < 2; ++j) {
        lds_cptr p = wbase[(st % 6) / 3][j] + ((st % 6) % 3) * 16384 + ks * 2048;
        f[j] = cat8(lds_tr(p), lds_tr(p + 1024));
      }
    };
    asm volatile("s_waitcnt vmcnt(17)" : "+v"(of[0][0]), "+v"(of[0][1]), "+v"(of[0][2]), "+v"(of[0][3])::"memory");
    rdw(wf[0], 0, 0); rdw(wf[1], 0, 1);
#define OTRACE(i) do {} while (0)
#define O_MM(f, a) do { acc[0] = MFMA32(f[0], a, acc[0]); SBAR(); acc[1] = MFMA32(f[1], a, acc[1]); SBAR(); } while (0)
#define O_STAGE(st, VM, PRE, POST) do { \
      rdw(wf[2], st, 2); SBAR(); O_MM(wf[0], of[(st) % 5][0]); \
      rdw(wf[3], st, 3); SBAR(); O_MM(wf[1], of[(st) % 5][1]); \
      if ((st) < 15) { \
        OTRACE(3 * (st)); \
        asm volatile("s_waitcnt vmcnt(" #VM ")" : "+v"(of[((st) + 1) % 5][0]), "+v"(of[((st) + 1) % 5][1]), \
                     "+v"(of[((st) + 1) % 5][2]), "+v"(of[((st) + 1) % 5][3]), "+v"(seenA)::"memory"); \
        PRE; \
        OTRACE(3 * (st) + 1); \
        asm volatile("s_barrier" ::: "memory"); \
        OTRACE(3 * (st) + 2); \
        POST; \
        SBAR(); \
        rdw(wf[0], (st) + 1, 0); \
      } \
      SBAR(); O_MM(wf[2], of[(st) % 5][2]); \
      if ((st) < 15) rdw(wf[1], (st) + 1, 1); \
      SBAR(); O_MM(wf[3], of[(st) % 5][3]); \
    } while (0)
#define OW(st) (pieceW(st, 0), pieceW(st, 1))
#define OL(st) loadO(st, of[(st) % 5])
    O_STAGE(0, 11, NOP, OL(4));
    O_STAGE(1, 4, NOP, { if (tid == 0) signal(cntA_mine);   OW(6); OL(5); });
    O_STAGE(2, 10, NOP, (OW(7), OL(6)));
    O_STAGE(3, 12, NOP, { if (w == 0) poll_issue(seenA, cntA_need); OW(8); OL(7); });
    O_STAGE(4, 12, NOP, OW(9));
    O_STAGE(5, 8, if (w == 0) poll_finish(seenA, cntA_need, tmo, 16u), (OW(10), OL(8), OL(9)));
    O_STAGE(6, 12, NOP, (OW(11), OL(10)));
    O_STAGE(7, 10, NOP, (OW(12), OL(11)));
    O_STAGE(8, 12, NOP, (OW(13), OL(12)));
    O_STAGE(9, 12, NOP, (OW(14), OL(13)));
    O_STAGE(10, 12, NOP, (OW(15), OL(14)));
    O_STAGE(11, 12, NOP, OL(15));
    O_STAGE(12, 10, NOP, NOP);
    O_STAGE(13, 4, NOP, NOP);
    O_STAGE(14, 0, NOP, NOP);
    O_STAGE(15, 0, NOP, NOP);
    STAMP(6);
    float* xw = (float*)(lds + XCH_OFF) + w * 1024 + lane * 4;
    const f32x16& as = kh ? acc[0] : acc[1];
#pragma unroll
    for (int g = 0; g < 4; ++g) *(f32x4*)(xw + g * 256) = (f32x4){as[4 * g], as[4 * g + 1], as[4 * g + 2], as[4 * g + 3]};
    asm volatile("s_waitcnt lgkmcnt(0)\n\ts_barrier" ::: "memory");
    f32x16 t = kh ? acc[1] : acc[0];
    const float* xr = (const float*)(lds + XCH_OFF) + (w ^ 4) * 1024 + lane * 4;
#pragma unroll
    for (int g = 0; g < 4; ++g) {
      f32x4 v = *(const f32x4*)(xr + g * 256);
#pragma unroll
      for (int e = 0; e < 4; ++e) t[4 * g + e] += v[e];
    }
    const int row = mb * 128 + wm * 32 + r;
    char* dst = out + (size_t)row * 2048 + (nb * 64 + kh * 32) * 2;
#pragma unroll
    for (int g = 0; g < 4; g += 2) {
      u32x2 a = {cvtpk(t[4 * g], t[4 * g + 1]), cvtpk(t[4 * g + 2], t[4 * g + 3])};
      u32x2 c = {cvtpk(t[4 * g + 4], t[4 * g + 5]), cvtpk(t[4 * g + 6], t[4 * g + 7])};
      auto x = __builtin_amdgcn_permlane32_swap(a[0], c[0], false, false);
      auto y = __builtin_amdgcn_permlane32_swap(a[1], c[1], false, false);
      *(u32x4*)(dst + g * 16 + h * 16) = (u32x4){x[0], y[0], x[1], y[1]};
    }
  }
  STAMP(7);
  STAMP_OUT(1, ws); TRACE_OUT(ws);
}

extern "C" void kernel_launch(void* const* d_in, const int* in_sizes, int n_in, void* d_out, int out_size, void* d_ws,
                              size_t ws_size, hipStream_t stream) {
  const char* xa = (const char*)d_in[0];
  const char* wq = (const char*)d_in[1];
  const char* wk = (const char*)d_in[2];
  const char* wv = (const char*)d_in[3];
  const char* woa = (const char*)d_in[4];
  const char* wqkv = (const char*)d_in[6];
  const char* wob = (const char*)d_in[7];
  const char* gw = (const char*)d_in[8];
  char* ws = (char*)d_ws;
  (void)hipFuncSetAttribute((const void*)k_proj, hipFuncAttributeMaxDynamicSharedMemorySize, P_LDS);
  (void)hipFuncSetAttribute((const void*)k_rest, hipFuncAttributeMaxDynamicSharedMemorySize, R_LDS);
  k_proj<<<256, 512, P_LDS, stream>>>(xa, wq, wk, wv, wqkv, gw, ws);
  k_rest<<<256, 512, R_LDS, stream>>>(woa, wob, ws, (char*)d_out);
}
```

```cpp
#include <hip/hip_runtime.h>
#include <hip/hip_bf16.h>
#include <stdint.h>

typedef float f32x16 __attribute__((ext_vector_type(16)));
typedef float f32x4 __attribute__((ext_vector_type(4)));
typedef float f32x2 __attribute__((ext_vector_type(2)));
typedef short bf16x8 __attribute__((ext_vector_type(8)));
typedef short s16x4 __attribute__((ext_vector_type(4)));
typedef __bf16 bf16x2 __attribute__((ext_vector_type(2)));
typedef unsigned u32x4 __attribute__((ext_vector_type(4)));
typedef unsigned u32x2 __attribute__((ext_vector_type(2)));
typedef __attribute__((address_space(3))) const char* lds_cptr;
typedef __attribute__((address_space(3))) char* lds_ptr;

#define DI __device__ __forceinline__
#define SBAR() __builtin_amdgcn_sched_barrier(0)
#define PIN(x) asm volatile("" : "+v"(x))
#define MFMA32(a, b, c) __builtin_amdgcn_mfma_f32_32x32x16_bf16(a, b, c, 0, 0, 0)
#define MFMA16(a, b, c) __builtin_amdgcn_mfma_f32_16x16x32_bf16(a, b, c, 0, 0, 0)

constexpr int SEQ = 2048, DM = 1024;
constexpr float QSCALE = 0.17677669529663687f * 1.4426950408889634f;

constexpr size_t WS_QA = 0;
constexpr size_t WS_KVA = 4u << 20;
constexpr size_t WS_G = WS_KVA + (256u << 10);
constexpr size_t WS_CNT = WS_G + 8192;
constexpr size_t WS_QB = 5u << 20;
constexpr size_t WS_KVB = 9u << 20;
constexpr size_t WS_O = 17u << 20;

#define TRACE(i) do {} while (0)
#define TRACE_DECL
#define TRACE_OUT(wsp) do {} while (0)
#define STAMP_DECL
#define STAMP(i) do {} while (0)
#define STAMP_OUT(k, wsp) do {} while (0)
DI unsigned rfl(unsigned x) { return __builtin_amdgcn_readfirstlane(x); }
DI unsigned cvtpk(float lo, float hi) {
  f32x2 v = {lo, hi};
  bf16x2 b = __builtin_convertvector(v, bf16x2);
  return __builtin_bit_cast(unsigned, b);
}
DI bf16x8 pack8(const f32x16& a, int o) {
  u32x4 r = {cvtpk(a[o], a[o + 1]), cvtpk(a[o + 2], a[o + 3]), cvtpk(a[o + 4], a[o + 5]), cvtpk(a[o + 6], a[o + 7])};
  return __builtin_bit_cast(bf16x8, r);
}
DI bf16x8 pack8s(const f32x16& a, int o, float s) {
  u32x4 r = {cvtpk(a[o] * s, a[o + 1] * s), cvtpk(a[o + 2] * s, a[o + 3] * s), cvtpk(a[o + 4] * s, a[o + 5] * s),
             cvtpk(a[o + 6] * s, a[o + 7] * s)};
  return __builtin_bit_cast(bf16x8, r);
}
template <int OFF>
DI void glds16(unsigned voff, const void* sbase, unsigned lds_base) {
  asm volatile("s_mov_b32 m0, %2\n\ts_nop 0\n\tglobal_load_lds_dwordx4 %0, %1 offset:%3"
               :: "v"(voff), "s"(sbase), "s"(lds_base), "i"(OFF) : "memory");
}
DI bf16x8 lds_rd128(lds_cptr p) { return *(const __attribute__((address_space(3))) bf16x8*)p; }
DI s16x4 lds_tr(lds_cptr p) {
  typedef short v4i16_t __attribute__((ext_vector_type(4)));
  return __builtin_bit_cast(s16x4, __builtin_amdgcn_ds_read_tr16_b64_v4i16((__attribute__((address_space(3))) v4i16_t*)p));
}
DI bf16x8 cat8(s16x4 lo, s16x4 hi) { return (bf16x8){lo[0], lo[1], lo[2], lo[3], hi[0], hi[1], hi[2], hi[3]}; }
DI float bf2f(unsigned short b) { return __uint_as_float((unsigned)b << 16); }

constexpr int P_SLOT = 53248;
constexpr int P_XOFF = 0, P_WOFF = 16384, P_KVOFF = 49152;
constexpr int P_LDS = 3 * P_SLOT;

struct PFrag { bf16x8 x[2]; bf16x8 w[2]; };

template <int MODE>
DI void proj_body(const char* __restrict__ xg, const char* __restrict__ wg, int ldw_bytes, const char* __restrict__ wkv,
                  char* __restrict__ ws, int mb, int nb, int ni, char* lds, const char* __restrict__ xa,
                  const char* __restrict__ gw) {
  STAMP_DECL STAMP(0);
  constexpr bool SIDE = MODE >= 2;
  constexpr int NP = SIDE ? 7 : 6;
  const int tid = threadIdx.x, lane = tid & 63;
  const int w = rfl(tid >> 6);
  const int wm = w & 1, wn = w >> 1;
  const int r = lane & 31, h = lane >> 5;
  const unsigned lds0 = (unsigned)(uintptr_t)lds;
  const int grow = blockIdx.x * 8 + w;
  u32x4 gx0, gx1, gw0, gw1;
  asm volatile("global_load_dwordx4 %0, %4, %5\n\tglobal_load_dwordx4 %1, %4, %5 offset:16\n\t"
               "global_load_dwordx4 %2, %4, %6\n\tglobal_load_dwordx4 %3, %4, %6 offset:16"
               : "=&v"(gx0), "=&v"(gx1), "=&v"(gw0), "=&v"(gw1) : "v"(lane * 32), "s"(xa + (size_t)grow * 2048), "s"(gw) : "memory");
  unsigned xo[2], wo[4];
#pragma unroll
  for (int e = 0; e < 2; ++e) {
    const int row = 16 * w + 8 * e + (lane >> 3);
    const int c = (lane & 7) ^ (4 * e + (lane >> 4));
    xo[e] = row * 2048 + c * 16;
  }
#pragma unroll
  for (int e = 0; e < 4; ++e) {
    const int krow = 8 * w + 2 * e + (lane >> 5);
    const int c = (lane & 31) ^ ((krow & 3) << 2);
    wo[e] = krow * ldw_bytes + c * 16;
  }
  const unsigned kvo = w * 512 + (lane & 31) * 16;
  const char* xsrc = xg + (size_t)mb * 128 * 2048;
  auto piece = [&](int pc, int st, int slot) {
    const unsigned lb = lds0 + slot * P_SLOT;
    if (pc < 2) glds16<0>(xo[pc], xsrc + st * 128, lb + P_XOFF + w * 2048 + pc * 1024);
    else if (pc < 6) glds16<0>(wo[pc - 2], wg + (size_t)st * 64 * ldw_bytes, lb + P_WOFF + w * 4096 + (pc - 2) * 1024);
    else if constexpr (SIDE) {
      if (lane < 32) glds16<0>(kvo, wkv + st * 4096, lb + P_KVOFF + w * 512);
    }
  };
  auto dma = [&](int st, int slot) {
#pragma unroll
    for (int pc = 0; pc < NP; ++pc) piece(pc, st, slot);
  };
  const int t3 = (r >> 1) & 7;
  const int xa0 = P_XOFF + (wm * 64 + r) * 128 + ((h ^ t3) << 4);
  const int g16 = lane >> 4, q4 = (lane & 15) >> 2, p4 = lane & 3;
  const int wc = wn * 8 + 2 * (g16 & 1) + (p4 >> 1);
  const int wa0 = P_WOFF + (8 * h + q4) * 512 + ((wc ^ (q4 << 2)) << 4) + 8 * (p4 & 1);
  const int ss = w >> 1;
  const int sxa = P_XOFF + (32 * (2 * (ni & 1) + (w & 1)) + r) * 128 + (((2 * ss + h) ^ t3) << 4);
  const int swa = P_KVOFF + (16 * ss + 8 * h + q4) * 64 + 32 * (g16 & 1) + 8 * p4;

  auto rdx = [&](PFrag& f, int i, int slot, int s) { f.x[i] = lds_rd128((lds_cptr)lds + slot * P_SLOT + (xa0 ^ (s << 5)) + i * 4096); };
  auto rdw = [&](PFrag& f, int j, int slot, int s) {
    lds_cptr p = (lds_cptr)lds + slot * P_SLOT + (wa0 ^ (j << 6)) + s * 8192;
    f.w[j] = cat8(lds_tr(p), lds_tr(p + 2048));
  };
  f32x16 acc[2][2] = {};
  f32x16 sacc = {};
  auto mm = [&](const PFrag& f, int i, int j) {
    if constexpr (MODE == 1) acc[i][j] = MFMA32(f.x[i], f.w[j], acc[i][j]);
    else acc[i][j] = MFMA32(f.w[j], f.x[i], acc[i][j]);
  };
  auto step = [&](const PFrag& c, PFrag& n, int rslot, int rs, bool rd, int pa, int pb, int dst, int dslot, bool dm) {
    mm(c, 0, 0); SBAR();
    if (rd) { rdw(n, 0, rslot, rs); rdx(n, 0, rslot, rs); }
    SBAR();
    mm(c, 0, 1); SBAR();
    if (rd) { rdw(n, 1, rslot, rs); rdx(n, 1, rslot, rs); }
    SBAR();
    mm(c, 1, 0); SBAR();
    if (dm && pa >= 0) piece(pa, dst, dslot);
    SBAR();
    mm(c, 1, 1); SBAR();
    if (dm && pb >= 0) piece(pb, dst, dslot);
    SBAR();
  };
  bf16x8 sxf, swf;
  auto side_rd = [&](int slot) {
    if constexpr (SIDE) {
      lds_cptr b = (lds_cptr)lds + slot * P_SLOT;
      sxf = lds_rd128(b + sxa);
      swf = cat8(lds_tr(b + swa), lds_tr(b + swa + 256));
    }
  };
  auto side_mm = [&]() {
    if constexpr (MODE == 2) sacc = MFMA32(swf, sxf, sacc);
    if constexpr (MODE == 3) sacc = MFMA32(sxf, swf, sacc);
  };

  STAMP(5);
  dma(0, 0);
  asm volatile("s_barrier" ::: "memory");
  STAMP(6);
  dma(1, 1); dma(2, 2);
  STAMP(7);
  if constexpr (SIDE) asm volatile("s_waitcnt vmcnt(14)\n\ts_barrier" : "+v"(gx0), "+v"(gx1), "+v"(gw0), "+v"(gw1)::"memory");
  else asm volatile("s_waitcnt vmcnt(12)\n\ts_barrier" : "+v"(gx0), "+v"(gx1), "+v"(gw0), "+v"(gw1)::"memory");
  {
    float z = 0.f;
#pragma unroll
    for (int i = 0; i < 4; ++i) {
      z += __uint_as_float(gx0[i] << 16) * __uint_as_float(gw0[i] << 16) + __uint_as_float(gx0[i] & 0xffff0000u) * __uint_as_float(gw0[i] & 0xffff0000u);
      z += __uint_as_float(gx1[i] << 16) * __uint_as_float(gw1[i] << 16) + __uint_as_float(gx1[i] & 0xffff0000u) * __uint_as_float(gw1[i] & 0xffff0000u);
    }
#pragma unroll
    for (int o = 32; o >= 1; o >>= 1) z += __shfl_xor(z, o);
    if (lane == 0) ((float*)(ws + WS_G))[grow] = 1.f / (1.f + __expf(-z));
  }
  STAMP(1);
  PFrag f0, f1;
  rdw(f0, 0, 0, 0); rdx(f0, 0, 0, 0); rdw(f0, 1, 0, 0); rdx(f0, 1, 0, 0);
  int slot = 0, pslot = 0;
  for (int it = 0; it < 16; ++it) {
    const int nslot = slot == 2 ? 0 : slot + 1;
    const bool dmp = it >= 1 && it <= 13;
    side_rd(slot); SBAR();
    step(f0, f1, slot, 1, true, 2, 3, it + 2, pslot, dmp);
    side_mm(); SBAR();
    step(f1, f0, slot, 2, true, 4, 5, it + 2, pslot, dmp);
    step(f0, f1, slot, 3, true, SIDE ? 6 : -1, -1, it + 2, pslot, dmp);
    if (it < 14) {
      if constexpr (SIDE) asm volatile("s_waitcnt vmcnt(7) lgkmcnt(0)\n\ts_barrier" ::: "memory");
      else asm volatile("s_waitcnt vmcnt(6) lgkmcnt(0)\n\ts_barrier" ::: "memory");
    } else {
      asm volatile("s_waitcnt vmcnt(0) lgkmcnt(0)\n\ts_barrier" ::: "memory");
    }
    SBAR();
    step(f1, f0, nslot, 0, it < 15, 0, 1, it + 3, slot, it < 13);
    pslot = slot;
    slot = nslot;
  }
  STAMP(2);
#pragma unroll
  for (int i = 0; i < 2; ++i)
#pragma unroll
    for (int j = 0; j < 2; ++j) {
      const int mt = mb * 4 + wm * 2 + i;
      const int hd = (nb & 3) * 8 + wn * 2 + j;
      char* dst;
      float sc = 1.f;
      if constexpr (MODE >= 2) { dst = ws + WS_QA + (size_t)(mt * 32 + hd) * 2048; sc = QSCALE; }
      else if constexpr (MODE == 1) dst = ws + WS_KVB + (size_t)(hd * 64 + mt) * 4096 + 2048;
      else {
        if (nb < 4) { dst = ws + WS_QB + (size_t)(hd * 64 + mt) * 2048; sc = QSCALE; }
        else dst = ws + WS_KVB + (size_t)(hd * 64 + mt) * 4096;
      }
      *(bf16x8*)(dst + lane * 16) = pack8s(acc[i][j], 0, sc);
      *(bf16x8*)(dst + 1024 + lane * 16) = pack8s(acc[i][j], 8, sc);
    }
  if constexpr (SIDE) {
    __syncthreads();
    float* red = (float*)lds;
#pragma unroll
    for (int g = 0; g < 4; ++g)
      *(f32x4*)(red + (w * 4 + g) * 256 + lane * 4) = (f32x4){sacc[4 * g], sacc[4 * g + 1], sacc[4 * g + 2], sacc[4 * g + 3]};
    __syncthreads();
    if (w < 2) {
      f32x16 t;
#pragma unroll
      for (int g = 0; g < 4; ++g) {
        f32x4 a = *(const f32x4*)(red + ((w + 0) * 4 + g) * 256 + lane * 4);
        f32x4 b = *(const f32x4*)(red + ((w + 2) * 4 + g) * 256 + lane * 4);
        f32x4 c = *(const f32x4*)(red + ((w + 4) * 4 + g) * 256 + lane * 4);
        f32x4 d = *(const f32x4*)(red + ((w + 6) * 4 + g) * 256 + lane * 4);
#pragma unroll
        for (int e = 0; e < 4; ++e) t[4 * g + e] = (a[e] + b[e]) + (c[e] + d[e]);
      }
      const int mt = mb * 4 + 2 * (ni & 1) + w;
      char* dst = ws + WS_KVA + (size_t)mt * 4096 + (MODE == 3 ? 2048 : 0);
      *(bf16x8*)(dst + lane * 16) = pack8(t, 0);
      *(bf16x8*)(dst + 1024 + lane * 16) = pack8(t, 8);
    }
  }
  STAMP(3);
  STAMP(4);
  STAMP_OUT(0, ws);
}

__global__ __launch_bounds__(512, 2) void k_proj(const char* __restrict__ xa, const char* __restrict__ wq,
                                                 const char* __restrict__ wk, const char* __restrict__ wv,
                                                 const char* __restrict__ wqkv,
                                                 const char* __restrict__ gw, char* __restrict__ ws) {
  extern __shared__ __attribute__((aligned(16))) char lds[];
  const int b = blockIdx.x, xcd = b & 7, idx = b >> 3;
  const int mb = (xcd & 1) * 8 + (idx & 7), ni = idx >> 3, nb = (xcd >> 1) * 4 + ni;
  if (b == 0 && threadIdx.x < 32) *(unsigned*)(ws + WS_CNT + threadIdx.x * 256) = 0u;
  if (idx == 0) {
    unsigned long long pc;
    asm volatile("s_getpc_b64 s[40:41]\n\ts_add_u32 s40, s40, _Z6k_restPKcS0_PcS1_@rel32@lo+4\n\ts_addc_u32 s41, s41, _Z6k_restPKcS0_PcS1_@rel32@hi+12\n\ts_mov_b64 %0, s[40:41]" : "=s"(pc) :: "s40", "s41", "scc");
    unsigned junk;
    asm volatile("global_load_dword %0, %1, %2\n\ts_waitcnt vmcnt(0)" : "=v"(junk) : "v"((threadIdx.x < 230 ? threadIdx.x : 0) * 128u), "s"(pc) : "memory");
  }
  if (nb < 8) proj_body<0>(xa, wqkv + nb * 512, 6144, nullptr, ws, mb, nb, ni, lds, xa, gw);
  else if (nb < 12) proj_body<1>(xa, wqkv + nb * 512, 6144, nullptr, ws, mb, nb, ni, lds, xa, gw);
  else if (ni < 2) proj_body<2>(xa, wq + ni * 512, 2048, wk, ws, mb, nb, ni, lds, xa, gw);
  else proj_body<3>(xa, wq + ni * 512, 2048, wv, ws, mb, nb, ni, lds, xa, gw);
}

constexpr int SL = 32768;
constexpr int R_LDS = 5 * SL;
constexpr int XCH_OFF = 0;
constexpr int WO_OFF = 2 * SL;
typedef __attribute__((address_space(1))) unsigned gu32;

DI int crow(int r, int h) { return (r & 3) + 8 * (r >> 2) + 4 * h; }
DI float swap_half(float x) {
  auto rr = __builtin_amdgcn_permlane32_swap(__float_as_uint(x), __float_as_uint(x), false, false);
  return __uint_as_float((threadIdx.x & 32) ? rr[0] : rr[1]);
}
DI void exp16(f32x16& s) {
#pragma unroll
  for (int i = 0; i < 16; ++i) s[i] = __builtin_amdgcn_exp2f(s[i]);
}
template <int OFF>
DI void gload16(bf16x8& d, unsigned voff, const void* sbase) {
  asm volatile("global_load_dwordx4 %0, %1, %2 offset:%3" : "=v"(d) : "v"(voff), "s"(sbase), "i"(OFF) : "memory");
}
template <int OFF>
DI void gload16_sc1(bf16x8& d, unsigned voff, const void* sbase) {
  asm volatile("global_load_dwordx4 %0, %1, %2 offset:%3 sc1" : "=v"(d) : "v"(voff), "s"(sbase), "i"(OFF) : "memory");
}
template <int OFF>
DI void gstore16_sc1(unsigned voff, void* sbase, const bf16x8& d) {
  asm volatile("global_store_dwordx4 %0, %1, %2 offset:%3 sc1\n\ts_nop 1" :: "v"(voff), "v"(d), "s"(sbase), "i"(OFF) : "memory");
}
DI void gload4(float& d, unsigned voff, const void* sbase) {
  asm volatile("global_load_dword %0, %1, %2" : "=v"(d) : "v"(voff), "s"(sbase) : "memory");
}
DI void poll_issue(unsigned& d, const void* sbase) {
  asm volatile("global_load_dword %0, %1, %2 sc1" : "+v"(d) : "v"(0u), "s"(sbase) : "memory");
}
DI void poll_finish(unsigned seen, char* cnt, char* tmo, unsigned want) {
  if (__builtin_expect(rfl(seen) < want, 0)) {
    for (unsigned spins = 0; spins < (1u << 22); ++spins) {
      if (rfl(__hip_atomic_load((gu32*)cnt, __ATOMIC_RELAXED, __HIP_MEMORY_SCOPE_AGENT)) >= want) return;
      __builtin_amdgcn_s_sleep(2);
    }
    __hip_atomic_store((gu32*)tmo, 1u, __ATOMIC_RELAXED, __HIP_MEMORY_SCOPE_AGENT);
  }
}
DI void signal(char* cnt) { (void)__hip_atomic_fetch_add((gu32*)cnt, 1u, __ATOMIC_RELAXED, __HIP_MEMORY_SCOPE_AGENT); }
DI void setprio_dyn(int p) {
  if (p == 0) __builtin_amdgcn_s_setprio(0);
  else if (p == 1) __builtin_amdgcn_s_setprio(1);
  else if (p == 2) __builtin_amdgcn_s_setprio(2);
  else __builtin_amdgcn_s_setprio(3);
}

#define AE(S, i) S[i] = __builtin_amdgcn_exp2f(S[i])
#define AC(P, S, i) P[(i) & 3] = cvtpk(S[2 * (i)], S[2 * (i) + 1])
#define ABC(x) __builtin_bit_cast(bf16x8, x)
#define APHASE(QK, PV, SX, PXa, PXb, SY, CINY, PYa, PYb, QY0, QY1, OY, LY, KF0, KF1, VF0, VF1, R1, R2) do { \
  if (QK) { SY = MFMA32(KF0, QY0, CINY); PIN(SY); } SBAR(); \
  AE(SX, 0); AE(SX, 1); AE(SX, 2); AE(SX, 3); PIN(SX); SBAR(); \
  if (QK) { SY = MFMA32(KF1, QY1, SY); PIN(SY); } SBAR(); \
  R1; \
  AC(PXa, SX, 0); AC(PXa, SX, 1); AE(SX, 4); AE(SX, 5); AE(SX, 6); PIN(SX); PIN(PXa); SBAR(); \
  if (PV) { OY = MFMA32(VF0, ABC(PYa), OY); PIN(OY); } SBAR(); \
  AE(SX, 7); AC(PXa, SX, 2); AE(SX, 8); AE(SX, 9); AC(PXa, SX, 3); PIN(SX); PIN(PXa); SBAR(); \
  if (PV) { LY = MFMA16(ones, ABC(PYa), LY); PIN(LY); } SBAR(); \
  R2; \
  AE(SX, 10); AE(SX, 11); PIN(SX); SBAR(); \
  if (PV) { OY = MFMA32(VF1, ABC(PYb), OY); PIN(OY); } SBAR(); \
  AC(PXb, SX, 4); AC(PXb, SX, 5); AE(SX, 12); AE(SX, 13); AE(SX, 14); PIN(SX); PIN(PXb); SBAR(); \
  if (PV) { LY = MFMA16(ones, ABC(PYb), LY); PIN(LY); } SBAR(); \
  AE(SX, 15); AC(PXb, SX, 6); AC(PXb, SX, 7); PIN(PXb); SBAR(); \
} while (0)

template <bool MASKED>
DI void attn_exact(const char* __restrict__ kv, int t0, int t1, int qt, const bf16x8& q0, const bf16x8& q1, const bf16x8& ones,
                   f32x16& o, float& l) {
  const int lane = threadIdx.x & 63, r = lane & 31, h = lane >> 5;
  float m = -3.0e38f;
  for (int t = t0; t < t1; ++t) {
    const char* p = kv + (size_t)t * 4096 + lane * 16;
    bf16x8 k0 = *(const bf16x8*)p, k1 = *(const bf16x8*)(p + 1024);
    f32x16 s = {};
    s = MFMA32(k0, q0, s); s = MFMA32(k1, q1, s);
#pragma unroll
    for (int i = 0; i < 16; ++i) {
      bool ok = true;
      if (MASKED) { const int c = crow(i, h); ok = (t == qt) ? (c <= r) : ((t == qt - 8) ? (c > r) : true); }
      m = fmaxf(m, ok ? s[i] : -3.0e38f);
    }
  }
  m = fmaxf(m, swap_half(m));
  o = (f32x16){};
  f32x4 la = {};
  for (int t = t0; t < t1; ++t) {
    const char* p = kv + (size_t)t * 4096 + lane * 16;
    bf16x8 k0 = *(const bf16x8*)p, k1 = *(const bf16x8*)(p + 1024);
    bf16x8 v0 = *(const bf16x8*)(p + 2048), v1 = *(const bf16x8*)(p + 3072);
    f32x16 s = {};
    s = MFMA32(k0, q0, s); s = MFMA32(k1, q1, s);
#pragma unroll
    for (int i = 0; i < 16; ++i) {
      bool ok = true;
      if (MASKED) { const int c = crow(i, h); ok = (t == qt) ? (c <= r) : ((t == qt - 8) ? (c > r) : true); }
      s[i] = ok ? __builtin_amdgcn_exp2f(s[i] - m) : 0.f;
    }
    bf16x8 pa = pack8(s, 0), pb = pack8(s, 8);
    o = MFMA32(v0, pa, o); o = MFMA32(v1, pb, o);
    la = MFMA16(ones, pa, la); la = MFMA16(ones, pb, la);
  }
  l = la[0];
}
DI bool ref_unsafe(const f32x16& o, float l) {
  float z = 0.f;
#pragma unroll
  for (int i = 0; i < 16; ++i) z = __builtin_fmaf(o[i], 0.f, z);
  const bool ok = (l >= 5.421010862e-20f) && (l <= 1.8446744e19f) && (z == 0.f);
  return __any(!ok);
}

__global__ __launch_bounds__(512, 2) void k_rest(const char* __restrict__ woa, const char* __restrict__ wob, char* __restrict__ ws,
                                                 char* __restrict__ out) {
  extern __shared__ __attribute__((aligned(16))) char lds[];
  STAMP_DECL STAMP(0); TRACE_DECL
  const int b = blockIdx.x, tid = threadIdx.x, lane = tid & 63;
  const int w = rfl(tid >> 6);
  const int r = lane & 31, h = lane >> 5;
  const unsigned lds0 = (unsigned)(uintptr_t)lds;
  const unsigned lo16 = lane * 16;
  const int qt = b >> 2, pr = w & 3, half = w >> 2;
  const int hA = 8 * (b & 3) + 2 * pr;
  const int hB = (b & 7) + 8 * (b >> 6), rb = (b >> 3) & 7;
  const int qtB = rb * 8 + w;
  const int xcd = b & 7, idx = b >> 3;
  const int mb = (xcd & 3) * 4 + (idx & 3), nb = (xcd >> 2) * 8 + (idx >> 2);
  const char* kva = ws + WS_KVA;
  const char* kvb = ws + WS_KVB + (size_t)hB * 64 * 4096;
  char* cntA_mine = ws + WS_CNT + (qt >> 2) * 256;
  char* cntB_mine = ws + WS_CNT + (16 + rb) * 256;
  char* cntA_need = ws + WS_CNT + mb * 256;
  char* cntB_need = ws + WS_CNT + (16 + (mb >> 1)) * 256;
  char* tmo = ws + WS_CNT + 24 * 256;
  bf16x8 ones;
  {
    const short v = ((((lane >> 4) & 1) == (((lane & 15) >> 2) & 1)) ? (short)0x3f80 : (short)0);
    ones = (bf16x8){v, v, v, v, v, v, v, v};
  }
  auto pieceA = [&](int k, int sl, int i) {
    const char* src = kva + (size_t)(half * 32 + 4 * k + pr) * 4096;
    const unsigned dst = lds0 + sl * SL + w * 4096;
    if (i == 0) glds16<0>(lo16, src, dst);
    else if (i == 1) glds16<1024>(lo16, src, dst);
    else if (i == 2) glds16<2048>(lo16, src, dst);
    else glds16<3072>(lo16, src, dst);
  };
  bf16x8 qb[2], q[2][2];
  float gA, gB;
  {
    const char* pa = ws + WS_QA + (size_t)(qt * 32 + hA) * 2048;
    gload16<0>(q[0][0], lo16, pa); gload16<1024>(q[0][1], lo16, pa);
    gload16<2048>(q[1][0], lo16, pa); gload16<3072>(q[1][1], lo16, pa);
    gload4(gA, r * 4, ws + WS_G + qt * 128);
  }
#pragma unroll
  for (int i = 0; i < 4; ++i) pieceA(0, 2, i);
  auto pieceB = [&](int n) {
    int gt = rb * 8 - 8 + 2 * w + (n >> 2);
    gt = gt < 0 ? 0 : gt;
    const char* src = kvb + (size_t)gt * 4096;
    const unsigned dst = lds0 + (2 * w + (n >> 2)) * 4096;
    if ((n & 3) == 0) glds16<0>(lo16, src, dst);
    else if ((n & 3) == 1) glds16<1024>(lo16, src, dst);
    else if ((n & 3) == 2) glds16<2048>(lo16, src, dst);
    else glds16<3072>(lo16, src, dst);
  };
  const char* pqb = ws + WS_QB + (size_t)(hB * 64 + qtB) * 2048;
  unsigned wo[2];
#pragma unroll
  for (int e = 0; e < 2; ++e) {
    const int row = 16 * w + 8 * e + (lane >> 3);
    const int c = (lane & 7) ^ (((row >> 1) & 1) << 2);
    wo[e] = row * 2048 + c * 16;
  }
  auto pieceW = [&](int st, int e) {
    const char* src = (st < 8 ? wob : woa) + (size_t)(st & 7) * 128 * 2048 + nb * 128;
    glds16<0>(wo[e], src, lds0 + WO_OFF + (st % 6) * 16384 + w * 2048 + e * 1024);
  };
  const f32x16 zero = {};
  f32x16 o0 = {}, o1 = {}, s0, s1;
  f32x4 l0 = {}, l1 = {};
  u32x4 p0a, p0b, p1a, p1b;
  bf16x8 ka0, ka1, kb0, kb1, va0, va1, vb0, vb1;
  lds_cptr kvbase = (lds_cptr)lds + half * 16384 + lane * 16;
  unsigned seenB = 0;
#define APAIR(pp, SL0, SL1, FIRST, LAST, PA, PB, MID, D0, D1, D2, D3) do { \
    lds_cptr cur = kvbase + (SL0) * SL + ((pp) & 1) * 8192; PIN(cur);     \
    lds_cptr nxt = (SL0) == (SL1) && !((pp) & 1) ? cur + 8192 : kvbase + (SL1) * SL; \
    __builtin_amdgcn_s_setprio(PA); \
    if (FIRST) { ka0 = lds_rd128(cur); ka1 = lds_rd128(cur + 1024); s0 = MFMA32(ka0, q[0][0], zero); s0 = MFMA32(ka1, q[0][1], s0); } \
    SBAR(); \
    APHASE(1, !(FIRST), s0, p0a, p0b, s1, zero, p1a, p1b, q[1][0], q[1][1], o1, l1, ka0, ka1, vb0, vb1, \
           (kb0 = lds_rd128(cur + 4096), kb1 = lds_rd128(cur + 5120)), (va0 = lds_rd128(cur + 2048), va1 = lds_rd128(cur + 3072))); \
    D0; SBAR(); \
    APHASE(1, 1, s1, p1a, p1b, s0, zero, p0a, p0b, q[0][0], q[0][1], o0, l0, kb0, kb1, va0, va1, (void)0, (void)0); \
    D1; SBAR(); \
    MID; \
    __builtin_amdgcn_s_setprio(PB); SBAR(); \
    APHASE(1, 1, s0, p0a, p0b, s1, zero, p1a, p1b, q[1][0], q[1][1], o1, l1, kb0, kb1, va0, va1, \
           if (!(LAST)) (ka0 = lds_rd128(nxt), ka1 = lds_rd128(nxt + 1024)), (vb0 = lds_rd128(cur + 4096 + 2048), vb1 = lds_rd128(cur + 4096 + 3072))); \
    D2; SBAR(); \
    APHASE(!(LAST), 1, s1, p1a, p1b, s0, zero, p0a, p0b, q[0][0], q[0][1], o0, l0, ka0, ka1, vb0, vb1, (void)0, (void)0); \
    D3; SBAR(); \
    if (LAST) { \
      o1 = MFMA32(vb0, ABC(p1a), o1); o1 = MFMA32(vb1, ABC(p1b), o1); \
      l1 = MFMA16(ones, ABC(p1a), l1); l1 = MFMA16(ones, ABC(p1b), l1); \
    } \
  } while (0)
#define NOP (void)0
#define BAR_V0 asm volatile("s_waitcnt vmcnt(0)\n\ts_barrier" ::: "memory")
#define DA(k, sl, i) pieceA(k, sl, i)
#define DB(n) pieceB(n)
#define DW(st, e) pieceW(st, e)
#define BAR_V(n) asm volatile("s_waitcnt vmcnt(" #n ")\n\ts_barrier" ::: "memory")
  asm volatile("s_waitcnt vmcnt(0)\n\ts_barrier" : "+v"(q[0][0]), "+v"(q[0][1]), "+v"(q[1][0]), "+v"(q[1][1]), "+v"(gA)::"memory");
  STAMP(1);
  APAIR(0, 2, 2, 1, 0, 3, 2, NOP, DA(1, 3, 0), DA(1, 3, 1), DA(1, 3, 2), DA(1, 3, 3));
  APAIR(1, 2, 3, 0, 0, 0, 3, BAR_V(2), DA(2, 4, 0), DA(2, 4, 1), DA(2, 4, 2), DA(2, 4, 3));
  APAIR(2, 3, 3, 0, 0, 2, 1, NOP, DB(0), DB(1), DB(2), DB(3));
  APAIR(3, 3, 4, 0, 0, 0, 3, BAR_V(6), DB(4), DB(5), DB(6), DB(7));
  APAIR(4, 4, 4, 0, 0, 2, 1, NOP, gload16<0>(qb[0], lo16, pqb), gload16<1024>(qb[1], lo16, pqb), gload4(gB, r * 4, ws + WS_G + qtB * 128), NOP);
  APAIR(5, 4, 4, 0, 1, 1, 0, NOP, NOP, NOP, NOP, NOP);
  asm volatile("s_waitcnt vmcnt(0) lgkmcnt(0)\n\ts_barrier" : "+v"(qb[0]), "+v"(qb[1]), "+v"(gB)::"memory");
  STAMP(2);
  {
    f32x16 o = {};
    f32x4 la = {};
    lds_cptr bb = (lds_cptr)lds + w * 4096 + lane * 16;
    f32x16 mD;
#pragma unroll
    for (int i = 0; i < 16; ++i) mD[i] = crow(i, h) <= r ? 0.f : -1.0e30f;
    if (rb != 0) {
      f32x16 sa, sb;
#pragma unroll
      for (int i = 0; i < 16; ++i) sa[i] = -1.0e30f - mD[i];
      u32x4 pa0 = {}, pa1 = {}, pb0 = {}, pb1 = {};
      bf16x8 k0, k1, k2, k3, v0, v1, v2, v3;
      k0 = lds_rd128(bb); k1 = lds_rd128(bb + 1024);
      k2 = lds_rd128(bb + 4096); k3 = lds_rd128(bb + 4096 + 1024);
      sa = MFMA32(k0, qb[0], sa); sa = MFMA32(k1, qb[1], sa);
      SBAR();
#define BRDK(KA, KB, j) (KA = lds_rd128(bb + (j) * 4096), KB = lds_rd128(bb + (j) * 4096 + 1024))
#define BRDV(VA, VB, j) (VA = lds_rd128(bb + (j) * 4096 + 2048), VB = lds_rd128(bb + (j) * 4096 + 3072))
#define B_DMA(n) do { pieceA(3 + (n) / 4, 2 + (n) / 4, (n) & 3); SBAR(); } while (0)
      __builtin_amdgcn_s_setprio(3);
      APHASE(1, 0, sa, pa0, pa1, sb, zero, pb0, pb1, qb[0], qb[1], o, la, k2, k3, v2, v3, BRDK(k0, k1, 2), BRDV(v0, v1, 0));
      B_DMA(0); B_DMA(1);
      APHASE(1, 1, sb, pb0, pb1, sa, zero, pa0, pa1, qb[0], qb[1], o, la, k0, k1, v0, v1, BRDK(k2, k3, 3), BRDV(v2, v3, 1));
      B_DMA(2); B_DMA(3);
      __builtin_amdgcn_s_setprio(2);
      APHASE(1, 1, sa, pa0, pa1, sb, zero, pb0, pb1, qb[0], qb[1], o, la, k2, k3, v2, v3, BRDK(k0, k1, 4), BRDV(v0, v1, 2));
      B_DMA(4); B_DMA(5);
      APHASE(1, 1, sb, pb0, pb1, sa, zero, pa0, pa1, qb[0], qb[1], o, la, k0, k1, v0, v1, BRDK(k2, k3, 5), BRDV(v2, v3, 3));
      B_DMA(6); B_DMA(7);
      __builtin_amdgcn_s_setprio(1);
      APHASE(1, 1, sa, pa0, pa1, sb, zero, pb0, pb1, qb[0], qb[1], o, la, k2, k3, v2, v3, BRDK(k0, k1, 6), BRDV(v0, v1, 4));
      B_DMA(8); B_DMA(9);
      APHASE(1, 1, sb, pb0, pb1, sa, zero, pa0, pa1, qb[0], qb[1], o, la, k0, k1, v0, v1, BRDK(k2, k3, 7), BRDV(v2, v3, 5));
      B_DMA(10); B_DMA(11);
      __builtin_amdgcn_s_setprio(0);
      APHASE(1, 1, sa, pa0, pa1, sb, zero, pb0, pb1, qb[0], qb[1], o, la, k2, k3, v2, v3, BRDK(k0, k1, 8), BRDV(v0, v1, 6));
      APHASE(1, 1, sb, pb0, pb1, sa, mD, pa0, pa1, qb[0], qb[1], o, la, k0, k1, v0, v1, (void)0, BRDV(v2, v3, 7));
      APHASE(0, 1, sa, pa0, pa1, sb, zero, pb0, pb1, qb[0], qb[1], o, la, k2, k3, v2, v3, (void)0, BRDV(v0, v1, 8));
      o = MFMA32(v0, ABC(pa0), o); o = MFMA32(v1, ABC(pa1), o);
      la = MFMA16(ones, ABC(pa0), la); la = MFMA16(ones, ABC(pa1), la);
    } else {
#pragma unroll
      for (int n = 0; n < 12; ++n) B_DMA(n);
      auto tile = [&](int j, const f32x16& cin) {
        lds_cptr p = bb + j * 4096;
        bf16x8 k0 = lds_rd128(p), k1 = lds_rd128(p + 1024), v0 = lds_rd128(p + 2048), v1 = lds_rd128(p + 3072);
        f32x16 s = MFMA32(k0, qb[0], cin);
        s = MFMA32(k1, qb[1], s);
        exp16(s);
        bf16x8 pa = pack8(s, 0), pb = pack8(s, 8);
        o = MFMA32(v0, pa, o); o = MFMA32(v1, pb, o);
        la = MFMA16(ones, pa, la); la = MFMA16(ones, pb, la);
      };
      for (int j = 8 - w; j < 8; ++j) tile(j, zero);
      tile(8, mD);
    }
    float l = la[0];
    if (__builtin_expect(ref_unsafe(o, l), 0)) attn_exact<true>(kvb, qtB >= 8 ? qtB - 8 : 0, qtB + 1, qtB, qb[0], qb[1], ones, o, l);
    const float sc = (1.f - gB) * __builtin_amdgcn_rcpf(l);
    char* dst = ws + WS_O + (size_t)(qtB * 64 + 32 + hB) * 2048;
    gstore16_sc1<0>(lo16, dst, pack8s(o, 0, sc));
    gstore16_sc1<1024>(lo16, dst, pack8s(o, 8, sc));
  }
  asm volatile("s_waitcnt vmcnt(2) lgkmcnt(0)\n\ts_barrier" ::: "memory");
  STAMP(3);
  const int wm = w & 3, kh = w >> 2;
  const char* obase = ws + WS_O + (size_t)(mb * 4 + wm) * 64 * 2048 + kh * 4096;
  bf16x8 of[5][4];
  auto loadO1 = [&](int st, int i, bf16x8& f) {
    const char* src = obase + (size_t)(st < 8 ? 32 + 4 * st : 4 * (st - 8)) * 2048;
    if (i == 0) gload16_sc1<0>(f, lo16, src);
    else if (i == 1) gload16_sc1<1024>(f, lo16, src);
    else if (i == 2) gload16_sc1<2048>(f, lo16, src);
    else gload16_sc1<3072>(f, lo16, src);
  };
#define LO(st, i) loadO1(st, i, of[st][i])
  auto loadO = [&](int st, bf16x8 (&f)[4]) {
    const char* src = obase + (size_t)(st < 8 ? 32 + 4 * st : 4 * (st - 8)) * 2048;
    gload16_sc1<0>(f[0], lo16, src); gload16_sc1<1024>(f[1], lo16, src);
    gload16_sc1<2048>(f[2], lo16, src); gload16_sc1<3072>(f[3], lo16, src);
  };
  APAIR(6, 2, 2, 1, 0, 3, 3, NOP, DA(6, 0, 0), DA(6, 0, 1), DA(6, 0, 2), DA(6, 0, 3));
  APAIR(7, 2, 3, 0, 0, 3, 2, NOP, DA(7, 1, 0), DA(7, 1, 1), DA(7, 1, 2), DA(7, 1, 3));
  unsigned warm = 0;
  {
    const int R = 768 + (idx & 3) * 320 + w * 40 + (lane < 40 ? lane : 39);
    const char* pw = (R < 1024 ? wob + (size_t)R * 2048 : woa + (size_t)(R - 1024) * 2048) + nb * 128;
    asm volatile("global_load_dword %0, %1, off" : "+v"(warm) : "v"(pw) : "memory");
  }
  APAIR(8, 3, 3, 0, 0, 2, 2, NOP, NOP, NOP, NOP, NOP);
  APAIR(9, 3, 4, 0, 0, 1, 1, NOP, NOP, NOP, NOP, NOP);
  APAIR(10, 4, 4, 0, 0, 1, 0, NOP, NOP, NOP, NOP, NOP);
  APAIR(11, 4, 0, 0, 0, 0, 3, { asm volatile("s_waitcnt vmcnt(0)\n\ts_barrier" : "+v"(warm)::"memory"); if (tid == 0) signal(cntB_mine);   }, NOP, NOP, NOP, NOP);
  APAIR(12, 0, 0, 0, 0, 3, 2, NOP, DW(0, 0), DW(0, 1), DW(1, 0), DW(1, 1));
  if (w == 0) poll_issue(seenB, cntB_need);
  APAIR(13, 0, 1, 0, 0, 2, 1, NOP, DW(2, 0), DW(2, 1), DW(3, 0), DW(3, 1));
  APAIR(14, 1, 1, 0, 0, 0, 3, { asm volatile("s_waitcnt vmcnt(0)" : "+v"(seenB)::"memory"); if (w == 0) poll_finish(seenB, cntB_need, tmo, 32u); asm volatile("s_barrier" ::: "memory"); },
        NOP, NOP, (LO(0, 0), LO(0, 1), DW(4, 0)), (LO(0, 2), LO(0, 3), DW(4, 1)));
  APAIR(15, 1, 1, 0, 1, 2, 1, NOP, (LO(1, 0), LO(1, 1), DW(5, 0)), (LO(1, 2), LO(1, 3), DW(5, 1)), (LO(2, 0), LO(2, 1)), (LO(2, 2), LO(2, 3)));
  STAMP(4);

  loadO(3, of[3]);
  asm volatile("s_waitcnt lgkmcnt(0)\n\ts_barrier" ::: "memory");
  {
    float* xb = (float*)(lds + XCH_OFF) + w * (5 * 256);
    const f32x16& os = half ? o0 : o1;
    const float ls = half ? l0[0] : l1[0];
#pragma unroll
    for (int g = 0; g < 4; ++g) *(f32x4*)(xb + g * 256 + lane * 4) = (f32x4){os[4 * g], os[4 * g + 1], os[4 * g + 2], os[4 * g + 3]};
    xb[4 * 256 + lane] = ls;
  }
  asm volatile("s_waitcnt lgkmcnt(0)\n\ts_barrier" ::: "memory");
  {
    const float* xb = (const float*)(lds + XCH_OFF) + (w ^ 4) * (5 * 256);
    f32x16 o = half ? o1 : o0;
    float l = half ? l1[0] : l0[0];
#pragma unroll
    for (int g = 0; g < 4; ++g) {
      f32x4 v = *(const f32x4*)(xb + g * 256 + lane * 4);
#pragma unroll
      for (int e = 0; e < 4; ++e) o[4 * g + e] += v[e];
    }
    l += xb[4 * 256 + lane];
    if (__builtin_expect(ref_unsafe(o, l), 0)) {
      if (half) attn_exact<false>(kva, 0, 64, 0, q[1][0], q[1][1], ones, o, l);
      else attn_exact<false>(kva, 0, 64, 0, q[0][0], q[0][1], ones, o, l);
    }
    const float sc = gA * __builtin_amdgcn_rcpf(l);
    char* dst = ws + WS_O + (size_t)(qt * 64 + hA + half) * 2048;
    gstore16_sc1<0>(lo16, dst, pack8s(o, 0, sc));
    gstore16_sc1<1024>(lo16, dst, pack8s(o, 8, sc));
  }
  STAMP(5);
  {
    const int g16 = lane >> 4, q4 = (lane & 15) >> 2, p4 = lane & 3;
    const int wc = 2 * (g16 & 1) + (p4 >> 1);
    const int wa0 = (kh * 64 + 4 * h + q4) * 128 + ((wc ^ (((q4 >> 1) & 1) << 2)) << 4) + 8 * (p4 & 1);
    lds_cptr wbase[2][2] = {{(lds_cptr)lds + WO_OFF + wa0, (lds_cptr)lds + WO_OFF + (wa0 ^ 64)},
                            {(lds_cptr)lds + WO_OFF + 49152 + wa0, (lds_cptr)lds + WO_OFF + 49152 + (wa0 ^ 64)}};
    PIN(wbase[0][0]); PIN(wbase[0][1]); PIN(wbase[1][0]); PIN(wbase[1][1]);
    f32x16 acc[2] = {};
    bf16x8 wf[4][2];
    unsigned seenA = 0;
    auto rdw = [&](bf16x8 (&f)[2], int st, int ks) {
#pragma unroll
      for (int j = 0; j < 2; ++j) {
        lds_cptr p = wbase[(st % 6) / 3][j] + ((st % 6) % 3) * 16384 + ks * 2048;
        f[j] = cat8(lds_tr(p), lds_tr(p + 1024));
      }
    };
    asm volatile("s_waitcnt vmcnt(17)" : "+v"(of[0][0]), "+v"(of[0][1]), "+v"(of[0][2]), "+v"(of[0][3])::"memory");
    rdw(wf[0], 0, 0); rdw(wf[1], 0, 1);
#define OTRACE(i) do {} while (0)
#define O_MM(f, a) do { acc[0] = MFMA32(f[0], a, acc[0]); SBAR(); acc[1] = MFMA32(f[1], a, acc[1]); SBAR(); } while (0)
#define O_STAGE(st, VM, PRE, POST) do { \
      rdw(wf[2], st, 2); SBAR(); O_MM(wf[0], of[(st) % 5][0]); \
      rdw(wf[3], st, 3); SBAR(); O_MM(wf[1], of[(st) % 5][1]); \
      if ((st) < 15) { \
        OTRACE(3 * (st)); \
        asm volatile("s_waitcnt vmcnt(" #VM ")" : "+v"(of[((st) + 1) % 5][0]), "+v"(of[((st) + 1) % 5][1]), \
                     "+v"(of[((st) + 1) % 5][2]), "+v"(of[((st) + 1) % 5][3]), "+v"(seenA)::"memory"); \
        PRE; \
        OTRACE(3 * (st) + 1); \
        asm volatile("s_barrier" ::: "memory"); \
        OTRACE(3 * (st) + 2); \
        POST; \
        SBAR(); \
        rdw(wf[0], (st) + 1, 0); \
      } \
      SBAR(); O_MM(wf[2], of[(st) % 5][2]); \
      if ((st) < 15) rdw(wf[1], (st) + 1, 1); \
      SBAR(); O_MM(wf[3], of[(st) % 5][3]); \
    } while (0)
#define OW(st) (pieceW(st, 0), pieceW(st, 1))
#define OL(st) loadO(st, of[(st) % 5])
    O_STAGE(0, 11, NOP, OL(4));
    O_STAGE(1, 4, NOP, { if (tid == 0) signal(cntA_mine);   OW(6); OL(5); });
    O_STAGE(2, 10, NOP, (OW(7), OL(6)));
    O_STAGE(3, 12, NOP, { if (w == 0) poll_issue(seenA, cntA_need); OW(8); OL(7); });
    O_STAGE(4, 12, NOP, OW(9));
    O_STAGE(5, 8, if (w == 0) poll_finish(seenA, cntA_need, tmo, 16u), (OW(10), OL(8), OL(9)));
    O_STAGE(6, 12, NOP, (OW(11), OL(10)));
    O_STAGE(7, 10, NOP, (OW(12), OL(11)));
    O_STAGE(8, 12, NOP, (OW(13), OL(12)));
    O_STAGE(9, 12, NOP, (OW(14), OL(13)));
    O_STAGE(10, 12, NOP, (OW(15), OL(14)));
    O_STAGE(11, 12, NOP, OL(15));
    O_STAGE(12, 10, NOP, NOP);
    O_STAGE(13, 4, NOP, NOP);
    O_STAGE(14, 0, NOP, NOP);
    O_STAGE(15, 0, NOP, NOP);
    STAMP(6);
    float* xw = (float*)(lds + XCH_OFF) + w * 1024 + lane * 4;
    const f32x16& as = kh ? acc[0] : acc[1];
#pragma unroll
    for (int g = 0; g < 4; ++g) *(f32x4*)(xw + g * 256) = (f32x4){as[4 * g], as[4 * g + 1], as[4 * g + 2], as[4 * g + 3]};
    asm volatile("s_waitcnt lgkmcnt(0)\n\ts_barrier" ::: "memory");
    f32x16 t = kh ? acc[1] : acc[0];
    const float* xr = (const float*)(lds + XCH_OFF) + (w ^ 4) * 1024 + lane * 4;
#pragma unroll
    for (int g = 0; g < 4; ++g) {
      f32x4 v = *(const f32x4*)(xr + g * 256);
#pragma unroll
      for (int e = 0; e < 4; ++e) t[4 * g + e] += v[e];
    }
    const int row = mb * 128 + wm * 32 + r;
    char* dst = out + (size_t)row * 2048 + (nb * 64 + kh * 32) * 2;
#pragma unroll
    for (int g = 0; g < 4; g += 2) {
      u32x2 a = {cvtpk(t[4 * g], t[4 * g + 1]), cvtpk(t[4 * g + 2], t[4 * g + 3])};
      u32x2 c = {cvtpk(t[4 * g + 4], t[4 * g + 5]), cvtpk(t[4 * g + 6], t[4 * g + 7])};
      auto x = __builtin_amdgcn_permlane32_swap(a[0], c[0], false, false);
      auto y = __builtin_amdgcn_permlane32_swap(a[1], c[1], false, false);
      *(u32x4*)(dst + g * 16 + h * 16) = (u32x4){x[0], y[0], x[1], y[1]};
    }
  }
  STAMP(7);
  STAMP_OUT(1, ws); TRACE_OUT(ws);
}

extern "C" void kernel_launch(void* const* d_in, const int* in_sizes, int n_in, void* d_out, int out_size, void* d_ws,
                              size_t ws_size, hipStream_t stream) {
  const char* xa = (const char*)d_in[0];
  const char* wq = (const char*)d_in[1];
  const char* wk = (const char*)d_in[2];
  const char* wv = (const char*)d_in[3];
  const char* woa = (const char*)d_in[4];
  const char* wqkv = (const char*)d_in[6];
  const char* wob = (const char*)d_in[7];
  const char* gw = (const char*)d_in[8];
  char* ws = (char*)d_ws;
  (void)hipFuncSetAttribute((const void*)k_proj, hipFuncAttributeMaxDynamicSharedMemorySize, P_LDS);
  (void)hipFuncSetAttribute((const void*)k_rest, hipFuncAttributeMaxDynamicSharedMemorySize, R_LDS);
  k_proj<<<256, 512, P_LDS, stream>>>(xa, wq, wk, wv, wqkv, gw, ws);
  k_rest<<<256, 512, R_LDS, stream>>>(woa, wob, ws, (char*)d_out);
}
```

```cpp
#include <hip/hip_runtime.h>
#include <hip/hip_bf16.h>
#include <stdint.h>

typedef float f32x16 __attribute__((ext_vector_type(16)));
typedef float f32x4 __attribute__((ext_vector_type(4)));
typedef float f32x2 __attribute__((ext_vector_type(2)));
typedef short bf16x8 __attribute__((ext_vector_type(8)));
typedef short s16x4 __attribute__((ext_vector_type(4)));
typedef __bf16 bf16x2 __attribute__((ext_vector_type(2)));
typedef unsigned u32x4 __attribute__((ext_vector_type(4)));
typedef unsigned u32x2 __attribute__((ext_vector_type(2)));
typedef __attribute__((address_space(3))) const char* lds_cptr;
typedef __attribute__((address_space(3))) char* lds_ptr;

#define DI __device__ __forceinline__
#define SBAR() __builtin_amdgcn_sched_barrier(0)
#define PIN(x) asm volatile("" : "+v"(x))
#define MFMA32(a, b, c) __builtin_amdgcn_mfma_f32_32x32x16_bf16(a, b, c, 0, 0, 0)
#define MFMA16(a, b, c) __builtin_amdgcn_mfma_f32_16x16x32_bf16(a, b, c, 0, 0, 0)

constexpr int SEQ = 2048, DM = 1024;
constexpr float QSCALE = 0.17677669529663687f * 1.4426950408889634f;

constexpr size_t WS_KVA = 4u << 20;
constexpr size_t WS_G = WS_KVA + (256u << 10);
constexpr size_t WS_NONCE = WS_G + 8192;
constexpr size_t WS_FKV = WS_NONCE + 1024;
constexpr size_t WS_FPB = WS_FKV + 1024;
constexpr size_t WS_FOA = WS_FPB + 2048;
constexpr size_t WS_FOB = WS_FOA + 2048;
constexpr size_t WS_QB = 5u << 20;
constexpr size_t WS_KVB = 9u << 20;
constexpr size_t WS_O = 17u << 20;

#define TRACE(i) do {} while (0)
#define TRACE_DECL
#define TRACE_OUT(wsp) do {} while (0)
#define STAMP_ARG
#define STAMP_PASS
#define STAMP_DECL
#define STAMP(i) do {} while (0)
#define STAMP_OUT(k, wsp) do {} while (0)
DI unsigned rfl(unsigned x) { return __builtin_amdgcn_readfirstlane(x); }
DI unsigned cvtpk(float lo, float hi) {
  f32x2 v = {lo, hi};
  bf16x2 b = __builtin_convertvector(v, bf16x2);
  return __builtin_bit_cast(unsigned, b);
}
DI bf16x8 pack8(const f32x16& a, int o) {
  u32x4 r = {cvtpk(a[o], a[o + 1]), cvtpk(a[o + 2], a[o + 3]), cvtpk(a[o + 4], a[o + 5]), cvtpk(a[o + 6], a[o + 7])};
  return __builtin_bit_cast(bf16x8, r);
}
DI bf16x8 pack8s(const f32x16& a, int o, float s) {
  u32x4 r = {cvtpk(a[o] * s, a[o + 1] * s), cvtpk(a[o + 2] * s, a[o + 3] * s), cvtpk(a[o + 4] * s, a[o + 5] * s),
             cvtpk(a[o + 6] * s, a[o + 7] * s)};
  return __builtin_bit_cast(bf16x8, r);
}
template <int OFF>
DI void glds16(unsigned voff, const void* sbase, unsigned lds_base) {
  asm volatile("s_mov_b32 m0, %2\n\ts_nop 0\n\tglobal_load_lds_dwordx4 %0, %1 offset:%3"
               :: "v"(voff), "s"(sbase), "s"(rfl(lds_base)), "i"(OFF) : "memory");
}
template <int OFF>
DI void glds16_sc1(unsigned voff, const void* sbase, unsigned lds_base) {
  asm volatile("s_mov_b32 m0, %2\n\ts_nop 0\n\tglobal_load_lds_dwordx4 %0, %1 offset:%3 sc1"
               :: "v"(voff), "s"(sbase), "s"(rfl(lds_base)), "i"(OFF) : "memory");
}
DI bf16x8 lds_rd128(lds_cptr p) { return *(const __attribute__((address_space(3))) bf16x8*)p; }
DI s16x4 lds_tr(lds_cptr p) {
  typedef short v4i16_t __attribute__((ext_vector_type(4)));
  return __builtin_bit_cast(s16x4, __builtin_amdgcn_ds_read_tr16_b64_v4i16((__attribute__((address_space(3))) v4i16_t*)p));
}
DI bf16x8 cat8(s16x4 lo, s16x4 hi) { return (bf16x8){lo[0], lo[1], lo[2], lo[3], hi[0], hi[1], hi[2], hi[3]}; }
DI float bf2f(unsigned short b) { return __uint_as_float((unsigned)b << 16); }

template <int OFF>
DI void gload16_sc1(bf16x8& d, unsigned voff, const void* sbase) {
  asm volatile("global_load_dwordx4 %0, %1, %2 offset:%3 sc1" : "=v"(d) : "v"(voff), "s"(sbase), "i"(OFF) : "memory");
}
template <int OFF>
DI void gstore16_sc1(unsigned voff, void* sbase, const bf16x8& d) {
  asm volatile("global_store_dwordx4 %0, %1, %2 offset:%3 sc1\n\ts_nop 1" :: "v"(voff), "v"(d), "s"(sbase), "i"(OFF) : "memory");
}
DI void gload4_sc1(float& d, unsigned voff, const void* sbase) {
  asm volatile("global_load_dword %0, %1, %2 sc1" : "=v"(d) : "v"(voff), "s"(sbase) : "memory");
}
DI bf16x8 gload16_now(const char* p) {
  bf16x8 d;
  asm volatile("global_load_dwordx4 %0, %1, off sc1\n\ts_waitcnt vmcnt(0)" : "=&v"(d) : "v"(p) : "memory");
  return d;
}
typedef __attribute__((address_space(1))) unsigned long long gu64;
DI void flag_set(char* p, u32x2 tag) {
  asm volatile("global_store_dwordx2 %0, %1, %2 sc1\n\ts_nop 1" :: "v"(0u), "v"(tag), "s"(p) : "memory");
}
DI void flag_peek(u32x2& d, unsigned voff, const void* sbase) {
  asm volatile("global_load_dwordx2 %0, %1, %2 sc1" : "+v"(d) : "v"(voff), "s"(sbase) : "memory");
}
DI void flag_wait(u32x2 seen, unsigned voff, const char* sbase, u32x2 tag) {
  if (__builtin_expect(__all(seen[0] == tag[0] && seen[1] == tag[1]), 1)) return;
  const unsigned long long want = ((unsigned long long)tag[1] << 32) | tag[0];
  for (unsigned spins = 0; spins < (1u << 21); ++spins) {
    if (__all(__hip_atomic_load((gu64*)(sbase + voff), __ATOMIC_RELAXED, __HIP_MEMORY_SCOPE_AGENT) == want)) return;
    __builtin_amdgcn_s_sleep(4);
  }
}

DI void nonce_load(u32x2& nonce, const char* ws) {
  asm volatile("global_load_dwordx2 %0, %1, %2 sc1" : "=v"(nonce) : "v"(0u), "s"(ws + WS_NONCE) : "memory");
}
DI u32x2 nonce_tag(u32x2 nonce) {
  u32x2 tag;
  tag[0] = nonce[0] + 1u;
  tag[1] = nonce[1] + (tag[0] == 0u ? 1u : 0u);
  return tag;
}

constexpr int Q_OFF = 49152, Q_SLOT = 24576, QX_OFF = 32768;
struct QGemm {
  const char* xs; const char* wsrc; unsigned xo, wo[2], lds0; int w;
  DI void init(const char* xa, const char* wq, int qp, int hg, unsigned lds0_, int w_, int lane) {
    xs = xa + (size_t)qp * 64 * 2048; wsrc = wq + hg * 256; lds0 = lds0_; w = w_;
    const int row = 8 * w + (lane >> 3);
    xo = row * 2048 + (((lane & 7) ^ ((row >> 1) & 7)) << 4);
#pragma unroll
    for (int e = 0; e < 2; ++e) {
      const int krow = 8 * w + 4 * e + (lane >> 4);
      wo[e] = krow * 2048 + (((lane & 15) ^ ((krow & 3) << 2)) << 4);
    }
  }
  DI void piece(int st, int n) const {
    const unsigned lb = lds0 + Q_OFF + (st & 3) * Q_SLOT;
    if (n == 0) glds16<0>(xo, xs + st * 128, lb + w * 1024);
    else glds16<0>(wo[n - 1], wsrc + (size_t)st * 64 * 2048, lb + 8192 + (2 * w + n - 1) * 1024);
  }
  DI void stage(int st) const { piece(st, 0); piece(st, 1); piece(st, 2); }
};

constexpr int P_SLOT = 49152;
constexpr int P_XOFF = 0, P_WOFF = 16384;
struct PFrag { bf16x8 x[2]; bf16x8 w[2]; };

template <bool VT>
DI void proj_b(const char* __restrict__ xg, const char* __restrict__ wg, const char* __restrict__ ws, int mb, int nb, char* lds,
               const QGemm& qg, u32x2& tag, bf16x8 (&held)[8] STAMP_ARG) {
  constexpr int ldw_bytes = 6144;
  const int tid = threadIdx.x, lane = tid & 63;
  const int w = rfl(tid >> 6);
  const int wm = w & 1, wn = w >> 1;
  const int r = lane & 31, h = lane >> 5;
  const unsigned lds0 = (unsigned)(uintptr_t)lds;
  u32x2 nonce;
  nonce_load(nonce, ws);
  unsigned xo[2], wo[4];
#pragma unroll
  for (int e = 0; e < 2; ++e) {
    const int row = 16 * w + 8 * e + (lane >> 3);
    const int c = (lane & 7) ^ (4 * e + (lane >> 4));
    xo[e] = row * 2048 + c * 16;
  }
#pragma unroll
  for (int e = 0; e < 4; ++e) {
    const int krow = 8 * w + 2 * e + (lane >> 5);
    const int c = (lane & 31) ^ ((krow & 3) << 2);
    wo[e] = krow * ldw_bytes + c * 16;
  }
  const char* xsrc = xg + (size_t)mb * 128 * 2048;
  auto piece = [&](int pc, int st, int slot) {
    const unsigned lb = lds0 + slot * P_SLOT;
    if (pc < 2) glds16<0>(xo[pc], xsrc + st * 128, lb + P_XOFF + w * 2048 + pc * 1024);
    else glds16<0>(wo[pc - 2], wg + (size_t)st * 64 * ldw_bytes, lb + P_WOFF + w * 4096 + (pc - 2) * 1024);
  };
  auto dma = [&](int st, int slot) {
#pragma unroll
    for (int pc = 0; pc < 6; ++pc) piece(pc, st, slot);
  };
  auto win = [&](int j, int pc, int slot) {
    if (j < 0) return;
    if (j <= 12) piece(pc, j + 3, slot);
    else if (j <= 14) qg.piece((j - 13) * 2 + pc / 3, pc % 3);
  };
  const int t3 = (r >> 1) & 7;
  const int xa0 = P_XOFF + (wm * 64 + r) * 128 + ((h ^ t3) << 4);
  const int g16 = lane >> 4, q4 = (lane & 15) >> 2, p4 = lane & 3;
  const int wc = wn * 8 + 2 * (g16 & 1) + (p4 >> 1);
  const int wa0 = P_WOFF + (8 * h + q4) * 512 + ((wc ^ (q4 << 2)) << 4) + 8 * (p4 & 1);

  auto rdx = [&](PFrag& f, int i, int slot, int s) { f.x[i] = lds_rd128((lds_cptr)lds + slot * P_SLOT + (xa0 ^ (s << 5)) + i * 4096); };
  auto rdw = [&](PFrag& f, int j, int slot, int s) {
    lds_cptr p = (lds_cptr)lds + slot * P_SLOT + (wa0 ^ (j << 6)) + s * 8192;
    f.w[j] = cat8(lds_tr(p), lds_tr(p + 2048));
  };
  f32x16 acc[2][2] = {};
  auto mm = [&](const PFrag& f, int i, int j) {
    if constexpr (VT) acc[i][j] = MFMA32(f.x[i], f.w[j], acc[i][j]);
    else acc[i][j] = MFMA32(f.w[j], f.x[i], acc[i][j]);
  };
  auto step = [&](const PFrag& c, PFrag& n, int rslot, int rs, bool rd, int pa, int wj, int dslot) {
    mm(c, 0, 0); SBAR();
    if (rd) { rdw(n, 0, rslot, rs); rdx(n, 0, rslot, rs); }
    SBAR();
    mm(c, 0, 1); SBAR();
    if (rd) { rdw(n, 1, rslot, rs); rdx(n, 1, rslot, rs); }
    SBAR();
    mm(c, 1, 0); SBAR();
    if (pa >= 0) win(wj, pa, dslot);
    SBAR();
    mm(c, 1, 1); SBAR();
    if (pa >= 0) win(wj, pa + 1, dslot);
    SBAR();
  };

  dma(0, 0);
  asm volatile("s_barrier" ::: "memory");
  dma(1, 1); dma(2, 2);
  asm volatile("s_waitcnt vmcnt(12)\n\ts_barrier" : "+v"(nonce)::"memory");
  tag = nonce_tag(nonce);
  STAMP(1);
  PFrag f0, f1;
  rdw(f0, 0, 0, 0); rdx(f0, 0, 0, 0); rdw(f0, 1, 0, 0); rdx(f0, 1, 0, 0);
  int slot = 0, pslot = 0;
  for (int it = 0; it < 16; ++it) {
    const int nslot = slot == 2 ? 0 : slot + 1;
    step(f0, f1, slot, 1, true, 2, it - 1, pslot);
    step(f1, f0, slot, 2, true, 4, it - 1, pslot);
    step(f0, f1, slot, 3, true, -1, 0, 0);
    if (it < 15) asm volatile("s_waitcnt vmcnt(6) lgkmcnt(0)\n\ts_barrier" ::: "memory");
    else asm volatile("s_waitcnt lgkmcnt(0)\n\ts_barrier" ::: "memory");
    SBAR();
    step(f1, f0, nslot, 0, it < 15, 0, it, slot);
    pslot = slot;
    slot = nslot;
  }
  STAMP(2);
  const float sc = (!VT && nb < 4) ? QSCALE : 1.f;
#pragma unroll
  for (int i = 0; i < 2; ++i)
#pragma unroll
    for (int j = 0; j < 2; ++j) {
      held[(i * 2 + j) * 2] = pack8s(acc[i][j], 0, sc);
      held[(i * 2 + j) * 2 + 1] = pack8s(acc[i][j], 8, sc);
    }
}

DI void proj_kv(const char* __restrict__ xg, const char* __restrict__ wk, const char* __restrict__ wv, const char* __restrict__ gw,
                char* __restrict__ ws, int rt, char* lds, const QGemm& qg, u32x2& tag STAMP_ARG) {
  const int tid = threadIdx.x, lane = tid & 63;
  const int w = rfl(tid >> 6);
  const int r = lane & 31, h = lane >> 5;
  const unsigned lds0 = (unsigned)(uintptr_t)lds;
  u32x2 nonce;
  nonce_load(nonce, ws);
  const int xrow = 4 * w + (lane >> 4);
  const unsigned xo = xrow * 2048 + (((lane & 15) ^ (xrow & 15)) << 4), wo = w * 1024 + lane * 16;
  const char* xs = xg + (size_t)rt * 32 * 2048;
  auto stage = [&](int st) {
    const unsigned lb = lds0 + (st % 6) * 24576 + w * 1024;
    glds16<0>(xo, xs + st * 256, lb);
    glds16<0>(wo, wk + st * 8192, lb + 8192);
    glds16<0>(wo, wv + st * 8192, lb + 16384);
  };
#pragma unroll
  for (int st = 0; st < 6; ++st) stage(st);
  const int t = w & 1, kq = w >> 1;
  const int g16 = lane >> 4, q4 = (lane & 15) >> 2, p4 = lane & 3;
  const int xa = r * 256, wa = 8192 + t * 8192 + (32 * kq + 8 * h + q4) * 64 + 32 * (g16 & 1) + 8 * p4;
  f32x16 acc = {};
#define KV_STAGE(st, VM) do { \
    asm volatile("s_waitcnt vmcnt(" #VM ") lgkmcnt(0)\n\ts_barrier" : "+v"(nonce)::"memory"); \
    if ((st) >= 1 && (st) + 5 < 8) stage((st) + 5); \
    lds_cptr b = (lds_cptr)lds + ((st) % 6) * 24576; \
    _Pragma("unroll") for (int e = 0; e < 2; ++e) { \
      const bf16x8 xf = lds_rd128(b + xa + (((4 * kq + 2 * e + h) ^ (r & 15)) << 4)); \
      const bf16x8 wf = cat8(lds_tr(b + wa + e * 1024), lds_tr(b + wa + e * 1024 + 256)); \
      if (t) acc = MFMA32(xf, wf, acc); else acc = MFMA32(wf, xf, acc); \
    } \
  } while (0)
  KV_STAGE(0, 15); KV_STAGE(1, 12); KV_STAGE(2, 12); KV_STAGE(3, 12); KV_STAGE(4, 9); KV_STAGE(5, 6); KV_STAGE(6, 3); KV_STAGE(7, 0);
#undef KV_STAGE
  tag = nonce_tag(nonce);
  STAMP(1);
  asm volatile("s_waitcnt lgkmcnt(0)\n\ts_barrier" ::: "memory");
  float* red = (float*)lds;
#pragma unroll
  for (int g = 0; g < 4; ++g)
    *(f32x4*)(red + (w * 4 + g) * 256 + lane * 4) = (f32x4){acc[4 * g], acc[4 * g + 1], acc[4 * g + 2], acc[4 * g + 3]};
  asm volatile("s_waitcnt lgkmcnt(0)\n\ts_barrier" ::: "memory");
#pragma unroll
  for (int st = 0; st < 4; ++st) qg.stage(st);
  if (w < 2) {
    f32x16 tt;
#pragma unroll
    for (int g = 0; g < 4; ++g) {
      f32x4 a = *(const f32x4*)(red + ((w + 0) * 4 + g) * 256 + lane * 4);
      f32x4 b = *(const f32x4*)(red + ((w + 2) * 4 + g) * 256 + lane * 4);
      f32x4 c = *(const f32x4*)(red + ((w + 4) * 4 + g) * 256 + lane * 4);
      f32x4 d = *(const f32x4*)(red + ((w + 6) * 4 + g) * 256 + lane * 4);
#pragma unroll
      for (int e = 0; e < 4; ++e) tt[4 * g + e] = (a[e] + b[e]) + (c[e] + d[e]);
    }
    char* dst = ws + WS_KVA + (size_t)rt * 4096 + w * 2048;
    gstore16_sc1<0>(lane * 16, dst, pack8(tt, 0));
    gstore16_sc1<1024>(lane * 16, dst, pack8(tt, 8));
  }
  {
    const u32x4 gw0 = *(const u32x4*)(gw + lane * 32), gw1 = *(const u32x4*)(gw + lane * 32 + 16);
#pragma unroll
    for (int i = 0; i < 4; ++i) {
      const char* px = xs + (size_t)(4 * w + i) * 2048 + lane * 32;
      const u32x4 gx0 = *(const u32x4*)px, gx1 = *(const u32x4*)(px + 16);
      float z = 0.f;
#pragma unroll
      for (int e = 0; e < 4; ++e) {
        z += __uint_as_float(gx0[e] << 16) * __uint_as_float(gw0[e] << 16) + __uint_as_float(gx0[e] & 0xffff0000u) * __uint_as_float(gw0[e] & 0xffff0000u);
        z += __uint_as_float(gx1[e] << 16) * __uint_as_float(gw1[e] << 16) + __uint_as_float(gx1[e] & 0xffff0000u) * __uint_as_float(gw1[e] & 0xffff0000u);
      }
#pragma unroll
      for (int o = 32; o >= 1; o >>= 1) z += __shfl_xor(z, o);
      const float g = 1.f / (1.f + __expf(-z));
      if (lane == 0) asm volatile("global_store_dword %0, %1, %2 sc1\n\ts_nop 0" :: "v"(0u), "v"(g), "s"(ws + WS_G + (rt * 32 + 4 * w + i) * 4) : "memory");
    }
  }
  asm volatile("s_waitcnt vmcnt(0) lgkmcnt(0)\n\ts_barrier" ::: "memory");
  if (tid == 0) flag_set(ws + WS_FKV + rt * 8, tag);
  STAMP(2);
}

constexpr int SL = 32768;
constexpr int R_LDS = 5 * SL;
constexpr int XCH_OFF = 0;
constexpr int WO_OFF = 2 * SL;

DI int crow(int r, int h) { return (r & 3) + 8 * (r >> 2) + 4 * h; }
DI float swap_half(float x) {
  auto rr = __builtin_amdgcn_permlane32_swap(__float_as_uint(x), __float_as_uint(x), false, false);
  return __uint_as_float((threadIdx.x & 32) ? rr[0] : rr[1]);
}
DI void exp16(f32x16& s) {
#pragma unroll
  for (int i = 0; i < 16; ++i) s[i] = __builtin_amdgcn_exp2f(s[i]);
}
#define AE(S, i) S[i] = __builtin_amdgcn_exp2f(S[i])
#define AC(P, S, i) P[(i) & 3] = cvtpk(S[2 * (i)], S[2 * (i) + 1])
#define ABC(x) __builtin_bit_cast(bf16x8, x)
#define APHASE(QK, PV, SX, PXa, PXb, SY, CINY, PYa, PYb, QY0, QY1, OY, LY, KF0, KF1, VF0, VF1, R1, R2) do { \
  if (QK) { SY = MFMA32(KF0, QY0, CINY); PIN(SY); } SBAR(); \
  AE(SX, 0); AE(SX, 1); AE(SX, 2); AE(SX, 3); PIN(SX); SBAR(); \
  if (QK) { SY = MFMA32(KF1, QY1, SY); PIN(SY); } SBAR(); \
  R1; \
  AC(PXa, SX, 0); AC(PXa, SX, 1); AE(SX, 4); AE(SX, 5); AE(SX, 6); PIN(SX); PIN(PXa); SBAR(); \
  if (PV) { OY = MFMA32(VF0, ABC(PYa), OY); PIN(OY); } SBAR(); \
  AE(SX, 7); AC(PXa, SX, 2); AE(SX, 8); AE(SX, 9); AC(PXa, SX, 3); PIN(SX); PIN(PXa); SBAR(); \
  if (PV) { LY = MFMA16(ones, ABC(PYa), LY); PIN(LY); } SBAR(); \
  R2; \
  AE(SX, 10); AE(SX, 11); PIN(SX); SBAR(); \
  if (PV) { OY = MFMA32(VF1, ABC(PYb), OY); PIN(OY); } SBAR(); \
  AC(PXb, SX, 4); AC(PXb, SX, 5); AE(SX, 12); AE(SX, 13); AE(SX, 14); PIN(SX); PIN(PXb); SBAR(); \
  if (PV) { LY = MFMA16(ones, ABC(PYb), LY); PIN(LY); } SBAR(); \
  AE(SX, 15); AC(PXb, SX, 6); AC(PXb, SX, 7); PIN(PXb); SBAR(); \
} while (0)

template <bool MASKED>
DI void attn_exact(const char* __restrict__ kv, int t0, int t1, int qt, const bf16x8& q0, const bf16x8& q1, const bf16x8& ones,
                   f32x16& o, float& l) {
  const int lane = threadIdx.x & 63, r = lane & 31, h = lane >> 5;
  float m = -3.0e38f;
  for (int t = t0; t < t1; ++t) {
    const char* p = kv + (size_t)t * 4096 + lane * 16;
    bf16x8 k0 = gload16_now(p), k1 = gload16_now(p + 1024);
    f32x16 s = {};
    s = MFMA32(k0, q0, s); s = MFMA32(k1, q1, s);
#pragma unroll
    for (int i = 0; i < 16; ++i) {
      bool ok = true;
      if (MASKED) { const int c = crow(i, h); ok = (t == qt) ? (c <= r) : ((t == qt - 8) ? (c > r) : true); }
      m = fmaxf(m, ok ? s[i] : -3.0e38f);
    }
  }
  m = fmaxf(m, swap_half(m));
  o = (f32x16){};
  f32x4 la = {};
  for (int t = t0; t < t1; ++t) {
    const char* p = kv + (size_t)t * 4096 + lane * 16;
    bf16x8 k0 = gload16_now(p), k1 = gload16_now(p + 1024);
    bf16x8 v0 = gload16_now(p + 2048), v1 = gload16_now(p + 3072);
    f32x16 s = {};
    s = MFMA32(k0, q0, s); s = MFMA32(k1, q1, s);
#pragma unroll
    for (int i = 0; i < 16; ++i) {
      bool ok = true;
      if (MASKED) { const int c = crow(i, h); ok = (t == qt) ? (c <= r) : ((t == qt - 8) ? (c > r) : true); }
      s[i] = ok ? __builtin_amdgcn_exp2f(s[i] - m) : 0.f;
    }
    bf16x8 pa = pack8(s, 0), pb = pack8(s, 8);
    o = MFMA32(v0, pa, o); o = MFMA32(v1, pb, o);
    la = MFMA16(ones, pa, la); la = MFMA16(ones, pb, la);
  }
  l = la[0];
}
DI bool ref_unsafe(const f32x16& o, float l) {
  float z = 0.f;
#pragma unroll
  for (int i = 0; i < 16; ++i) z = __builtin_fmaf(o[i], 0.f, z);
  const bool ok = (l >= 5.421010862e-20f) && (l <= 1.8446744e19f) && (z == 0.f);
  return __any(!ok);
}

__global__ __launch_bounds__(512, 2) void k_fused(const char* __restrict__ xa, const char* __restrict__ wq,
                                                  const char* __restrict__ wk, const char* __restrict__ wv,
                                                  const char* __restrict__ woa, const char* __restrict__ wqkv,
                                                  const char* __restrict__ wob, const char* __restrict__ gw,
                                                  char* __restrict__ ws, char* __restrict__ out) {
  extern __shared__ __attribute__((aligned(16))) char lds[];
  STAMP_DECL STAMP(0); TRACE_DECL
  const int b = blockIdx.x;
  const int xcd = b & 7, idx = b >> 3;
  const int tid = threadIdx.x, lane = tid & 63;
  const int w = rfl(tid >> 6);
  const int r = lane & 31, h = lane >> 5;
  const unsigned lds0 = (unsigned)(uintptr_t)lds;
  const unsigned lo16 = lane * 16;
  const int qp = (xcd & 3) * 8 + (idx & 7), hg = (xcd >> 2) * 4 + (idx >> 3);
  QGemm qg;
  qg.init(xa, wq, qp, hg, lds0, w, lane);
  u32x2 tag;
  bf16x8 held[8];
  const int pmb = (xcd & 1) * 8 + (idx & 7), pnb = (idx >> 3) * 4 + (xcd >> 1);
  const bool isB = pnb < 12;
  if (pnb < 8) proj_b<false>(xa, wqkv + pnb * 512, ws, pmb, pnb, lds, qg, tag, held STAMP_PASS);
  else if (pnb < 12) proj_b<true>(xa, wqkv + pnb * 512, ws, pmb, pnb, lds, qg, tag, held STAMP_PASS);
  else {
    proj_kv(xa, wk, wv, gw, ws, xcd * 8 + (idx & 7), lds, qg, tag STAMP_PASS);
#pragma unroll
    for (int n = 0; n < 8; ++n) held[n] = (bf16x8){};
  }
  STAMP(3);

  const unsigned hstep = pnb < 4 ? 2048 : 4096;
  char* hbase = ws + (pnb < 4 ? WS_QB : WS_KVB + (pnb >= 8 ? 2048 : 0)) +
                (size_t)(((pnb & 3) * 8 + (w >> 1) * 2) * 64 + pmb * 4 + (w & 1) * 2) * hstep;
  auto storeH = [&](int n) {
    if (isB) {
      char* dst = hbase + (size_t)((n >> 2) + ((n >> 1) & 1) * 64) * hstep;
      if (n & 1) gstore16_sc1<1024>(lo16, dst, held[n]);
      else gstore16_sc1<0>(lo16, dst, held[n]);
    }
  };
  const int pr = w & 3, half = w >> 2;
  const int hA = 4 * hg + pr;
  const int hB = (b & 7) + 8 * (b >> 6), rb = (b >> 3) & 7;
  const int qtB = rb * 8 + w;
  const int mb = (xcd & 3) * 4 + (idx & 3), nb = (xcd >> 2) * 8 + (idx >> 2);
  const char* kva = ws + WS_KVA;
  const char* kvb = ws + WS_KVB + (size_t)hB * 64 * 4096;
  bf16x8 ones;
  {
    const short v = ((((lane >> 4) & 1) == (((lane & 15) >> 2) & 1)) ? (short)0x3f80 : (short)0);
    ones = (bf16x8){v, v, v, v, v, v, v, v};
  }
  auto pieceA = [&](int k, int sl, int i) {
    const char* src = kva + (size_t)(half * 32 + 4 * k + pr) * 4096;
    const unsigned dst = lds0 + sl * SL + w * 4096;
    if (i == 0) glds16_sc1<0>(lo16, src, dst);
    else if (i == 1) glds16_sc1<1024>(lo16, src, dst);
    else if (i == 2) glds16_sc1<2048>(lo16, src, dst);
    else glds16_sc1<3072>(lo16, src, dst);
  };
  auto pieceB = [&](int n) {
    int gt = rb * 8 - 8 + 2 * w + (n >> 2);
    gt = gt < 0 ? 0 : gt;
    const char* src = kvb + (size_t)gt * 4096;
    const unsigned dst = lds0 + (2 * w + (n >> 2)) * 4096;
    if ((n & 3) == 0) glds16_sc1<0>(lo16, src, dst);
    else if ((n & 3) == 1) glds16_sc1<1024>(lo16, src, dst);
    else if ((n & 3) == 2) glds16_sc1<2048>(lo16, src, dst);
    else glds16_sc1<3072>(lo16, src, dst);
  };
  const char* pqb = ws + WS_QB + (size_t)(hB * 64 + qtB) * 2048;
  unsigned wo[2];
#pragma unroll
  for (int e = 0; e < 2; ++e) {
    const int row = 16 * w + 8 * e + (lane >> 3);
    const int c = (lane & 7) ^ (((row >> 1) & 1) << 2);
    wo[e] = row * 2048 + c * 16;
  }
  auto pieceW = [&](int st, int e) {
    const char* src = (st < 8 ? wob : woa) + (size_t)(st & 7) * 128 * 2048 + nb * 128;
    glds16<0>(wo[e], src, lds0 + WO_OFF + (st % 6) * 16384 + w * 2048 + e * 1024);
  };
#define NOP (void)0
  bf16x8 qb[2], q[2][2];
  u32x2 seen = {~tag[0], 0u};
  {
    const int g16 = lane >> 4, q4 = (lane & 15) >> 2, p4 = lane & 3;
    const int xa0 = Q_OFF + (32 * half + r) * 128 + ((h ^ ((r >> 1) & 7)) << 4);
    const int wa0 = Q_OFF + 8192 + (8 * h + q4) * 256 + (((pr * 4 + 2 * (g16 & 1) + (p4 >> 1)) ^ (q4 << 2)) << 4) + 8 * (p4 & 1);
    lds_cptr xb[2][4], wb[2];
#pragma unroll
    for (int u = 0; u < 2; ++u) {
#pragma unroll
      for (int s = 0; s < 4; ++s) { xb[u][s] = (lds_cptr)lds + u * 2 * Q_SLOT + (xa0 ^ (s << 5)); PIN(xb[u][s]); }
      wb[u] = (lds_cptr)lds + u * 2 * Q_SLOT + wa0; PIN(wb[u]);
    }
    f32x16 acc = {};
#define Q_STAGE(st, VM, PRE, POST) do { \
      asm volatile("s_waitcnt vmcnt(" #VM ") lgkmcnt(0)" : "+v"(seen)::"memory"); \
      PRE; \
      asm volatile("s_barrier" ::: "memory"); \
      bf16x8 xf[4], wf[4]; \
      _Pragma("unroll") for (int s = 0; s < 4; ++s) { \
        lds_cptr pw = wb[((st) >> 1) & 1] + ((st) & 1) * Q_SLOT + s * 4096; \
        xf[s] = lds_rd128(xb[((st) >> 1) & 1][s] + ((st) & 1) * Q_SLOT); \
        wf[s] = cat8(lds_tr(pw), lds_tr(pw + 1024)); \
      } \
      SBAR(); \
      if ((st) >= 1 && (st) + 3 < 16) qg.stage((st) + 3); \
      POST; \
      SBAR(); \
      _Pragma("unroll") for (int s = 0; s < 4; ++s) acc = MFMA32(wf[s], xf[s], acc); \
    } while (0)
    Q_STAGE(0, 9, NOP, NOP);   Q_STAGE(1, 6, NOP, NOP);
    Q_STAGE(2, 6, NOP, flag_peek(seen, lane * 8, ws + WS_FKV));
    Q_STAGE(3, 7, NOP, NOP);   Q_STAGE(4, 7, NOP, NOP);
    Q_STAGE(5, 6, if (w == 0) flag_wait(seen, lane * 8, ws + WS_FKV, tag), (pieceA(0, 0, 0), pieceA(0, 0, 1), pieceA(0, 0, 2), pieceA(0, 0, 3)));
    Q_STAGE(6, 10, NOP, NOP);  Q_STAGE(7, 10, NOP, NOP);  Q_STAGE(8, 10, NOP, NOP);
    Q_STAGE(9, 6, NOP, NOP);   Q_STAGE(10, 6, NOP, NOP);  Q_STAGE(11, 6, NOP, NOP);
    Q_STAGE(12, 6, NOP, NOP);  Q_STAGE(13, 6, NOP, NOP);  Q_STAGE(14, 3, NOP, NOP);
    Q_STAGE(15, 0, NOP, (storeH(0), storeH(1), storeH(2), storeH(3), storeH(4), storeH(5), storeH(6), storeH(7)));
#undef Q_STAGE
    STAMP(4);
    const bf16x8 mine0 = pack8s(acc, 0, QSCALE), mine1 = pack8s(acc, 8, QSCALE);
    typedef __attribute__((address_space(3))) bf16x8* lds_v8;
    *(lds_v8)((lds_ptr)lds + QX_OFF + w * 2048 + lo16) = mine0;
    *(lds_v8)((lds_ptr)lds + QX_OFF + w * 2048 + 1024 + lo16) = mine1;
    asm volatile("s_waitcnt lgkmcnt(0)\n\ts_barrier" ::: "memory");
    const bf16x8 oth0 = lds_rd128((lds_cptr)lds + QX_OFF + (w ^ 4) * 2048 + lo16);
    const bf16x8 oth1 = lds_rd128((lds_cptr)lds + QX_OFF + (w ^ 4) * 2048 + 1024 + lo16);
    q[0][0] = half ? oth0 : mine0; q[0][1] = half ? oth1 : mine1;
    q[1][0] = half ? mine0 : oth0; q[1][1] = half ? mine1 : oth1;
  }

  const f32x16 zero = {};
  f32x16 o0 = {}, o1 = {}, s0, s1;
  f32x4 l0 = {}, l1 = {};
  u32x4 p0a, p0b, p1a, p1b;
  bf16x8 ka0, ka1, kb0, kb1, va0, va1, vb0, vb1;
  lds_cptr kvbase = (lds_cptr)lds + half * 16384 + lane * 16;
#define APAIR(pp, SL0, SL1, FIRST, LAST, PA, PB, MID, D0, D1, D2, D3) do { \
    lds_cptr cur = kvbase + (SL0) * SL + ((pp) & 1) * 8192; PIN(cur);     \
    lds_cptr nxt = (SL0) == (SL1) && !((pp) & 1) ? cur + 8192 : kvbase + (SL1) * SL; \
    __builtin_amdgcn_s_setprio(PA); \
    if (FIRST) { ka0 = lds_rd128(cur); ka1 = lds_rd128(cur + 1024); s0 = MFMA32(ka0, q[0][0], zero); s0 = MFMA32(ka1, q[0][1], s0); } \
    SBAR(); \
    APHASE(1, !(FIRST), s0, p0a, p0b, s1, zero, p1a, p1b, q[1][0], q[1][1], o1, l1, ka0, ka1, vb0, vb1, \
           (kb0 = lds_rd128(cur + 4096), kb1 = lds_rd128(cur + 5120)), (va0 = lds_rd128(cur + 2048), va1 = lds_rd128(cur + 3072))); \
    D0; SBAR(); \
    APHASE(1, 1, s1, p1a, p1b, s0, zero, p0a, p0b, q[0][0], q[0][1], o0, l0, kb0, kb1, va0, va1, (void)0, (void)0); \
    D1; SBAR(); \
    ATRACE(2 * (pp)); \
    MID; \
    ATRACE(2 * (pp) + 1); \
    __builtin_amdgcn_s_setprio(PB); SBAR(); \
    APHASE(1, 1, s0, p0a, p0b, s1, zero, p1a, p1b, q[1][0], q[1][1], o1, l1, kb0, kb1, va0, va1, \
           if (!(LAST)) (ka0 = lds_rd128(nxt), ka1 = lds_rd128(nxt + 1024)), (vb0 = lds_rd128(cur + 4096 + 2048), vb1 = lds_rd128(cur + 4096 + 3072))); \
    D2; SBAR(); \
    APHASE(!(LAST), 1, s1, p1a, p1b, s0, zero, p0a, p0b, q[0][0], q[0][1], o0, l0, ka0, ka1, vb0, vb1, (void)0, (void)0); \
    D3; SBAR(); \
    if (LAST) { \
      o1 = MFMA32(vb0, ABC(p1a), o1); o1 = MFMA32(vb1, ABC(p1b), o1); \
      l1 = MFMA16(ones, ABC(p1a), l1); l1 = MFMA16(ones, ABC(p1b), l1); \
    } \
  } while (0)
#define ATRACE(i) do {} while (0)
#define DA(k, sl, i) pieceA(k, sl, i)
#define DA2(k, sl, i) (pieceA(k, sl, i), pieceA(k, sl, (i) + 1))
#define DB2(n) (pieceB(n), pieceB((n) + 1))
#define DH2(n) (storeH(n), storeH((n) + 1))
#define DW(st, e) pieceW(st, e)
#define BAR_V(n) asm volatile("s_waitcnt vmcnt(" #n ")\n\ts_barrier" ::: "memory")
  STAMP(5);
  const int fbm = 2 * rb - 2 + (lane & 3);
  const unsigned fbo = (((lane >> 2) % 3 * 4 + (hB >> 3)) * 16 + (fbm < 0 ? 2 * rb : fbm)) * 8;
  seen = (u32x2){~tag[0], 0u};
  APAIR(0, 0, 0, 1, 0, 2, 1, NOP, DA2(1, 3, 0), DA2(1, 3, 2), DA2(2, 4, 0), DA2(2, 4, 2));
  APAIR(1, 0, 3, 0, 0, 0, 3, { BAR_V(4); if (isB && tid == 0) flag_set(ws + WS_FPB + (pnb * 16 + pmb) * 8, tag);   },
        NOP, NOP, NOP, NOP);
  APAIR(2, 3, 3, 0, 0, 2, 1, NOP, NOP, NOP, NOP, NOP);
  flag_peek(seen, fbo, ws + WS_FPB);
  APAIR(3, 3, 4, 0, 0, 0, 3, { asm volatile("s_waitcnt vmcnt(0)" : "+v"(seen)::"memory"); if (w == 0) flag_wait(seen, fbo, ws + WS_FPB, tag); asm volatile("s_barrier" ::: "memory"); },
        NOP, NOP, DB2(0), DB2(2));
  APAIR(4, 4, 4, 0, 0, 2, 2, NOP, DB2(4), DB2(6), (gload16_sc1<0>(qb[0], lo16, pqb), gload16_sc1<1024>(qb[1], lo16, pqb)), DA2(3, 2, 0));
  APAIR(5, 4, 4, 0, 1, 1, 0, NOP, DA2(3, 2, 2), NOP, NOP, NOP);
  asm volatile("s_waitcnt vmcnt(0) lgkmcnt(0)\n\ts_barrier" : "+v"(qb[0]), "+v"(qb[1])::"memory");
  STAMP(6);
  {
    f32x16 o = {};
    f32x4 la = {};
    lds_cptr bb = (lds_cptr)lds + w * 4096 + lane * 16;
    f32x16 mD;
#pragma unroll
    for (int i = 0; i < 16; ++i) mD[i] = crow(i, h) <= r ? 0.f : -1.0e30f;
    if (rb != 0) {
      f32x16 sa, sb;
#pragma unroll
      for (int i = 0; i < 16; ++i) sa[i] = -1.0e30f - mD[i];
      u32x4 pa0 = {}, pa1 = {}, pb0 = {}, pb1 = {};
      bf16x8 k0, k1, k2, k3, v0, v1, v2, v3;
      k0 = lds_rd128(bb); k1 = lds_rd128(bb + 1024);
      k2 = lds_rd128(bb + 4096); k3 = lds_rd128(bb + 4096 + 1024);
      sa = MFMA32(k0, qb[0], sa); sa = MFMA32(k1, qb[1], sa);
      SBAR();
#define BRDK(KA, KB, j) (KA = lds_rd128(bb + (j) * 4096), KB = lds_rd128(bb + (j) * 4096 + 1024))
#define BRDV(VA, VB, j) (VA = lds_rd128(bb + (j) * 4096 + 2048), VB = lds_rd128(bb + (j) * 4096 + 3072))
#define B_DMA(n) do { pieceA(4 + (n) / 4, 3 + (n) / 4, (n) & 3); SBAR(); } while (0)
      __builtin_amdgcn_s_setprio(3);
      APHASE(1, 0, sa, pa0, pa1, sb, zero, pb0, pb1, qb[0], qb[1], o, la, k2, k3, v2, v3, BRDK(k0, k1, 2), BRDV(v0, v1, 0));
      B_DMA(0); B_DMA(1);
      APHASE(1, 1, sb, pb0, pb1, sa, zero, pa0, pa1, qb[0], qb[1], o, la, k0, k1, v0, v1, BRDK(k2, k3, 3), BRDV(v2, v3, 1));
      B_DMA(2); B_DMA(3);
      __builtin_amdgcn_s_setprio(2);
      APHASE(1, 1, sa, pa0, pa1, sb, zero, pb0, pb1, qb[0], qb[1], o, la, k2, k3, v2, v3, BRDK(k0, k1, 4), BRDV(v0, v1, 2));
      B_DMA(4); B_DMA(5);
      APHASE(1, 1, sb, pb0, pb1, sa, zero, pa0, pa1, qb[0], qb[1], o, la, k0, k1, v0, v1, BRDK(k2, k3, 5), BRDV(v2, v3, 3));
      B_DMA(6); B_DMA(7);
      __builtin_amdgcn_s_setprio(1);
      APHASE(1, 1, sa, pa0, pa1, sb, zero, pb0, pb1, qb[0], qb[1], o, la, k2, k3, v2, v3, BRDK(k0, k1, 6), BRDV(v0, v1, 4));
      APHASE(1, 1, sb, pb0, pb1, sa, zero, pa0, pa1, qb[0], qb[1], o, la, k0, k1, v0, v1, BRDK(k2, k3, 7), BRDV(v2, v3, 5));
      __builtin_amdgcn_s_setprio(0);
      APHASE(1, 1, sa, pa0, pa1, sb, zero, pb0, pb1, qb[0], qb[1], o, la, k2, k3, v2, v3, BRDK(k0, k1, 8), BRDV(v0, v1, 6));
      APHASE(1, 1, sb, pb0, pb1, sa, mD, pa0, pa1, qb[0], qb[1], o, la, k0, k1, v0, v1, (void)0, BRDV(v2, v3, 7));
      APHASE(0, 1, sa, pa0, pa1, sb, zero, pb0, pb1, qb[0], qb[1], o, la, k2, k3, v2, v3, (void)0, BRDV(v0, v1, 8));
      o = MFMA32(v0, ABC(pa0), o); o = MFMA32(v1, ABC(pa1), o);
      la = MFMA16(ones, ABC(pa0), la); la = MFMA16(ones, ABC(pa1), la);
    } else {
#pragma unroll
      for (int n = 0; n < 8; ++n) B_DMA(n);
      auto tile = [&](int j, const f32x16& cin) {
        lds_cptr p = bb + j * 4096;
        bf16x8 k0 = lds_rd128(p), k1 = lds_rd128(p + 1024), v0 = lds_rd128(p + 2048), v1 = lds_rd128(p + 3072);
        f32x16 s = MFMA32(k0, qb[0], cin);
        s = MFMA32(k1, qb[1], s);
        exp16(s);
        bf16x8 pa = pack8(s, 0), pb = pack8(s, 8);
        o = MFMA32(v0, pa, o); o = MFMA32(v1, pb, o);
        la = MFMA16(ones, pa, la); la = MFMA16(ones, pb, la);
      };
      for (int j = 8 - w; j < 8; ++j) tile(j, zero);
      tile(8, mD);
    }
    float l = la[0];
    if (__builtin_expect(ref_unsafe(o, l), 0)) attn_exact<true>(kvb, qtB >= 8 ? qtB - 8 : 0, qtB + 1, qtB, qb[0], qb[1], ones, o, l);
    const float sc = __builtin_amdgcn_rcpf(l);
    char* dst = ws + WS_O + (size_t)(qtB * 64 + 32 + hB) * 2048;
    gstore16_sc1<0>(lo16, dst, pack8s(o, 0, sc));
    gstore16_sc1<1024>(lo16, dst, pack8s(o, 8, sc));
  }
  asm volatile("s_waitcnt vmcnt(2) lgkmcnt(0)\n\ts_barrier" ::: "memory");
  STAMP(7);
  const int wm = w & 3, kh = w >> 2;
  const char* obase = ws + WS_O + (size_t)(mb * 4 + wm) * 64 * 2048 + kh * 4096;
  bf16x8 of[5][4];
  auto loadO1 = [&](int st, int i, bf16x8& f) {
    const char* src = obase + (size_t)(st < 8 ? 32 + 4 * st : 4 * (st - 8)) * 2048;
    if (i == 0) gload16_sc1<0>(f, lo16, src);
    else if (i == 1) gload16_sc1<1024>(f, lo16, src);
    else if (i == 2) gload16_sc1<2048>(f, lo16, src);
    else gload16_sc1<3072>(f, lo16, src);
  };
#define LO(st, i) loadO1(st, i, of[st][i])
  auto loadO = [&](int st, bf16x8 (&f)[4]) {
    const char* src = obase + (size_t)(st < 8 ? 32 + 4 * st : 4 * (st - 8)) * 2048;
    gload16_sc1<0>(f[0], lo16, src); gload16_sc1<1024>(f[1], lo16, src);
    gload16_sc1<2048>(f[2], lo16, src); gload16_sc1<3072>(f[3], lo16, src);
  };
  const unsigned fob = ((mb >> 1) * 32 + (lane & 31)) * 8;
  seen = (u32x2){~tag[0], 0u};
  unsigned warm = 0;
  APAIR(6, 2, 2, 1, 0, 3, 3, NOP, DA(6, 0, 0), DA(6, 0, 1), DA(6, 0, 2), DA(6, 0, 3));
  APAIR(7, 2, 3, 0, 0, 3, 2, NOP, DA(7, 1, 0), DA(7, 1, 1), DA(7, 1, 2), DA(7, 1, 3));
  {
    const int R = 768 + (idx & 3) * 320 + w * 40 + (lane < 40 ? lane : 39);
    const char* pw = (R < 1024 ? wob + (size_t)R * 2048 : woa + (size_t)(R - 1024) * 2048) + nb * 128;
    asm volatile("global_load_dword %0, %1, off" : "+v"(warm) : "v"(pw) : "memory");
  }
  APAIR(8, 3, 3, 0, 0, 2, 2, NOP, NOP, NOP, NOP, NOP);
  APAIR(9, 3, 4, 0, 0, 1, 1, NOP, NOP, NOP, NOP, NOP);
  APAIR(10, 4, 4, 0, 0, 1, 0, NOP, NOP, NOP, NOP, NOP);
  APAIR(11, 4, 0, 0, 0, 0, 3, { asm volatile("s_waitcnt vmcnt(0)\n\ts_barrier" : "+v"(warm)::"memory");
                                if (tid == 0) flag_set(ws + WS_FOB + (rb * 32 + hB) * 8, tag);   }, NOP, NOP, NOP, NOP);
  APAIR(12, 0, 0, 0, 0, 3, 2, NOP, DW(0, 0), DW(0, 1), DW(1, 0), DW(1, 1));
  flag_peek(seen, fob, ws + WS_FOB);
  APAIR(13, 0, 1, 0, 0, 2, 1, NOP, DW(2, 0), DW(2, 1), DW(3, 0), DW(3, 1));
  APAIR(14, 1, 1, 0, 0, 0, 3, { asm volatile("s_waitcnt vmcnt(0)" : "+v"(seen)::"memory"); if (w == 0) flag_wait(seen, fob, ws + WS_FOB, tag); asm volatile("s_barrier" ::: "memory"); },
        NOP, NOP, (LO(0, 0), LO(0, 1), DW(4, 0)), (LO(0, 2), LO(0, 3), DW(4, 1)));
  APAIR(15, 1, 1, 0, 1, 2, 1, NOP, (LO(1, 0), LO(1, 1), DW(5, 0)), (LO(1, 2), LO(1, 3), DW(5, 1)), (LO(2, 0), LO(2, 1)), (LO(2, 2), LO(2, 3)));
  STAMP(8);

  loadO(3, of[3]);
  asm volatile("s_waitcnt lgkmcnt(0)\n\ts_barrier" ::: "memory");
  {
    float* xb = (float*)(lds + XCH_OFF) + w * (5 * 256);
    const f32x16& os = half ? o0 : o1;
    const float ls = half ? l0[0] : l1[0];
#pragma unroll
    for (int g = 0; g < 4; ++g) *(f32x4*)(xb + g * 256 + lane * 4) = (f32x4){os[4 * g], os[4 * g + 1], os[4 * g + 2], os[4 * g + 3]};
    xb[4 * 256 + lane] = ls;
  }
  asm volatile("s_waitcnt lgkmcnt(0)\n\ts_barrier" ::: "memory");
  {
    const float* xb = (const float*)(lds + XCH_OFF) + (w ^ 4) * (5 * 256);
    f32x16 o = half ? o1 : o0;
    float l = half ? l1[0] : l0[0];
#pragma unroll
    for (int g = 0; g < 4; ++g) {
      f32x4 v = *(const f32x4*)(xb + g * 256 + lane * 4);
#pragma unroll
      for (int e = 0; e < 4; ++e) o[4 * g + e] += v[e];
    }
    l += xb[4 * 256 + lane];
    if (__builtin_expect(ref_unsafe(o, l), 0)) {
      if (half) attn_exact<false>(kva, 0, 64, 0, q[1][0], q[1][1], ones, o, l);
      else attn_exact<false>(kva, 0, 64, 0, q[0][0], q[0][1], ones, o, l);
    }
    const float sc = __builtin_amdgcn_rcpf(l);
    char* dst = ws + WS_O + (size_t)((2 * qp + half) * 64 + hA) * 2048;
    gstore16_sc1<0>(lo16, dst, pack8s(o, 0, sc));
    gstore16_sc1<1024>(lo16, dst, pack8s(o, 8, sc));
  }
  STAMP(9);
  {
    const int g16 = lane >> 4, q4 = (lane & 15) >> 2, p4 = lane & 3;
    const int wc = 2 * (g16 & 1) + (p4 >> 1);
    const int wa0 = (kh * 64 + 4 * h + q4) * 128 + ((wc ^ (((q4 >> 1) & 1) << 2)) << 4) + 8 * (p4 & 1);
    lds_cptr wbase[2][2] = {{(lds_cptr)lds + WO_OFF + wa0, (lds_cptr)lds + WO_OFF + (wa0 ^ 64)},
                            {(lds_cptr)lds + WO_OFF + 49152 + wa0, (lds_cptr)lds + WO_OFF + 49152 + (wa0 ^ 64)}};
    PIN(wbase[0][0]); PIN(wbase[0][1]); PIN(wbase[1][0]); PIN(wbase[1][1]);
    f32x16 accB[2] = {}, accA[2] = {};
    bf16x8 wf[4][2];
    const unsigned foa = (mb * 16 + (lane & 15)) * 8;
    u32x2 seenA = {~tag[0], 0u};
    u32x2 all0 = seenA, all1 = seenA, all2 = seenA, all3 = seenA;
    float g = 0.f;
    auto rdw = [&](bf16x8 (&f)[2], int st, int ks) {
#pragma unroll
      for (int j = 0; j < 2; ++j) {
        lds_cptr p = wbase[(st % 6) / 3][j] + ((st % 6) % 3) * 16384 + ks * 2048;
        f[j] = cat8(lds_tr(p), lds_tr(p + 1024));
      }
    };
    asm volatile("s_waitcnt vmcnt(17)" : "+v"(of[0][0]), "+v"(of[0][1]), "+v"(of[0][2]), "+v"(of[0][3])::"memory");
    rdw(wf[0], 0, 0); rdw(wf[1], 0, 1);
#define OTRACE(i) do {} while (0)
#define O_MM(ACC, f, a) do { ACC[0] = MFMA32(f[0], a, ACC[0]); SBAR(); ACC[1] = MFMA32(f[1], a, ACC[1]); SBAR(); } while (0)
#define O_STAGE(st, ACC, VM, PRE, POST) do { \
      rdw(wf[2], st, 2); SBAR(); O_MM(ACC, wf[0], of[(st) % 5][0]); \
      rdw(wf[3], st, 3); SBAR(); O_MM(ACC, wf[1], of[(st) % 5][1]); \
      if ((st) < 15) { \
        OTRACE(3 * (st)); \
        asm volatile("s_waitcnt vmcnt(" #VM ")" : "+v"(of[((st) + 1) % 5][0]), "+v"(of[((st) + 1) % 5][1]), \
                     "+v"(of[((st) + 1) % 5][2]), "+v"(of[((st) + 1) % 5][3]), "+v"(seenA)::"memory"); \
        PRE; \
        OTRACE(3 * (st) + 1); \
        asm volatile("s_barrier" ::: "memory"); \
        OTRACE(3 * (st) + 2); \
        POST; \
        SBAR(); \
        rdw(wf[0], (st) + 1, 0); \
      } \
      SBAR(); O_MM(ACC, wf[2], of[(st) % 5][2]); \
      if ((st) < 15) rdw(wf[1], (st) + 1, 1); \
      SBAR(); O_MM(ACC, wf[3], of[(st) % 5][3]); \
    } while (0)
#define OW(st) (pieceW(st, 0), pieceW(st, 1))
#define OL(st) loadO(st, of[(st) % 5])
    O_STAGE(0, accB, 11, NOP, OL(4));
    O_STAGE(1, accB, 4, NOP, { if (tid == 0) flag_set(ws + WS_FOA + (qp * 8 + hg) * 8, tag);   OW(6); OL(5); });
    O_STAGE(2, accB, 10, NOP, (OW(7), OL(6)));
    O_STAGE(3, accB, 12, NOP, (flag_peek(seenA, foa, ws + WS_FOA), OW(8), OL(7)));
    O_STAGE(4, accB, 13, NOP, OW(9));
    O_STAGE(5, accB, 8, if (w == 0) flag_wait(seenA, foa, ws + WS_FOA, tag),
            (gload4_sc1(g, (wm * 32 + r) * 4, ws + WS_G + mb * 512), OW(10), OL(8), OL(9)));
    O_STAGE(6, accB, 13, NOP, (OW(11), OL(10)));
    O_STAGE(7, accB, 10, NOP, (OW(12), OL(11)));
    O_STAGE(8, accA, 12, NOP, (OW(13), OL(12)));
    O_STAGE(9, accA, 12, NOP, (flag_peek(all0, lane * 8, ws + WS_FOA), flag_peek(all1, lane * 8, ws + WS_FOA + 512),
                                     flag_peek(all2, lane * 8, ws + WS_FOA + 1024), flag_peek(all3, lane * 8, ws + WS_FOA + 1536), OW(14), OL(13)));
    O_STAGE(10, accA, 16, NOP, (OW(15), OL(14)));
    O_STAGE(11, accA, 16, NOP, OL(15));
    O_STAGE(12, accA, 10, NOP, NOP);
    O_STAGE(13, accA, 4, NOP, NOP);
    O_STAGE(14, accA, 0, NOP, NOP);
    O_STAGE(15, accA, 0, NOP, NOP);
    asm volatile("" : "+v"(g), "+v"(all0), "+v"(all1), "+v"(all2), "+v"(all3));
    if (b == 0 && w == 0) {
      flag_wait(all0, lane * 8, ws + WS_FOA, tag); flag_wait(all1, lane * 8, ws + WS_FOA + 512, tag);
      flag_wait(all2, lane * 8, ws + WS_FOA + 1024, tag); flag_wait(all3, lane * 8, ws + WS_FOA + 1536, tag);
      if (lane == 0) flag_set(ws + WS_NONCE, tag);
    }
    STAMP(10);
    float* xw = (float*)(lds + XCH_OFF) + w * 1024 + lane * 4;
    f32x16 acc[2];
#pragma unroll
    for (int j = 0; j < 2; ++j)
#pragma unroll
      for (int i = 0; i < 16; ++i) acc[j][i] = g * accA[j][i] + (1.f - g) * accB[j][i];
    const f32x16& as = kh ? acc[0] : acc[1];
#pragma unroll
    for (int g = 0; g < 4; ++g) *(f32x4*)(xw + g * 256) = (f32x4){as[4 * g], as[4 * g + 1], as[4 * g + 2], as[4 * g + 3]};
    asm volatile("s_waitcnt lgkmcnt(0)\n\ts_barrier" ::: "memory");
    f32x16 t = kh ? acc[1] : acc[0];
    const float* xr = (const float*)(lds + XCH_OFF) + (w ^ 4) * 1024 + lane * 4;
#pragma unroll
    for (int g = 0; g < 4; ++g) {
      f32x4 v = *(const f32x4*)(xr + g * 256);
#pragma unroll
      for (int e = 0; e < 4; ++e) t[4 * g + e] += v[e];
    }
    const int row = mb * 128 + wm * 32 + r;
    char* dst = out + (size_t)row * 2048 + (nb * 64 + kh * 32) * 2;
#pragma unroll
    for (int g = 0; g < 4; g += 2) {
      u32x2 a = {cvtpk(t[4 * g], t[4 * g + 1]), cvtpk(t[4 * g + 2], t[4 * g + 3])};
      u32x2 c = {cvtpk(t[4 * g + 4], t[4 * g + 5]), cvtpk(t[4 * g + 6], t[4 * g + 7])};
      auto x = __builtin_amdgcn_permlane32_swap(a[0], c[0], false, false);
      auto y = __builtin_amdgcn_permlane32_swap(a[1], c[1], false, false);
      *(u32x4*)(dst + g * 16 + h * 16) = (u32x4){x[0], y[0], x[1], y[1]};
    }
  }
  STAMP(11);
  STAMP_OUT(0, ws); TRACE_OUT(ws);
}

extern "C" void kernel_launch(void* const* d_in, const int* in_sizes, int n_in, void* d_out, int out_size, void* d_ws,
                              size_t ws_size, hipStream_t stream) {
  const char* xa = (const char*)d_in[0];
  const char* wq = (const char*)d_in[1];
  const char* wk = (const char*)d_in[2];
  const char* wv = (const char*)d_in[3];
  const char* woa = (const char*)d_in[4];
  const char* wqkv = (const char*)d_in[6];
  const char* wob = (const char*)d_in[7];
  const char* gw = (const char*)d_in[8];
  (void)hipFuncSetAttribute((const void*)k_fused, hipFuncAttributeMaxDynamicSharedMemorySize, R_LDS);
  k_fused<<<256, 512, R_LDS, stream>>>(xa, wq, wk, wv, woa, wqkv, wob, gw, (char*)d_ws, (char*)d_out);
}
```

```cpp
#define PROBE 1
#define PROBE_A 0
#define PROBE_B 1
#define PROBE_BLK 0
#include <hip/hip_runtime.h>
#include <hip/hip_bf16.h>
#include <stdint.h>

typedef float f32x16 __attribute__((ext_vector_type(16)));
typedef float f32x4 __attribute__((ext_vector_type(4)));
typedef float f32x2 __attribute__((ext_vector_type(2)));
typedef short bf16x8 __attribute__((ext_vector_type(8)));
typedef short s16x4 __attribute__((ext_vector_type(4)));
typedef __bf16 bf16x2 __attribute__((ext_vector_type(2)));
typedef unsigned u32x4 __attribute__((ext_vector_type(4)));
typedef unsigned u32x2 __attribute__((ext_vector_type(2)));
typedef __attribute__((address_space(3))) const char* lds_cptr;
typedef __attribute__((address_space(3))) char* lds_ptr;

#define DI __device__ __forceinline__
#define SBAR() __builtin_amdgcn_sched_barrier(0)
#define PIN(x) asm volatile("" : "+v"(x))
#define MFMA32(a, b, c) __builtin_amdgcn_mfma_f32_32x32x16_bf16(a, b, c, 0, 0, 0)
#define MFMA16(a, b, c) __builtin_amdgcn_mfma_f32_16x16x32_bf16(a, b, c, 0, 0, 0)

constexpr int SEQ = 2048, DM = 1024;
constexpr float QSCALE = 0.17677669529663687f * 1.4426950408889634f;

constexpr size_t WS_KVA = 4u << 20;
constexpr size_t WS_G = WS_KVA + (256u << 10);
constexpr size_t WS_NONCE = WS_G + 8192;
constexpr size_t WS_FKV = WS_NONCE + 1024;
constexpr size_t WS_FPB = WS_FKV + 1024;
constexpr size_t WS_FOA = WS_FPB + 2048;
constexpr size_t WS_FOB = WS_FOA + 2048;
constexpr size_t WS_QB = 5u << 20;
constexpr size_t WS_KVB = 9u << 20;
constexpr size_t WS_O = 17u << 20;

#define STAMP_ARG , unsigned (&pr_)[2]
#define STAMP_PASS , pr_
#define STAMP_DECL unsigned pr_[2] = {0, 0};
#define STAMP(i) do { if ((i) == PROBE_A) pr_[0] = (unsigned)__builtin_amdgcn_s_memrealtime(); if ((i) == PROBE_B) pr_[1] = (unsigned)__builtin_amdgcn_s_memrealtime(); } while (0)
#define STAMP_OUT(k, wsp) do { if (blockIdx.x == PROBE_BLK && threadIdx.x < 64) { const unsigned d_ = (pr_[1] - pr_[0]) * 10u, t_ = (unsigned)__builtin_amdgcn_s_memrealtime(); \
  while ((unsigned)__builtin_amdgcn_s_memrealtime() - t_ < d_) __builtin_amdgcn_s_sleep(1); } } while (0)
#define TRACE(i) do {} while (0)
#define TRACE_DECL
#define TRACE_OUT(wsp) do {} while (0)
DI unsigned rfl(unsigned x) { return __builtin_amdgcn_readfirstlane(x); }
DI unsigned cvtpk(float lo, float hi) {
  f32x2 v = {lo, hi};
  bf16x2 b = __builtin_convertvector(v, bf16x2);
  return __builtin_bit_cast(unsigned, b);
}
DI bf16x8 pack8(const f32x16& a, int o) {
  u32x4 r = {cvtpk(a[o], a[o + 1]), cvtpk(a[o + 2], a[o + 3]), cvtpk(a[o + 4], a[o + 5]), cvtpk(a[o + 6], a[o + 7])};
  return __builtin_bit_cast(bf16x8, r);
}
DI bf16x8 pack8s(const f32x16& a, int o, float s) {
  u32x4 r = {cvtpk(a[o] * s, a[o + 1] * s), cvtpk(a[o + 2] * s, a[o + 3] * s), cvtpk(a[o + 4] * s, a[o + 5] * s),
             cvtpk(a[o + 6] * s, a[o + 7] * s)};
  return __builtin_bit_cast(bf16x8, r);
}
template <int OFF>
DI void glds16(unsigned voff, const void* sbase, unsigned lds_base) {
  asm volatile("s_mov_b32 m0, %2\n\ts_nop 0\n\tglobal_load_lds_dwordx4 %0, %1 offset:%3"
               :: "v"(voff), "s"(sbase), "s"(rfl(lds_base)), "i"(OFF) : "memory");
}
template <int OFF>
DI void glds16_sc1(unsigned voff, const void* sbase, unsigned lds_base) {
  asm volatile("s_mov_b32 m0, %2\n\ts_nop 0\n\tglobal_load_lds_dwordx4 %0, %1 offset:%3 sc1"
               :: "v"(voff), "s"(sbase), "s"(rfl(lds_base)), "i"(OFF) : "memory");
}
DI bf16x8 lds_rd128(lds_cptr p) { return *(const __attribute__((address_space(3))) bf16x8*)p; }
DI s16x4 lds_tr(lds_cptr p) {
  typedef short v4i16_t __attribute__((ext_vector_type(4)));
  return __builtin_bit_cast(s16x4, __builtin_amdgcn_ds_read_tr16_b64_v4i16((__attribute__((address_space(3))) v4i16_t*)p));
}
DI bf16x8 cat8(s16x4 lo, s16x4 hi) { return (bf16x8){lo[0], lo[1], lo[2], lo[3], hi[0], hi[1], hi[2], hi[3]}; }
DI float bf2f(unsigned short b) { return __uint_as_float((unsigned)b << 16); }

template <int OFF>
DI void gload16_sc1(bf16x8& d, unsigned voff, const void* sbase) {
  asm volatile("global_load_dwordx4 %0, %1, %2 offset:%3 sc1" : "=v"(d) : "v"(voff), "s"(sbase), "i"(OFF) : "memory");
}
template <int OFF>
DI void gstore16_sc1(unsigned voff, void* sbase, const bf16x8& d) {
  asm volatile("global_store_dwordx4 %0, %1, %2 offset:%3 sc1\n\ts_nop 1" :: "v"(voff), "v"(d), "s"(sbase), "i"(OFF) : "memory");
}
DI void gload4_sc1(float& d, unsigned voff, const void* sbase) {
  asm volatile("global_load_dword %0, %1, %2 sc1" : "=v"(d) : "v"(voff), "s"(sbase) : "memory");
}
DI bf16x8 gload16_now(const char* p) {
  bf16x8 d;
  asm volatile("global_load_dwordx4 %0, %1, off sc1\n\ts_waitcnt vmcnt(0)" : "=&v"(d) : "v"(p) : "memory");
  return d;
}
typedef __attribute__((address_space(1))) unsigned long long gu64;
DI void flag_set(char* p, u32x2 tag) {
  asm volatile("global_store_dwordx2 %0, %1, %2 sc1\n\ts_nop 1" :: "v"(0u), "v"(tag), "s"(p) : "memory");
}
DI void flag_peek(u32x2& d, unsigned voff, const void* sbase) {
  asm volatile("global_load_dwordx2 %0, %1, %2 sc1" : "+v"(d) : "v"(voff), "s"(sbase) : "memory");
}
DI void flag_wait(u32x2 seen, unsigned voff, const char* sbase, u32x2 tag) {
  if (__builtin_expect(__all(seen[0] == tag[0] && seen[1] == tag[1]), 1)) return;
  const unsigned long long want = ((unsigned long long)tag[1] << 32) | tag[0];
  for (unsigned spins = 0; spins < (1u << 21); ++spins) {
    if (__all(__hip_atomic_load((gu64*)(sbase + voff), __ATOMIC_RELAXED, __HIP_MEMORY_SCOPE_AGENT) == want)) return;
    __builtin_amdgcn_s_sleep(4);
  }
}

DI void nonce_load(u32x2& nonce, const char* ws) {
  asm volatile("global_load_dwordx2 %0, %1, %2 sc1" : "=v"(nonce) : "v"(0u), "s"(ws + WS_NONCE) : "memory");
}
DI u32x2 nonce_tag(u32x2 nonce) {
  u32x2 tag;
  tag[0] = nonce[0] + 1u;
  tag[1] = nonce[1] + (tag[0] == 0u ? 1u : 0u);
  return tag;
}

constexpr int Q_OFF = 49152, Q_SLOT = 24576, QX_OFF = 32768;
struct QGemm {
  const char* xs; const char* wsrc; unsigned xo, wo[2], lds0; int w;
  DI void init(const char* xa, const char* wq, int qp, int hg, unsigned lds0_, int w_, int lane) {
    xs = xa + (size_t)qp * 64 * 2048; wsrc = wq + hg * 256; lds0 = lds0_; w = w_;
    const int row = 8 * w + (lane >> 3);
    xo = row * 2048 + (((lane & 7) ^ ((row >> 1) & 7)) << 4);
#pragma unroll
    for (int e = 0; e < 2; ++e) {
      const int krow = 8 * w + 4 * e + (lane >> 4);
      wo[e] = krow * 2048 + (((lane & 15) ^ ((krow & 3) << 2)) << 4);
    }
  }
  DI void piece(int st, int n) const {
    const unsigned lb = lds0 + Q_OFF + (st & 3) * Q_SLOT;
    if (n == 0) glds16<0>(xo, xs + st * 128, lb + w * 1024);
    else glds16<0>(wo[n - 1], wsrc + (size_t)st * 64 * 2048, lb + 8192 + (2 * w + n - 1) * 1024);
  }
  DI void stage(int st) const { piece(st, 0); piece(st, 1); piece(st, 2); }
};

constexpr int P_SLOT = 49152;
constexpr int P_XOFF = 0, P_WOFF = 16384;
struct PFrag { bf16x8 x[2]; bf16x8 w[2]; };

template <bool VT>
DI void proj_b(const char* __restrict__ xg, const char* __restrict__ wg, const char* __restrict__ ws, int mb, int nb, char* lds,
               const QGemm& qg, u32x2& tag, bf16x8 (&held)[8] STAMP_ARG) {
  constexpr int ldw_bytes = 6144;
  const int tid = threadIdx.x, lane = tid & 63;
  const int w = rfl(tid >> 6);
  const int wm = w & 1, wn = w >> 1;
  const int r = lane & 31, h = lane >> 5;
  const unsigned lds0 = (unsigned)(uintptr_t)lds;
  u32x2 nonce;
  nonce_load(nonce, ws);
  unsigned xo[2], wo[4];
#pragma unroll
  for (int e = 0; e < 2; ++e) {
    const int row = 16 * w + 8 * e + (lane >> 3);
    const int c = (lane & 7) ^ (4 * e + (lane >> 4));
    xo[e] = row * 2048 + c * 16;
  }
#pragma unroll
  for (int e = 0; e < 4; ++e) {
    const int krow = 8 * w + 2 * e + (lane >> 5);
    const int c = (lane & 31) ^ ((krow & 3) << 2);
    wo[e] = krow * ldw_bytes + c * 16;
  }
  const char* xsrc = xg + (size_t)mb * 128 * 2048;
  auto piece = [&](int pc, int st, int slot) {
    const unsigned lb = lds0 + slot * P_SLOT;
    if (pc < 2) glds16<0>(xo[pc], xsrc + st * 128, lb + P_XOFF + w * 2048 + pc * 1024);
    else glds16<0>(wo[pc - 2], wg + (size_t)st * 64 * ldw_bytes, lb + P_WOFF + w * 4096 + (pc - 2) * 1024);
  };
  auto dma = [&](int st, int slot) {
#pragma unroll
    for (int pc = 0; pc < 6; ++pc) piece(pc, st, slot);
  };
  auto win = [&](int j, int pc, int slot) {
    if (j < 0) return;
    if (j <= 12) piece(pc, j + 3, slot);
    else if (j <= 14) qg.piece((j - 13) * 2 + pc / 3, pc % 3);
  };
  const int t3 = (r >> 1) & 7;
  const int xa0 = P_XOFF + (wm * 64 + r) * 128 + ((h ^ t3) << 4);
  const int g16 = lane >> 4, q4 = (lane & 15) >> 2, p4 = lane & 3;
  const int wc = wn * 8 + 2 * (g16 & 1) + (p4 >> 1);
  const int wa0 = P_WOFF + (8 * h + q4) * 512 + ((wc ^ (q4 << 2)) << 4) + 8 * (p4 & 1);

  auto rdx = [&](PFrag& f, int i, int slot, int s) { f.x[i] = lds_rd128((lds_cptr)lds + slot * P_SLOT + (xa0 ^ (s << 5)) + i * 4096); };
  auto rdw = [&](PFrag& f, int j, int slot, int s) {
    lds_cptr p = (lds_cptr)lds + slot * P_SLOT + (wa0 ^ (j << 6)) + s * 8192;
    f.w[j] = cat8(lds_tr(p), lds_tr(p + 2048));
  };
  f32x16 acc[2][2] = {};
  auto mm = [&](const PFrag& f, int i, int j) {
    if constexpr (VT) acc[i][j] = MFMA32(f.x[i], f.w[j], acc[i][j]);
    else acc[i][j] = MFMA32(f.w[j], f.x[i], acc[i][j]);
  };
  auto step = [&](const PFrag& c, PFrag& n, int rslot, int rs, bool rd, int pa, int wj, int dslot) {
    mm(c, 0, 0); SBAR();
    if (rd) { rdw(n, 0, rslot, rs); rdx(n, 0, rslot, rs); }
    SBAR();
    mm(c, 0, 1); SBAR();
    if (rd) { rdw(n, 1, rslot, rs); rdx(n, 1, rslot, rs); }
    SBAR();
    mm(c, 1, 0); SBAR();
    if (pa >= 0) win(wj, pa, dslot);
    SBAR();
    mm(c, 1, 1); SBAR();
    if (pa >= 0) win(wj, pa + 1, dslot);
    SBAR();
  };

  dma(0, 0);
  asm volatile("s_barrier" ::: "memory");
  dma(1, 1); dma(2, 2);
  asm volatile("s_waitcnt vmcnt(12)\n\ts_barrier" : "+v"(nonce)::"memory");
  tag = nonce_tag(nonce);
  STAMP(1);
  PFrag f0, f1;
  rdw(f0, 0, 0, 0); rdx(f0, 0, 0, 0); rdw(f0, 1, 0, 0); rdx(f0, 1, 0, 0);
  int slot = 0, pslot = 0;
  for (int it = 0; it < 16; ++it) {
    const int nslot = slot == 2 ? 0 : slot + 1;
    step(f0, f1, slot, 1, true, 2, it - 1, pslot);
    step(f1, f0, slot, 2, true, 4, it - 1, pslot);
    step(f0, f1, slot, 3, true, -1, 0, 0);
    if (it < 15) asm volatile("s_waitcnt vmcnt(6) lgkmcnt(0)\n\ts_barrier" ::: "memory");
    else asm volatile("s_waitcnt lgkmcnt(0)\n\ts_barrier" ::: "memory");
    SBAR();
    step(f1, f0, nslot, 0, it < 15, 0, it, slot);
    pslot = slot;
    slot = nslot;
  }
  STAMP(2);
  const float sc = (!VT && nb < 4) ? QSCALE : 1.f;
#pragma unroll
  for (int i = 0; i < 2; ++i)
#pragma unroll
    for (int j = 0; j < 2; ++j) {
      held[(i * 2 + j) * 2] = pack8s(acc[i][j], 0, sc);
      held[(i * 2 + j) * 2 + 1] = pack8s(acc[i][j], 8, sc);
    }
}

DI void proj_kv(const char* __restrict__ xg, const char* __restrict__ wk, const char* __restrict__ wv, const char* __restrict__ gw,
                char* __restrict__ ws, int rt, char* lds, const QGemm& qg, u32x2& tag STAMP_ARG) {
  const int tid = threadIdx.x, lane = tid & 63;
  const int w = rfl(tid >> 6);
  const int r = lane & 31, h = lane >> 5;
  const unsigned lds0 = (unsigned)(uintptr_t)lds;
  u32x2 nonce;
  nonce_load(nonce, ws);
  const int xrow = 4 * w + (lane >> 4);
  const unsigned xo = xrow * 2048 + (((lane & 15) ^ (xrow & 15)) << 4), wo = w * 1024 + lane * 16;
  const char* xs = xg + (size_t)rt * 32 * 2048;
  auto stage = [&](int st) {
    const unsigned lb = lds0 + (st % 6) * 24576 + w * 1024;
    glds16<0>(xo, xs + st * 256, lb);
    glds16<0>(wo, wk + st * 8192, lb + 8192);
    glds16<0>(wo, wv + st * 8192, lb + 16384);
  };
#pragma unroll
  for (int st = 0; st < 6; ++st) stage(st);
  const int t = w & 1, kq = w >> 1;
  const int g16 = lane >> 4, q4 = (lane & 15) >> 2, p4 = lane & 3;
  const int xa = r * 256, wa = 8192 + t * 8192 + (32 * kq + 8 * h + q4) * 64 + 32 * (g16 & 1) + 8 * p4;
  f32x16 acc = {};
#define KV_STAGE(st, VM) do { \
    asm volatile("s_waitcnt vmcnt(" #VM ") lgkmcnt(0)\n\ts_barrier" : "+v"(nonce)::"memory"); \
    if ((st) >= 1 && (st) + 5 < 8) stage((st) + 5); \
    lds_cptr b = (lds_cptr)lds + ((st) % 6) * 24576; \
    _Pragma("unroll") for (int e = 0; e < 2; ++e) { \
      const bf16x8 xf = lds_rd128(b + xa + (((4 * kq + 2 * e + h) ^ (r & 15)) << 4)); \
      const bf16x8 wf = cat8(lds_tr(b + wa + e * 1024), lds_tr(b + wa + e * 1024 + 256)); \
      if (t) acc = MFMA32(xf, wf, acc); else acc = MFMA32(wf, xf, acc); \
    } \
  } while (0)
  KV_STAGE(0, 15); KV_STAGE(1, 12); KV_STAGE(2, 12); KV_STAGE(3, 12); KV_STAGE(4, 9); KV_STAGE(5, 6); KV_STAGE(6, 3); KV_STAGE(7, 0);
#undef KV_STAGE
  tag = nonce_tag(nonce);
  STAMP(1);
  asm volatile("s_waitcnt lgkmcnt(0)\n\ts_barrier" ::: "memory");
  float* red = (float*)lds;
#pragma unroll
  for (int g = 0; g < 4; ++g)
    *(f32x4*)(red + (w * 4 + g) * 256 + lane * 4) = (f32x4){acc[4 * g], acc[4 * g + 1], acc[4 * g + 2], acc[4 * g + 3]};
  asm volatile("s_waitcnt lgkmcnt(0)\n\ts_barrier" ::: "memory");
#pragma unroll
  for (int st = 0; st < 4; ++st) qg.stage(st);
  if (w < 2) {
    f32x16 tt;
#pragma unroll
    for (int g = 0; g < 4; ++g) {
      f32x4 a = *(const f32x4*)(red + ((w + 0) * 4 + g) * 256 + lane * 4);
      f32x4 b = *(const f32x4*)(red + ((w + 2) * 4 + g) * 256 + lane * 4);
      f32x4 c = *(const f32x4*)(red + ((w + 4) * 4 + g) * 256 + lane * 4);
      f32x4 d = *(const f32x4*)(red + ((w + 6) * 4 + g) * 256 + lane * 4);
#pragma unroll
      for (int e = 0; e < 4; ++e) tt[4 * g + e] = (a[e] + b[e]) + (c[e] + d[e]);
    }
    char* dst = ws + WS_KVA + (size_t)rt * 4096 + w * 2048;
    gstore16_sc1<0>(lane * 16, dst, pack8(tt, 0));
    gstore16_sc1<1024>(lane * 16, dst, pack8(tt, 8));
  }
  {
    const u32x4 gw0 = *(const u32x4*)(gw + lane * 32), gw1 = *(const u32x4*)(gw + lane * 32 + 16);
#pragma unroll
    for (int i = 0; i < 4; ++i) {
      const char* px = xs + (size_t)(4 * w + i) * 2048 + lane * 32;
      const u32x4 gx0 = *(const u32x4*)px, gx1 = *(const u32x4*)(px + 16);
      float z = 0.f;
#pragma unroll
      for (int e = 0; e < 4; ++e) {
        z += __uint_as_float(gx0[e] << 16) * __uint_as_float(gw0[e] << 16) + __uint_as_float(gx0[e] & 0xffff0000u) * __uint_as_float(gw0[e] & 0xffff0000u);
        z += __uint_as_float(gx1[e] << 16) * __uint_as_float(gw1[e] << 16) + __uint_as_float(gx1[e] & 0xffff0000u) * __uint_as_float(gw1[e] & 0xffff0000u);
      }
#pragma unroll
      for (int o = 32; o >= 1; o >>= 1) z += __shfl_xor(z, o);
      const float g = 1.f / (1.f + __expf(-z));
      if (lane == 0) asm volatile("global_store_dword %0, %1, %2 sc1\n\ts_nop 0" :: "v"(0u), "v"(g), "s"(ws + WS_G + (rt * 32 + 4 * w + i) * 4) : "memory");
    }
  }
  asm volatile("s_waitcnt vmcnt(0) lgkmcnt(0)\n\ts_barrier" ::: "memory");
  if (tid == 0) flag_set(ws + WS_FKV + rt * 8, tag);
  STAMP(2);
}

constexpr int SL = 32768;
constexpr int R_LDS = 5 * SL;
constexpr int XCH_OFF = 0;
constexpr int WO_OFF = 2 * SL;

DI int crow(int r, int h) { return (r & 3) + 8 * (r >> 2) + 4 * h; }
DI float swap_half(float x) {
  auto rr = __builtin_amdgcn_permlane32_swap(__float_as_uint(x), __float_as_uint(x), false, false);
  return __uint_as_float((threadIdx.x & 32) ? rr[0] : rr[1]);
}
DI void exp16(f32x16& s) {
#pragma unroll
  for (int i = 0; i < 16; ++i) s[i] = __builtin_amdgcn_exp2f(s[i]);
}
#define AE(S, i) S[i] = __builtin_amdgcn_exp2f(S[i])
#define AC(P, S, i) P[(i) & 3] = cvtpk(S[2 * (i)], S[2 * (i) + 1])
#define ABC(x) __builtin_bit_cast(bf16x8, x)
#define APHASE(QK, PV, SX, PXa, PXb, SY, CINY, PYa, PYb, QY0, QY1, OY, LY, KF0, KF1, VF0, VF1, R1, R2) do { \
  if (QK) { SY = MFMA32(KF0, QY0, CINY); PIN(SY); } SBAR(); \
  AE(SX, 0); AE(SX, 1); AE(SX, 2); AE(SX, 3); PIN(SX); SBAR(); \
  if (QK) { SY = MFMA32(KF1, QY1, SY); PIN(SY); } SBAR(); \
  R1; \
  AC(PXa, SX, 0); AC(PXa, SX, 1); AE(SX, 4); AE(SX, 5); AE(SX, 6); PIN(SX); PIN(PXa); SBAR(); \
  if (PV) { OY = MFMA32(VF0, ABC(PYa), OY); PIN(OY); } SBAR(); \
  AE(SX, 7); AC(PXa, SX, 2); AE(SX, 8); AE(SX, 9); AC(PXa, SX, 3); PIN(SX); PIN(PXa); SBAR(); \
  if (PV) { LY = MFMA16(ones, ABC(PYa), LY); PIN(LY); } SBAR(); \
  R2; \
  AE(SX, 10); AE(SX, 11); PIN(SX); SBAR(); \
  if (PV) { OY = MFMA32(VF1, ABC(PYb), OY); PIN(OY); } SBAR(); \
  AC(PXb, SX, 4); AC(PXb, SX, 5); AE(SX, 12); AE(SX, 13); AE(SX, 14); PIN(SX); PIN(PXb); SBAR(); \
  if (PV) { LY = MFMA16(ones, ABC(PYb), LY); PIN(LY); } SBAR(); \
  AE(SX, 15); AC(PXb, SX, 6); AC(PXb, SX, 7); PIN(PXb); SBAR(); \
} while (0)

template <bool MASKED>
DI void attn_exact(const char* __restrict__ kv, int t0, int t1, int qt, const bf16x8& q0, const bf16x8& q1, const bf16x8& ones,
                   f32x16& o, float& l) {
  const int lane = threadIdx.x & 63, r = lane & 31, h = lane >> 5;
  float m = -3.0e38f;
  for (int t = t0; t < t1; ++t) {
    const char* p = kv + (size_t)t * 4096 + lane * 16;
    bf16x8 k0 = gload16_now(p), k1 = gload16_now(p + 1024);
    f32x16 s = {};
    s = MFMA32(k0, q0, s); s = MFMA32(k1, q1, s);
#pragma unroll
    for (int i = 0; i < 16; ++i) {
      bool ok = true;
      if (MASKED) { const int c = crow(i, h); ok = (t == qt) ? (c <= r) : ((t == qt - 8) ? (c > r) : true); }
      m = fmaxf(m, ok ? s[i] : -3.0e38f);
    }
  }
  m = fmaxf(m, swap_half(m));
  o = (f32x16){};
  f32x4 la = {};
  for (int t = t0; t < t1; ++t) {
    const char* p = kv + (size_t)t * 4096 + lane * 16;
    bf16x8 k0 = gload16_now(p), k1 = gload16_now(p + 1024);
    bf16x8 v0 = gload16_now(p + 2048), v1 = gload16_now(p + 3072);
    f32x16 s = {};
    s = MFMA32(k0, q0, s); s = MFMA32(k1, q1, s);
#pragma unroll
    for (int i = 0; i < 16; ++i) {
      bool ok = true;
      if (MASKED) { const int c = crow(i, h); ok = (t == qt) ? (c <= r) : ((t == qt - 8) ? (c > r) : true); }
      s[i] = ok ? __builtin_amdgcn_exp2f(s[i] - m) : 0.f;
    }
    bf16x8 pa = pack8(s, 0), pb = pack8(s, 8);
    o = MFMA32(v0, pa, o); o = MFMA32(v1, pb, o);
    la = MFMA16(ones, pa, la); la = MFMA16(ones, pb, la);
  }
  l = la[0];
}
DI bool ref_unsafe(const f32x16& o, float l) {
  float z = 0.f;
#pragma unroll
  for (int i = 0; i < 16; ++i) z = __builtin_fmaf(o[i], 0.f, z);
  const bool ok = (l >= 5.421010862e-20f) && (l <= 1.8446744e19f) && (z == 0.f);
  return __any(!ok);
}

__global__ __launch_bounds__(512, 2) void k_fused(const char* __restrict__ xa, const char* __restrict__ wq,
                                                  const char* __restrict__ wk, const char* __restrict__ wv,
                                                  const char* __restrict__ woa, const char* __restrict__ wqkv,
                                                  const char* __restrict__ wob, const char* __restrict__ gw,
                                                  char* __restrict__ ws, char* __restrict__ out) {
  extern __shared__ __attribute__((aligned(16))) char lds[];
  STAMP_DECL STAMP(0); TRACE_DECL
  const int b = blockIdx.x;
  const int xcd = b & 7, idx = b >> 3;
  const int tid = threadIdx.x, lane = tid & 63;
  const int w = rfl(tid >> 6);
  const int r = lane & 31, h = lane >> 5;
  const unsigned lds0 = (unsigned)(uintptr_t)lds;
  const unsigned lo16 = lane * 16;
  const int qp = (xcd & 3) * 8 + (idx & 7), hg = (xcd >> 2) * 4 + (idx >> 3);
  QGemm qg;
  qg.init(xa, wq, qp, hg, lds0, w, lane);
  u32x2 tag;
  bf16x8 held[8];
  const int pmb = (xcd & 1) * 8 + (idx & 7), pnb = (idx >> 3) * 4 + (xcd >> 1);
  const bool isB = pnb < 12;
  if (pnb < 8) proj_b<false>(xa, wqkv + pnb * 512, ws, pmb, pnb, lds, qg, tag, held STAMP_PASS);
  else if (pnb < 12) proj_b<true>(xa, wqkv + pnb * 512, ws, pmb, pnb, lds, qg, tag, held STAMP_PASS);
  else {
    proj_kv(xa, wk, wv, gw, ws, xcd * 8 + (idx & 7), lds, qg, tag STAMP_PASS);
#pragma unroll
    for (int n = 0; n < 8; ++n) held[n] = (bf16x8){};
  }
  STAMP(3);

  const unsigned hstep = pnb < 4 ? 2048 : 4096;
  char* hbase = ws + (pnb < 4 ? WS_QB : WS_KVB + (pnb >= 8 ? 2048 : 0)) +
                (size_t)(((pnb & 3) * 8 + (w >> 1) * 2) * 64 + pmb * 4 + (w & 1) * 2) * hstep;
  auto storeH = [&](int n) {
    if (isB) {
      char* dst = hbase + (size_t)((n >> 2) + ((n >> 1) & 1) * 64) * hstep;
      if (n & 1) gstore16_sc1<1024>(lo16, dst, held[n]);
      else gstore16_sc1<0>(lo16, dst, held[n]);
    }
  };
  const int pr = w & 3, half = w >> 2;
  const int hA = 4 * hg + pr;
  const int hB = (b & 7) + 8 * (b >> 6), rb = (b >> 3) & 7;
  const int qtB = rb * 8 + w;
  const int mb = (xcd & 3) * 4 + (idx & 3), nb = (xcd >> 2) * 8 + (idx >> 2);
  const char* kva = ws + WS_KVA;
  const char* kvb = ws + WS_KVB + (size_t)hB * 64 * 4096;
  bf16x8 ones;
  {
    const short v = ((((lane >> 4) & 1) == (((lane & 15) >> 2) & 1)) ? (short)0x3f80 : (short)0);
    ones = (bf16x8){v, v, v, v, v, v, v, v};
  }
  auto pieceA = [&](int k, int sl, int i) {
    const char* src = kva + (size_t)(half * 32 + 4 * k + pr) * 4096;
    const unsigned dst = lds0 + sl * SL + w * 4096;
    if (i == 0) glds16_sc1<0>(lo16, src, dst);
    else if (i == 1) glds16_sc1<1024>(lo16, src, dst);
    else if (i == 2) glds16_sc1<2048>(lo16, src, dst);
    else glds16_sc1<3072>(lo16, src, dst);
  };
  auto pieceB = [&](int n) {
    int gt = rb * 8 - 8 + 2 * w + (n >> 2);
    gt = gt < 0 ? 0 : gt;
    const char* src = kvb + (size_t)gt * 4096;
    const unsigned dst = lds0 + (2 * w + (n >> 2)) * 4096;
    if ((n & 3) == 0) glds16_sc1<0>(lo16, src, dst);
    else if ((n & 3) == 1) glds16_sc1<1024>(lo16, src, dst);
    else if ((n & 3) == 2) glds16_sc1<2048>(lo16, src, dst);
    else glds16_sc1<3072>(lo16, src, dst);
  };
  const char* pqb = ws + WS_QB + (size_t)(hB * 64 + qtB) * 2048;
  unsigned wo[2];
#pragma unroll
  for (int e = 0; e < 2; ++e) {
    const int row = 16 * w + 8 * e + (lane >> 3);
    const int c = (lane & 7) ^ (((row >> 1) & 1) << 2);
    wo[e] = row * 2048 + c * 16;
  }
  auto pieceW = [&](int st, int e) {
    const char* src = (st < 8 ? wob : woa) + (size_t)(st & 7) * 128 * 2048 + nb * 128;
    glds16<0>(wo[e], src, lds0 + WO_OFF + (st % 6) * 16384 + w * 2048 + e * 1024);
  };
#define NOP (void)0
  bf16x8 qb[2], q[2][2];
  u32x2 seen = {~tag[0], 0u};
  {
    const int g16 = lane >> 4, q4 = (lane & 15) >> 2, p4 = lane & 3;
    const int xa0 = Q_OFF + (32 * half + r) * 128 + ((h ^ ((r >> 1) & 7)) << 4);
    const int wa0 = Q_OFF + 8192 + (8 * h + q4) * 256 + (((pr * 4 + 2 * (g16 & 1) + (p4 >> 1)) ^ (q4 << 2)) << 4) + 8 * (p4 & 1);
    lds_cptr xb[2][4], wb[2];
#pragma unroll
    for (int u = 0; u < 2; ++u) {
#pragma unroll
      for (int s = 0; s < 4; ++s) { xb[u][s] = (lds_cptr)lds + u * 2 * Q_SLOT + (xa0 ^ (s << 5)); PIN(xb[u][s]); }
      wb[u] = (lds_cptr)lds + u * 2 * Q_SLOT + wa0; PIN(wb[u]);
    }
    f32x16 acc = {};
#define Q_STAGE(st, VM, PRE, POST) do { \
      asm volatile("s_waitcnt vmcnt(" #VM ") lgkmcnt(0)" : "+v"(seen)::"memory"); \
      PRE; \
      asm volatile("s_barrier" ::: "memory"); \
      bf16x8 xf[4], wf[4]; \
      _Pragma("unroll") for (int s = 0; s < 4; ++s) { \
        lds_cptr pw = wb[((st) >> 1) & 1] + ((st) & 1) * Q_SLOT + s * 4096; \
        xf[s] = lds_rd128(xb[((st) >> 1) & 1][s] + ((st) & 1) * Q_SLOT); \
        wf[s] = cat8(lds_tr(pw), lds_tr(pw + 1024)); \
      } \
      SBAR(); \
      if ((st) >= 1 && (st) + 3 < 16) qg.stage((st) + 3); \
      POST; \
      SBAR(); \
      _Pragma("unroll") for (int s = 0; s < 4; ++s) acc = MFMA32(wf[s], xf[s], acc); \
    } while (0)
    Q_STAGE(0, 9, NOP, NOP);   Q_STAGE(1, 6, NOP, NOP);
    Q_STAGE(2, 6, NOP, flag_peek(seen, lane * 8, ws + WS_FKV));
    Q_STAGE(3, 7, NOP, NOP);   Q_STAGE(4, 7, NOP, NOP);
    Q_STAGE(5, 6, if (w == 0) flag_wait(seen, lane * 8, ws + WS_FKV, tag), (pieceA(0, 0, 0), pieceA(0, 0, 1), pieceA(0, 0, 2), pieceA(0, 0, 3)));
    Q_STAGE(6, 10, NOP, NOP);  Q_STAGE(7, 10, NOP, NOP);  Q_STAGE(8, 10, NOP, NOP);
    Q_STAGE(9, 6, NOP, NOP);   Q_STAGE(10, 6, NOP, NOP);  Q_STAGE(11, 6, NOP, NOP);
    Q_STAGE(12, 6, NOP, NOP);  Q_STAGE(13, 6, NOP, NOP);  Q_STAGE(14, 3, NOP, NOP);
    Q_STAGE(15, 0, NOP, (storeH(0), storeH(1), storeH(2), storeH(3), storeH(4), storeH(5), storeH(6), storeH(7)));
#undef Q_STAGE
    STAMP(4);
    const bf16x8 mine0 = pack8s(acc, 0, QSCALE), mine1 = pack8s(acc, 8, QSCALE);
    typedef __attribute__((address_space(3))) bf16x8* lds_v8;
    *(lds_v8)((lds_ptr)lds + QX_OFF + w * 2048 + lo16) = mine0;
    *(lds_v8)((lds_ptr)lds + QX_OFF + w * 2048 + 1024 + lo16) = mine1;
    asm volatile("s_waitcnt lgkmcnt(0)\n\ts_barrier" ::: "memory");
    const bf16x8 oth0 = lds_rd128((lds_cptr)lds + QX_OFF + (w ^ 4) * 2048 + lo16);
    const bf16x8 oth1 = lds_rd128((lds_cptr)lds + QX_OFF + (w ^ 4) * 2048 + 1024 + lo16);
    q[0][0] = half ? oth0 : mine0; q[0][1] = half ? oth1 : mine1;
    q[1][0] = half ? mine0 : oth0; q[1][1] = half ? mine1 : oth1;
  }

  const f32x16 zero = {};
  f32x16 o0 = {}, o1 = {}, s0, s1;
  f32x4 l0 = {}, l1 = {};
  u32x4 p0a, p0b, p1a, p1b;
  bf16x8 ka0, ka1, kb0, kb1, va0, va1, vb0, vb1;
  lds_cptr kvbase = (lds_cptr)lds + half * 16384 + lane * 16;
#define APAIR(pp, SL0, SL1, FIRST, LAST, PA, PB, MID, D0, D1, D2, D3) do { \
    lds_cptr cur = kvbase + (SL0) * SL + ((pp) & 1) * 8192; PIN(cur);     \
    lds_cptr nxt = (SL0) == (SL1) && !((pp) & 1) ? cur + 8192 : kvbase + (SL1) * SL; \
    __builtin_amdgcn_s_setprio(PA); \
    if (FIRST) { ka0 = lds_rd128(cur); ka1 = lds_rd128(cur + 1024); s0 = MFMA32(ka0, q[0][0], zero); s0 = MFMA32(ka1, q[0][1], s0); } \
    SBAR(); \
    APHASE(1, !(FIRST), s0, p0a, p0b, s1, zero, p1a, p1b, q[1][0], q[1][1], o1, l1, ka0, ka1, vb0, vb1, \
           (kb0 = lds_rd128(cur + 4096), kb1 = lds_rd128(cur + 5120)), (va0 = lds_rd128(cur + 2048), va1 = lds_rd128(cur + 3072))); \
    D0; SBAR(); \
    APHASE(1, 1, s1, p1a, p1b, s0, zero, p0a, p0b, q[0][0], q[0][1], o0, l0, kb0, kb1, va0, va1, (void)0, (void)0); \
    D1; SBAR(); \
    ATRACE(2 * (pp)); \
    MID; \
    ATRACE(2 * (pp) + 1); \
    __builtin_amdgcn_s_setprio(PB); SBAR(); \
    APHASE(1, 1, s0, p0a, p0b, s1, zero, p1a, p1b, q[1][0], q[1][1], o1, l1, kb0, kb1, va0, va1, \
           if (!(LAST)) (ka0 = lds_rd128(nxt), ka1 = lds_rd128(nxt + 1024)), (vb0 = lds_rd128(cur + 4096 + 2048), vb1 = lds_rd128(cur + 4096 + 3072))); \
    D2; SBAR(); \
    APHASE(!(LAST), 1, s1, p1a, p1b, s0, zero, p0a, p0b, q[0][0], q[0][1], o0, l0, ka0, ka1, vb0, vb1, (void)0, (void)0); \
    D3; SBAR(); \
    if (LAST) { \
      o1 = MFMA32(vb0, ABC(p1a), o1); o1 = MFMA32(vb1, ABC(p1b), o1); \
      l1 = MFMA16(ones, ABC(p1a), l1); l1 = MFMA16(ones, ABC(p1b), l1); \
    } \
  } while (0)
#define ATRACE(i) do {} while (0)
#define DA(k, sl, i) pieceA(k, sl, i)
#define DA2(k, sl, i) (pieceA(k, sl, i), pieceA(k, sl, (i) + 1))
#define DB2(n) (pieceB(n), pieceB((n) + 1))
#define DH2(n) (storeH(n), storeH((n) + 1))
#define DW(st, e) pieceW(st, e)
#define BAR_V(n) asm volatile("s_waitcnt vmcnt(" #n ")\n\ts_barrier" ::: "memory")
  STAMP(5);
  const int fbm = 2 * rb - 2 + (lane & 3);
  const unsigned fbo = (((lane >> 2) % 3 * 4 + (hB >> 3)) * 16 + (fbm < 0 ? 2 * rb : fbm)) * 8;
  seen = (u32x2){~tag[0], 0u};
  APAIR(0, 0, 0, 1, 0, 2, 1, NOP, DA2(1, 3, 0), DA2(1, 3, 2), DA2(2, 4, 0), DA2(2, 4, 2));
  APAIR(1, 0, 3, 0, 0, 0, 3, { BAR_V(4); if (isB && tid == 0) flag_set(ws + WS_FPB + (pnb * 16 + pmb) * 8, tag);   },
        NOP, NOP, NOP, NOP);
  APAIR(2, 3, 3, 0, 0, 2, 1, NOP, NOP, NOP, NOP, NOP);
  flag_peek(seen, fbo, ws + WS_FPB);
  APAIR(3, 3, 4, 0, 0, 0, 3, { asm volatile("s_waitcnt vmcnt(0)" : "+v"(seen)::"memory"); if (w == 0) flag_wait(seen, fbo, ws + WS_FPB, tag); asm volatile("s_barrier" ::: "memory"); },
        NOP, NOP, DB2(0), DB2(2));
  APAIR(4, 4, 4, 0, 0, 2, 2, NOP, DB2(4), DB2(6), (gload16_sc1<0>(qb[0], lo16, pqb), gload16_sc1<1024>(qb[1], lo16, pqb)), DA2(3, 2, 0));
  APAIR(5, 4, 4, 0, 1, 1, 0, NOP, DA2(3, 2, 2), NOP, NOP, NOP);
  asm volatile("s_waitcnt vmcnt(0) lgkmcnt(0)\n\ts_barrier" : "+v"(qb[0]), "+v"(qb[1])::"memory");
  STAMP(6);
  {
    f32x16 o = {};
    f32x4 la = {};
    lds_cptr bb = (lds_cptr)lds + w * 4096 + lane * 16;
    f32x16 mD;
#pragma unroll
    for (int i = 0; i < 16; ++i) mD[i] = crow(i, h) <= r ? 0.f : -1.0e30f;
    if (rb != 0) {
      f32x16 sa, sb;
#pragma unroll
      for (int i = 0; i < 16; ++i) sa[i] = -1.0e30f - mD[i];
      u32x4 pa0 = {}, pa1 = {}, pb0 = {}, pb1 = {};
      bf16x8 k0, k1, k2, k3, v0, v1, v2, v3;
      k0 = lds_rd128(bb); k1 = lds_rd128(bb + 1024);
      k2 = lds_rd128(bb + 4096); k3 = lds_rd128(bb + 4096 + 1024);
      sa = MFMA32(k0, qb[0], sa); sa = MFMA32(k1, qb[1], sa);
      SBAR();
#define BRDK(KA, KB, j) (KA = lds_rd128(bb + (j) * 4096), KB = lds_rd128(bb + (j) * 4096 + 1024))
#define BRDV(VA, VB, j) (VA = lds_rd128(bb + (j) * 4096 + 2048), VB = lds_rd128(bb + (j) * 4096 + 3072))
#define B_DMA(n) do { pieceA(4 + (n) / 4, 3 + (n) / 4, (n) & 3); SBAR(); } while (0)
      __builtin_amdgcn_s_setprio(3);
      APHASE(1, 0, sa, pa0, pa1, sb, zero, pb0, pb1, qb[0], qb[1], o, la, k2, k3, v2, v3, BRDK(k0, k1, 2), BRDV(v0, v1, 0));
      B_DMA(0); B_DMA(1);
      APHASE(1, 1, sb, pb0, pb1, sa, zero, pa0, pa1, qb[0], qb[1], o, la, k0, k1, v0, v1, BRDK(k2, k3, 3), BRDV(v2, v3, 1));
      B_DMA(2); B_DMA(3);
      __builtin_amdgcn_s_setprio(2);
      APHASE(1, 1, sa, pa0, pa1, sb, zero, pb0, pb1, qb[0], qb[1], o, la, k2, k3, v2, v3, BRDK(k0, k1, 4), BRDV(v0, v1, 2));
      B_DMA(4); B_DMA(5);
      APHASE(1, 1, sb, pb0, pb1, sa, zero, pa0, pa1, qb[0], qb[1], o, la, k0, k1, v0, v1, BRDK(k2, k3, 5), BRDV(v2, v3, 3));
      B_DMA(6); B_DMA(7);
      __builtin_amdgcn_s_setprio(1);
      APHASE(1, 1, sa, pa0, pa1, sb, zero, pb0, pb1, qb[0], qb[1], o, la, k2, k3, v2, v3, BRDK(k0, k1, 6), BRDV(v0, v1, 4));
      APHASE(1, 1, sb, pb0, pb1, sa, zero, pa0, pa1, qb[0], qb[1], o, la, k0, k1, v0, v1, BRDK(k2, k3, 7), BRDV(v2, v3, 5));
      __builtin_amdgcn_s_setprio(0);
      APHASE(1, 1, sa, pa0, pa1, sb, zero, pb0, pb1, qb[0], qb[1], o, la, k2, k3, v2, v3, BRDK(k0, k1, 8), BRDV(v0, v1, 6));
      APHASE(1, 1, sb, pb0, pb1, sa, mD, pa0, pa1, qb[0], qb[1], o, la, k0, k1, v0, v1, (void)0, BRDV(v2, v3, 7));
      APHASE(0, 1, sa, pa0, pa1, sb, zero, pb0, pb1, qb[0], qb[1], o, la, k2, k3, v2, v3, (void)0, BRDV(v0, v1, 8));
      o = MFMA32(v0, ABC(pa0), o); o = MFMA32(v1, ABC(pa1), o);
      la = MFMA16(ones, ABC(pa0), la); la = MFMA16(ones, ABC(pa1), la);
    } else {
#pragma unroll
      for (int n = 0; n < 8; ++n) B_DMA(n);
      auto tile = [&](int j, const f32x16& cin) {
        lds_cptr p = bb + j * 4096;
        bf16x8 k0 = lds_rd128(p), k1 = lds_rd128(p + 1024), v0 = lds_rd128(p + 2048), v1 = lds_rd128(p + 3072);
        f32x16 s = MFMA32(k0, qb[0], cin);
        s = MFMA32(k1, qb[1], s);
        exp16(s);
        bf16x8 pa = pack8(s, 0), pb = pack8(s, 8);
        o = MFMA32(v0, pa, o); o = MFMA32(v1, pb, o);
        la = MFMA16(ones, pa, la); la = MFMA16(ones, pb, la);
      };
      for (int j = 8 - w; j < 8; ++j) tile(j, zero);
      tile(8, mD);
    }
    float l = la[0];
    if (__builtin_expect(ref_unsafe(o, l), 0)) attn_exact<true>(kvb, qtB >= 8 ? qtB - 8 : 0, qtB + 1, qtB, qb[0], qb[1], ones, o, l);
    const float sc = __builtin_amdgcn_rcpf(l);
    char* dst = ws + WS_O + (size_t)(qtB * 64 + 32 + hB) * 2048;
    gstore16_sc1<0>(lo16, dst, pack8s(o, 0, sc));
    gstore16_sc1<1024>(lo16, dst, pack8s(o, 8, sc));
  }
  asm volatile("s_waitcnt vmcnt(2) lgkmcnt(0)\n\ts_barrier" ::: "memory");
  STAMP(7);
  const int wm = w & 3, kh = w >> 2;
  const char* obase = ws + WS_O + (size_t)(mb * 4 + wm) * 64 * 2048 + kh * 4096;
  bf16x8 of[5][4];
  auto loadO1 = [&](int st, int i, bf16x8& f) {
    const char* src = obase + (size_t)(st < 8 ? 32 + 4 * st : 4 * (st - 8)) * 2048;
    if (i == 0) gload16_sc1<0>(f, lo16, src);
    else if (i == 1) gload16_sc1<1024>(f, lo16, src);
    else if (i == 2) gload16_sc1<2048>(f, lo16, src);
    else gload16_sc1<3072>(f, lo16, src);
  };
#define LO(st, i) loadO1(st, i, of[st][i])
  auto loadO = [&](int st, bf16x8 (&f)[4]) {
    const char* src = obase + (size_t)(st < 8 ? 32 + 4 * st : 4 * (st - 8)) * 2048;
    gload16_sc1<0>(f[0], lo16, src); gload16_sc1<1024>(f[1], lo16, src);
    gload16_sc1<2048>(f[2], lo16, src); gload16_sc1<3072>(f[3], lo16, src);
  };
  const unsigned fob = ((mb >> 1) * 32 + (lane & 31)) * 8;
  seen = (u32x2){~tag[0], 0u};
  unsigned warm = 0;
  APAIR(6, 2, 2, 1, 0, 3, 3, NOP, DA(6, 0, 0), DA(6, 0, 1), DA(6, 0, 2), DA(6, 0, 3));
  APAIR(7, 2, 3, 0, 0, 3, 2, NOP, DA(7, 1, 0), DA(7, 1, 1), DA(7, 1, 2), DA(7, 1, 3));
  {
    const int R = 768 + (idx & 3) * 320 + w * 40 + (lane < 40 ? lane : 39);
    const char* pw = (R < 1024 ? wob + (size_t)R * 2048 : woa + (size_t)(R - 1024) * 2048) + nb * 128;
    asm volatile("global_load_dword %0, %1, off" : "+v"(warm) : "v"(pw) : "memory");
  }
  APAIR(8, 3, 3, 0, 0, 2, 2, NOP, NOP, NOP, NOP, NOP);
  APAIR(9, 3, 4, 0, 0, 1, 1, NOP, NOP, NOP, NOP, NOP);
  APAIR(10, 4, 4, 0, 0, 1, 0, NOP, NOP, NOP, NOP, NOP);
  APAIR(11, 4, 0, 0, 0, 0, 3, { asm volatile("s_waitcnt vmcnt(0)\n\ts_barrier" : "+v"(warm)::"memory");
                                if (tid == 0) flag_set(ws + WS_FOB + (rb * 32 + hB) * 8, tag);   }, NOP, NOP, NOP, NOP);
  APAIR(12, 0, 0, 0, 0, 3, 2, NOP, DW(0, 0), DW(0, 1), DW(1, 0), DW(1, 1));
  flag_peek(seen, fob, ws + WS_FOB);
  APAIR(13, 0, 1, 0, 0, 2, 1, NOP, DW(2, 0), DW(2, 1), DW(3, 0), DW(3, 1));
  APAIR(14, 1, 1, 0, 0, 0, 3, { asm volatile("s_waitcnt vmcnt(0)" : "+v"(seen)::"memory"); if (w == 0) flag_wait(seen, fob, ws + WS_FOB, tag); asm volatile("s_barrier" ::: "memory"); },
        NOP, NOP, (LO(0, 0), LO(0, 1), DW(4, 0)), (LO(0, 2), LO(0, 3), DW(4, 1)));
  APAIR(15, 1, 1, 0, 1, 2, 1, NOP, (LO(1, 0), LO(1, 1), DW(5, 0)), (LO(1, 2), LO(1, 3), DW(5, 1)), (LO(2, 0), LO(2, 1)), (LO(2, 2), LO(2, 3)));
  STAMP(8);

  loadO(3, of[3]);
  asm volatile("s_waitcnt lgkmcnt(0)\n\ts_barrier" ::: "memory");
  {
    float* xb = (float*)(lds + XCH_OFF) + w * (5 * 256);
    const f32x16& os = half ? o0 : o1;
    const float ls = half ? l0[0] : l1[0];
#pragma unroll
    for (int g = 0; g < 4; ++g) *(f32x4*)(xb + g * 256 + lane * 4) = (f32x4){os[4 * g], os[4 * g + 1], os[4 * g + 2], os[4 * g + 3]};
    xb[4 * 256 + lane] = ls;
  }
  asm volatile("s_waitcnt lgkmcnt(0)\n\ts_barrier" ::: "memory");
  {
    const float* xb = (const float*)(lds + XCH_OFF) + (w ^ 4) * (5 * 256);
    f32x16 o = half ? o1 : o0;
    float l = half ? l1[0] : l0[0];
#pragma unroll
    for (int g = 0; g < 4; ++g) {
      f32x4 v = *(const f32x4*)(xb + g * 256 + lane * 4);
#pragma unroll
      for (int e = 0; e < 4; ++e) o[4 * g + e] += v[e];
    }
    l += xb[4 * 256 + lane];
    if (__builtin_expect(ref_unsafe(o, l), 0)) {
      if (half) attn_exact<false>(kva, 0, 64, 0, q[1][0], q[1][1], ones, o, l);
      else attn_exact<false>(kva, 0, 64, 0, q[0][0], q[0][1], ones, o, l);
    }
    const float sc = __builtin_amdgcn_rcpf(l);
    char* dst = ws + WS_O + (size_t)((2 * qp + half) * 64 + hA) * 2048;
    gstore16_sc1<0>(lo16, dst, pack8s(o, 0, sc));
    gstore16_sc1<1024>(lo16, dst, pack8s(o, 8, sc));
  }
  STAMP(9);
  {
    const int g16 = lane >> 4, q4 = (lane & 15) >> 2, p4 = lane & 3;
    const int wc = 2 * (g16 & 1) + (p4 >> 1);
    const int wa0 = (kh * 64 + 4 * h + q4) * 128 + ((wc ^ (((q4 >> 1) & 1) << 2)) << 4) + 8 * (p4 & 1);
    lds_cptr wbase[2][2] = {{(lds_cptr)lds + WO_OFF + wa0, (lds_cptr)lds + WO_OFF + (wa0 ^ 64)},
                            {(lds_cptr)lds + WO_OFF + 49152 + wa0, (lds_cptr)lds + WO_OFF + 49152 + (wa0 ^ 64)}};
    PIN(wbase[0][0]); PIN(wbase[0][1]); PIN(wbase[1][0]); PIN(wbase[1][1]);
    f32x16 accB[2] = {}, accA[2] = {};
    bf16x8 wf[4][2];
    const unsigned foa = (mb * 16 + (lane & 15)) * 8;
    u32x2 seenA = {~tag[0], 0u};
    u32x2 all0 = seenA, all1 = seenA, all2 = seenA, all3 = seenA;
    float g = 0.f;
    auto rdw = [&](bf16x8 (&f)[2], int st, int ks) {
#pragma unroll
      for (int j = 0; j < 2; ++j) {
        lds_cptr p = wbase[(st % 6) / 3][j] + ((st % 6) % 3) * 16384 + ks * 2048;
        f[j] = cat8(lds_tr(p), lds_tr(p + 1024));
      }
    };
    asm volatile("s_waitcnt vmcnt(17)" : "+v"(of[0][0]), "+v"(of[0][1]), "+v"(of[0][2]), "+v"(of[0][3])::"memory");
    rdw(wf[0], 0, 0); rdw(wf[1], 0, 1);
#define OTRACE(i) do {} while (0)
#define O_MM(ACC, f, a) do { ACC[0] = MFMA32(f[0], a, ACC[0]); SBAR(); ACC[1] = MFMA32(f[1], a, ACC[1]); SBAR(); } while (0)
#define O_STAGE(st, ACC, VM, PRE, POST) do { \
      rdw(wf[2], st, 2); SBAR(); O_MM(ACC, wf[0], of[(st) % 5][0]); \
      rdw(wf[3], st, 3); SBAR(); O_MM(ACC, wf[1], of[(st) % 5][1]); \
      if ((st) < 15) { \
        OTRACE(3 * (st)); \
        asm volatile("s_waitcnt vmcnt(" #VM ")" : "+v"(of[((st) + 1) % 5][0]), "+v"(of[((st) + 1) % 5][1]), \
                     "+v"(of[((st) + 1) % 5][2]), "+v"(of[((st) + 1) % 5][3]), "+v"(seenA)::"memory"); \
        PRE; \
        OTRACE(3 * (st) + 1); \
        asm volatile("s_barrier" ::: "memory"); \
        OTRACE(3 * (st) + 2); \
        POST; \
        SBAR(); \
        rdw(wf[0], (st) + 1, 0); \
      } \
      SBAR(); O_MM(ACC, wf[2], of[(st) % 5][2]); \
      if ((st) < 15) rdw(wf[1], (st) + 1, 1); \
      SBAR(); O_MM(ACC, wf[3], of[(st) % 5][3]); \
    } while (0)
#define OW(st) (pieceW(st, 0), pieceW(st, 1))
#define OL(st) loadO(st, of[(st) % 5])
    O_STAGE(0, accB, 11, NOP, OL(4));
    O_STAGE(1, accB, 4, NOP, { if (tid == 0) flag_set(ws + WS_FOA + (qp * 8 + hg) * 8, tag);   OW(6); OL(5); });
    O_STAGE(2, accB, 10, NOP, (OW(7), OL(6)));
    O_STAGE(3, accB, 12, NOP, (flag_peek(seenA, foa, ws + WS_FOA), OW(8), OL(7)));
    O_STAGE(4, accB, 13, NOP, OW(9));
    O_STAGE(5, accB, 8, if (w == 0) flag_wait(seenA, foa, ws + WS_FOA, tag),
            (gload4_sc1(g, (wm * 32 + r) * 4, ws + WS_G + mb * 512), OW(10), OL(8), OL(9)));
    O_STAGE(6, accB, 13, NOP, (OW(11), OL(10)));
    O_STAGE(7, accB, 10, NOP, (OW(12), OL(11)));
    O_STAGE(8, accA, 12, NOP, (OW(13), OL(12)));
    O_STAGE(9, accA, 12, NOP, (flag_peek(all0, lane * 8, ws + WS_FOA), flag_peek(all1, lane * 8, ws + WS_FOA + 512),
                                     flag_peek(all2, lane * 8, ws + WS_FOA + 1024), flag_peek(all3, lane * 8, ws + WS_FOA + 1536), OW(14), OL(13)));
    O_STAGE(10, accA, 16, NOP, (OW(15), OL(14)));
    O_STAGE(11, accA, 16, NOP, OL(15));
    O_STAGE(12, accA, 10, NOP, NOP);
    O_STAGE(13, accA, 4, NOP, NOP);
    O_STAGE(14, accA, 0, NOP, NOP);
    O_STAGE(15, accA, 0, NOP, NOP);
    asm volatile("" : "+v"(g), "+v"(all0), "+v"(all1), "+v"(all2), "+v"(all3));
    if (b == 0 && w == 0) {
      flag_wait(all0, lane * 8, ws + WS_FOA, tag); flag_wait(all1, lane * 8, ws + WS_FOA + 512, tag);
      flag_wait(all2, lane * 8, ws + WS_FOA + 1024, tag); flag_wait(all3, lane * 8, ws + WS_FOA + 1536, tag);
      if (lane == 0) flag_set(ws + WS_NONCE, tag);
    }
    STAMP(10);
    float* xw = (float*)(lds + XCH_OFF) + w * 1024 + lane * 4;
    f32x16 acc[2];
#pragma unroll
    for (int j = 0; j < 2; ++j)
#pragma unroll
      for (int i = 0; i < 16; ++i) acc[j][i] = g * accA[j][i] + (1.f - g) * accB[j][i];
    const f32x16& as = kh ? acc[0] : acc[1];
#pragma unroll
    for (int g = 0; g < 4; ++g) *(f32x4*)(xw + g * 256) = (f32x4){as[4 * g], as[4 * g + 1], as[4 * g + 2], as[4 * g + 3]};
    asm volatile("s_waitcnt lgkmcnt(0)\n\ts_barrier" ::: "memory");
    f32x16 t = kh ? acc[1] : acc[0];
    const float* xr = (const float*)(lds + XCH_OFF) + (w ^ 4) * 1024 + lane * 4;
#pragma unroll
    for (int g = 0; g < 4; ++g) {
      f32x4 v = *(const f32x4*)(xr + g * 256);
#pragma unroll
      for (int e = 0; e < 4; ++e) t[4 * g + e] += v[e];
    }
    const int row = mb * 128 + wm * 32 + r;
    char* dst = out + (size_t)row * 2048 + (nb * 64 + kh * 32) * 2;
#pragma unroll
    for (int g = 0; g < 4; g += 2) {
      u32x2 a = {cvtpk(t[4 * g], t[4 * g + 1]), cvtpk(t[4 * g + 2], t[4 * g + 3])};
      u32x2 c = {cvtpk(t[4 * g + 4], t[4 * g + 5]), cvtpk(t[4 * g + 6], t[4 * g + 7])};
      auto x = __builtin_amdgcn_permlane32_swap(a[0], c[0], false, false);
      auto y = __builtin_amdgcn_permlane32_swap(a[1], c[1], false, false);
      *(u32x4*)(dst + g * 16 + h * 16) = (u32x4){x[0], y[0], x[1], y[1]};
    }
  }
  STAMP(11);
  STAMP_OUT(0, ws); TRACE_OUT(ws);
}

extern "C" void kernel_launch(void* const* d_in, const int* in_sizes, int n_in, void* d_out, int out_size, void* d_ws,
                              size_t ws_size, hipStream_t stream) {
  const char* xa = (const char*)d_in[0];
  const char* wq = (const char*)d_in[1];
  const char* wk = (const char*)d_in[2];
  const char* wv = (const char*)d_in[3];
  const char* woa = (const char*)d_in[4];
  const char* wqkv = (const char*)d_in[6];
  const char* wob = (const char*)d_in[7];
  const char* gw = (const char*)d_in[8];
  (void)hipFuncSetAttribute((const void*)k_fused, hipFuncAttributeMaxDynamicSharedMemorySize, R_LDS);
  k_fused<<<256, 512, R_LDS, stream>>>(xa, wq, wk, wv, woa, wqkv, wob, gw, (char*)d_ws, (char*)d_out);
}
```

```cpp
#define CODE_LINES 400
#include <hip/hip_runtime.h>
#include <hip/hip_bf16.h>
#include <stdint.h>

typedef float f32x16 __attribute__((ext_vector_type(16)));
typedef float f32x4 __attribute__((ext_vector_type(4)));
typedef float f32x2 __attribute__((ext_vector_type(2)));
typedef short bf16x8 __attribute__((ext_vector_type(8)));
typedef short s16x4 __attribute__((ext_vector_type(4)));
typedef __bf16 bf16x2 __attribute__((ext_vector_type(2)));
typedef unsigned u32x4 __attribute__((ext_vector_type(4)));
typedef unsigned u32x2 __attribute__((ext_vector_type(2)));
typedef __attribute__((address_space(3))) const char* lds_cptr;
typedef __attribute__((address_space(3))) char* lds_ptr;

#define DI __device__ __forceinline__
#define SBAR() __builtin_amdgcn_sched_barrier(0)
#define PIN(x) asm volatile("" : "+v"(x))
#define MFMA32(a, b, c) __builtin_amdgcn_mfma_f32_32x32x16_bf16(a, b, c, 0, 0, 0)
#define MFMA16(a, b, c) __builtin_amdgcn_mfma_f32_16x16x32_bf16(a, b, c, 0, 0, 0)

constexpr int SEQ = 2048, DM = 1024;
constexpr float QSCALE = 0.17677669529663687f * 1.4426950408889634f;

constexpr size_t WS_KVA = 4u << 20;
constexpr size_t WS_G = WS_KVA + (256u << 10);
constexpr size_t WS_NONCE = WS_G + 8192;
constexpr size_t WS_FKV = WS_NONCE + 1024;
constexpr size_t WS_FPB = WS_FKV + 1024;
constexpr size_t WS_FOA = WS_FPB + 2048;
constexpr size_t WS_FOB = WS_FOA + 2048;
constexpr size_t WS_QB = 5u << 20;
constexpr size_t WS_KVB = 9u << 20;
constexpr size_t WS_O = 17u << 20;

#define TRACE(i) do {} while (0)
#define TRACE_DECL
#define TRACE_OUT(wsp) do {} while (0)
#define STAMP_ARG
#define STAMP_PASS
#define STAMP_DECL
#define STAMP(i) do {} while (0)
#define STAMP_OUT(k, wsp) do {} while (0)
DI unsigned rfl(unsigned x) { return __builtin_amdgcn_readfirstlane(x); }
DI unsigned cvtpk(float lo, float hi) {
  f32x2 v = {lo, hi};
  bf16x2 b = __builtin_convertvector(v, bf16x2);
  return __builtin_bit_cast(unsigned, b);
}
DI bf16x8 pack8(const f32x16& a, int o) {
  u32x4 r = {cvtpk(a[o], a[o + 1]), cvtpk(a[o + 2], a[o + 3]), cvtpk(a[o + 4], a[o + 5]), cvtpk(a[o + 6], a[o + 7])};
  return __builtin_bit_cast(bf16x8, r);
}
DI bf16x8 pack8s(const f32x16& a, int o, float s) {
  u32x4 r = {cvtpk(a[o] * s, a[o + 1] * s), cvtpk(a[o + 2] * s, a[o + 3] * s), cvtpk(a[o + 4] * s, a[o + 5] * s),
             cvtpk(a[o + 6] * s, a[o + 7] * s)};
  return __builtin_bit_cast(bf16x8, r);
}
template <int OFF>
DI void glds16(unsigned voff, const void* sbase, unsigned lds_base) {
  asm volatile("s_mov_b32 m0, %2\n\ts_nop 0\n\tglobal_load_lds_dwordx4 %0, %1 offset:%3"
               :: "v"(voff), "s"(sbase), "s"(rfl(lds_base)), "i"(OFF) : "memory");
}
template <int OFF>
DI void glds16_sc1(unsigned voff, const void* sbase, unsigned lds_base) {
  asm volatile("s_mov_b32 m0, %2\n\ts_nop 0\n\tglobal_load_lds_dwordx4 %0, %1 offset:%3 sc1"
               :: "v"(voff), "s"(sbase), "s"(rfl(lds_base)), "i"(OFF) : "memory");
}
DI bf16x8 lds_rd128(lds_cptr p) { return *(const __attribute__((address_space(3))) bf16x8*)p; }
DI s16x4 lds_tr(lds_cptr p) {
  typedef short v4i16_t __attribute__((ext_vector_type(4)));
  return __builtin_bit_cast(s16x4, __builtin_amdgcn_ds_read_tr16_b64_v4i16((__attribute__((address_space(3))) v4i16_t*)p));
}
DI bf16x8 cat8(s16x4 lo, s16x4 hi) { return (bf16x8){lo[0], lo[1], lo[2], lo[3], hi[0], hi[1], hi[2], hi[3]}; }
DI float bf2f(unsigned short b) { return __uint_as_float((unsigned)b << 16); }

template <int OFF>
DI void gload16_sc1(bf16x8& d, unsigned voff, const void* sbase) {
  asm volatile("global_load_dwordx4 %0, %1, %2 offset:%3 sc1" : "=v"(d) : "v"(voff), "s"(sbase), "i"(OFF) : "memory");
}
template <int OFF>
DI void gstore16_sc1(unsigned voff, void* sbase, const bf16x8& d) {
  asm volatile("global_store_dwordx4 %0, %1, %2 offset:%3 sc1\n\ts_nop 1" :: "v"(voff), "v"(d), "s"(sbase), "i"(OFF) : "memory");
}
DI void gload4_sc1(float& d, unsigned voff, const void* sbase) {
  asm volatile("global_load_dword %0, %1, %2 sc1" : "=v"(d) : "v"(voff), "s"(sbase) : "memory");
}
DI bf16x8 gload16_now(const char* p) {
  bf16x8 d;
  asm volatile("global_load_dwordx4 %0, %1, off sc1\n\ts_waitcnt vmcnt(0)" : "=&v"(d) : "v"(p) : "memory");
  return d;
}
typedef __attribute__((address_space(1))) unsigned long long gu64;
DI void flag_set(char* p, u32x2 tag) {
  asm volatile("global_store_dwordx2 %0, %1, %2 sc1\n\ts_nop 1" :: "v"(0u), "v"(tag), "s"(p) : "memory");
}
DI void flag_peek(u32x2& d, unsigned voff, const void* sbase) {
  asm volatile("global_load_dwordx2 %0, %1, %2 sc1" : "+v"(d) : "v"(voff), "s"(sbase) : "memory");
}
DI void flag_wait(u32x2 seen, unsigned voff, const char* sbase, u32x2 tag) {
  if (__builtin_expect(__all(seen[0] == tag[0] && seen[1] == tag[1]), 1)) return;
  const unsigned long long want = ((unsigned long long)tag[1] << 32) | tag[0];
  for (unsigned spins = 0; spins < (1u << 21); ++spins) {
    if (__all(__hip_atomic_load((gu64*)(sbase + voff), __ATOMIC_RELAXED, __HIP_MEMORY_SCOPE_AGENT) == want)) return;
    __builtin_amdgcn_s_sleep(4);
  }
}

DI void nonce_load(u32x2& nonce, const char* ws) {
  asm volatile("global_load_dwordx2 %0, %1, %2 sc1" : "=v"(nonce) : "v"(0u), "s"(ws + WS_NONCE) : "memory");
}
DI u32x2 nonce_tag(u32x2 nonce) {
  u32x2 tag;
  tag[0] = nonce[0] + 1u;
  tag[1] = nonce[1] + (tag[0] == 0u ? 1u : 0u);
  return tag;
}

constexpr int Q_OFF = 49152, Q_SLOT = 24576, QX_OFF = 32768;
struct QGemm {
  const char* xs; const char* wsrc; unsigned xo, wo[2], lds0; int w;
  DI void init(const char* xa, const char* wq, int qp, int hg, unsigned lds0_, int w_, int lane) {
    xs = xa + (size_t)qp * 64 * 2048; wsrc = wq + hg * 256; lds0 = lds0_; w = w_;
    const int row = 8 * w + (lane >> 3);
    xo = row * 2048 + (((lane & 7) ^ ((row >> 1) & 7)) << 4);
#pragma unroll
    for (int e = 0; e < 2; ++e) {
      const int krow = 8 * w + 4 * e + (lane >> 4);
      wo[e] = krow * 2048 + (((lane & 15) ^ ((krow & 3) << 2)) << 4);
    }
  }
  DI void piece(int st, int n) const {
    const unsigned lb = lds0 + Q_OFF + (st & 3) * Q_SLOT;
    if (n == 0) glds16<0>(xo, xs + st * 128, lb + w * 1024);
    else glds16<0>(wo[n - 1], wsrc + (size_t)st * 64 * 2048, lb + 8192 + (2 * w + n - 1) * 1024);
  }
  DI void stage(int st) const { piece(st, 0); piece(st, 1); piece(st, 2); }
};

constexpr int P_SLOT = 49152;
constexpr int P_XOFF = 0, P_WOFF = 16384;
struct PFrag { bf16x8 x[2]; bf16x8 w[2]; };

template <bool VT>
DI void proj_b(const char* __restrict__ xg, const char* __restrict__ wg, const char* __restrict__ ws, int mb, int nb, char* lds,
               const QGemm& qg, u32x2& tag, bf16x8 (&held)[8] STAMP_ARG) {
  constexpr int ldw_bytes = 6144;
  const int tid = threadIdx.x, lane = tid & 63;
  const int w = rfl(tid >> 6);
  const int wm = w & 1, wn = w >> 1;
  const int r = lane & 31, h = lane >> 5;
  const unsigned lds0 = (unsigned)(uintptr_t)lds;
  u32x2 nonce;
  nonce_load(nonce, ws);
  unsigned xo[2], wo[4];
#pragma unroll
  for (int e = 0; e < 2; ++e) {
    const int row = 16 * w + 8 * e + (lane >> 3);
    const int c = (lane & 7) ^ (4 * e + (lane >> 4));
    xo[e] = row * 2048 + c * 16;
  }
#pragma unroll
  for (int e = 0; e < 4; ++e) {
    const int krow = 8 * w + 2 * e + (lane >> 5);
    const int c = (lane & 31) ^ ((krow & 3) << 2);
    wo[e] = krow * ldw_bytes + c * 16;
  }
  const char* xsrc = xg + (size_t)mb * 128 * 2048;
  auto piece = [&](int pc, int st, int slot) {
    const unsigned lb = lds0 + slot * P_SLOT;
    if (pc < 2) glds16<0>(xo[pc], xsrc + st * 128, lb + P_XOFF + w * 2048 + pc * 1024);
    else glds16<0>(wo[pc - 2], wg + (size_t)st * 64 * ldw_bytes, lb + P_WOFF + w * 4096 + (pc - 2) * 1024);
  };
  auto dma = [&](int st, int slot) {
#pragma unroll
    for (int pc = 0; pc < 6; ++pc) piece(pc, st, slot);
  };
  auto win = [&](int j, int pc, int slot) {
    if (j < 0) return;
    if (j <= 12) piece(pc, j + 3, slot);
    else if (j <= 14) qg.piece((j - 13) * 2 + pc / 3, pc % 3);
  };
  const int t3 = (r >> 1) & 7;
  const int xa0 = P_XOFF + (wm * 64 + r) * 128 + ((h ^ t3) << 4);
  const int g16 = lane >> 4, q4 = (lane & 15) >> 2, p4 = lane & 3;
  const int wc = wn * 8 + 2 * (g16 & 1) + (p4 >> 1);
  const int wa0 = P_WOFF + (8 * h + q4) * 512 + ((wc ^ (q4 << 2)) << 4) + 8 * (p4 & 1);

  auto rdx = [&](PFrag& f, int i, int slot, int s) { f.x[i] = lds_rd128((lds_cptr)lds + slot * P_SLOT + (xa0 ^ (s << 5)) + i * 4096); };
  auto rdw = [&](PFrag& f, int j, int slot, int s) {
    lds_cptr p = (lds_cptr)lds + slot * P_SLOT + (wa0 ^ (j << 6)) + s * 8192;
    f.w[j] = cat8(lds_tr(p), lds_tr(p + 2048));
  };
  f32x16 acc[2][2] = {};
  auto mm = [&](const PFrag& f, int i, int j) {
    if constexpr (VT) acc[i][j] = MFMA32(f.x[i], f.w[j], acc[i][j]);
    else acc[i][j] = MFMA32(f.w[j], f.x[i], acc[i][j]);
  };
  auto step = [&](const PFrag& c, PFrag& n, int rslot, int rs, bool rd, int pa, int wj, int dslot) {
    mm(c, 0, 0); SBAR();
    if (rd) { rdw(n, 0, rslot, rs); rdx(n, 0, rslot, rs); }
    SBAR();
    mm(c, 0, 1); SBAR();
    if (rd) { rdw(n, 1, rslot, rs); rdx(n, 1, rslot, rs); }
    SBAR();
    mm(c, 1, 0); SBAR();
    if (pa >= 0) win(wj, pa, dslot);
    SBAR();
    mm(c, 1, 1); SBAR();
    if (pa >= 0) win(wj, pa + 1, dslot);
    SBAR();
  };

  dma(0, 0);
  asm volatile("s_barrier" ::: "memory");
  dma(1, 1); dma(2, 2);
  asm volatile("s_waitcnt vmcnt(12)\n\ts_barrier" : "+v"(nonce)::"memory");
  tag = nonce_tag(nonce);
  STAMP(1);
  PFrag f0, f1;
  rdw(f0, 0, 0, 0); rdx(f0, 0, 0, 0); rdw(f0, 1, 0, 0); rdx(f0, 1, 0, 0);
  int slot = 0, pslot = 0;
  for (int it = 0; it < 16; ++it) {
    const int nslot = slot == 2 ? 0 : slot + 1;
    step(f0, f1, slot, 1, true, 2, it - 1, pslot);
    step(f1, f0, slot, 2, true, 4, it - 1, pslot);
    step(f0, f1, slot, 3, true, -1, 0, 0);
    if (it < 15) asm volatile("s_waitcnt vmcnt(6) lgkmcnt(0)\n\ts_barrier" ::: "memory");
    else asm volatile("s_waitcnt lgkmcnt(0)\n\ts_barrier" ::: "memory");
    SBAR();
    step(f1, f0, nslot, 0, it < 15, 0, it, slot);
    pslot = slot;
    slot = nslot;
  }
  STAMP(2);
  const float sc = (!VT && nb < 4) ? QSCALE : 1.f;
#pragma unroll
  for (int i = 0; i < 2; ++i)
#pragma unroll
    for (int j = 0; j < 2; ++j) {
      held[(i * 2 + j) * 2] = pack8s(acc[i][j], 0, sc);
      held[(i * 2 + j) * 2 + 1] = pack8s(acc[i][j], 8, sc);
    }
}

DI void proj_kv(const char* __restrict__ xg, const char* __restrict__ wk, const char* __restrict__ wv, const char* __restrict__ gw,
                char* __restrict__ ws, int rt, char* lds, const QGemm& qg, u32x2& tag, const char* wq STAMP_ARG) {
  const int tid = threadIdx.x, lane = tid & 63;
  const int w = rfl(tid >> 6);
  const int r = lane & 31, h = lane >> 5;
  const unsigned lds0 = (unsigned)(uintptr_t)lds;
  u32x2 nonce;
  nonce_load(nonce, ws);
  const int xrow = 4 * w + (lane >> 4);
  const unsigned xo = xrow * 2048 + (((lane & 15) ^ (xrow & 15)) << 4), wo = w * 1024 + lane * 16;
  const char* xs = xg + (size_t)rt * 32 * 2048;
  auto stage = [&](int st) {
    const unsigned lb = lds0 + (st % 6) * 24576 + w * 1024;
    glds16<0>(xo, xs + st * 256, lb);
    glds16<0>(wo, wk + st * 8192, lb + 8192);
    glds16<0>(wo, wv + st * 8192, lb + 16384);
  };
#pragma unroll
  for (int st = 0; st < 6; ++st) stage(st);
  const int t = w & 1, kq = w >> 1;
  const int g16 = lane >> 4, q4 = (lane & 15) >> 2, p4 = lane & 3;
  const int xa = r * 256, wa = 8192 + t * 8192 + (32 * kq + 8 * h + q4) * 64 + 32 * (g16 & 1) + 8 * p4;
  f32x16 acc = {};
#define KV_STAGE(st, VM) do { \
    asm volatile("s_waitcnt vmcnt(" #VM ") lgkmcnt(0)\n\ts_barrier" : "+v"(nonce)::"memory"); \
    if ((st) >= 1 && (st) + 5 < 8) stage((st) + 5); \
    lds_cptr b = (lds_cptr)lds + ((st) % 6) * 24576; \
    _Pragma("unroll") for (int e = 0; e < 2; ++e) { \
      const bf16x8 xf = lds_rd128(b + xa + (((4 * kq + 2 * e + h) ^ (r & 15)) << 4)); \
      const bf16x8 wf = cat8(lds_tr(b + wa + e * 1024), lds_tr(b + wa + e * 1024 + 256)); \
      if (t) acc = MFMA32(xf, wf, acc); else acc = MFMA32(wf, xf, acc); \
    } \
  } while (0)
  KV_STAGE(0, 15); KV_STAGE(1, 12); KV_STAGE(2, 12); KV_STAGE(3, 12); KV_STAGE(4, 9); KV_STAGE(5, 6); KV_STAGE(6, 3); KV_STAGE(7, 0);
#undef KV_STAGE
  tag = nonce_tag(nonce);
  STAMP(1);
  asm volatile("s_waitcnt lgkmcnt(0)\n\ts_barrier" ::: "memory");
  float* red = (float*)lds;
#pragma unroll
  for (int g = 0; g < 4; ++g)
    *(f32x4*)(red + (w * 4 + g) * 256 + lane * 4) = (f32x4){acc[4 * g], acc[4 * g + 1], acc[4 * g + 2], acc[4 * g + 3]};
  asm volatile("s_waitcnt lgkmcnt(0)\n\ts_barrier" ::: "memory");
#pragma unroll
  for (int st = 0; st < 4; ++st) qg.stage(st);
  if (w < 2) {
    f32x16 tt;
#pragma unroll
    for (int g = 0; g < 4; ++g) {
      f32x4 a = *(const f32x4*)(red + ((w + 0) * 4 + g) * 256 + lane * 4);
      f32x4 b = *(const f32x4*)(red + ((w + 2) * 4 + g) * 256 + lane * 4);
      f32x4 c = *(const f32x4*)(red + ((w + 4) * 4 + g) * 256 + lane * 4);
      f32x4 d = *(const f32x4*)(red + ((w + 6) * 4 + g) * 256 + lane * 4);
#pragma unroll
      for (int e = 0; e < 4; ++e) tt[4 * g + e] = (a[e] + b[e]) + (c[e] + d[e]);
    }
    char* dst = ws + WS_KVA + (size_t)rt * 4096 + w * 2048;
    gstore16_sc1<0>(lane * 16, dst, pack8(tt, 0));
    gstore16_sc1<1024>(lane * 16, dst, pack8(tt, 8));
  }
  {
    const u32x4 gw0 = *(const u32x4*)(gw + lane * 32), gw1 = *(const u32x4*)(gw + lane * 32 + 16);
#pragma unroll
    for (int i = 0; i < 4; ++i) {
      const char* px = xs + (size_t)(4 * w + i) * 2048 + lane * 32;
      const u32x4 gx0 = *(const u32x4*)px, gx1 = *(const u32x4*)(px + 16);
      float z = 0.f;
#pragma unroll
      for (int e = 0; e < 4; ++e) {
        z += __uint_as_float(gx0[e] << 16) * __uint_as_float(gw0[e] << 16) + __uint_as_float(gx0[e] & 0xffff0000u) * __uint_as_float(gw0[e] & 0xffff0000u);
        z += __uint_as_float(gx1[e] << 16) * __uint_as_float(gw1[e] << 16) + __uint_as_float(gx1[e] & 0xffff0000u) * __uint_as_float(gw1[e] & 0xffff0000u);
      }
#pragma unroll
      for (int o = 32; o >= 1; o >>= 1) z += __shfl_xor(z, o);
      const float g = 1.f / (1.f + __expf(-z));
      if (lane == 0) asm volatile("global_store_dword %0, %1, %2 sc1\n\ts_nop 0" :: "v"(0u), "v"(g), "s"(ws + WS_G + (rt * 32 + 4 * w + i) * 4) : "memory");
    }
  }
  asm volatile("s_waitcnt vmcnt(0) lgkmcnt(0)\n\ts_barrier" ::: "memory");
  if (tid == 0) flag_set(ws + WS_FKV + rt * 8, tag);
  {
    unsigned j0 = 0, j1 = 0;
    const int xc = rt >> 3, n0 = (((rt & 7) * 8 + w) * 2) * 48 + (lane < 48 ? lane : 47), n1 = n0 + 48;
    asm volatile("global_load_dword %0, %2, %4\n\tglobal_load_dword %1, %3, %4\n\ts_waitcnt vmcnt(0)" : "+v"(j0), "+v"(j1)
                 : "v"((n0 / 6) * 2048 + (n0 % 6) * 128), "v"((n1 / 6) * 2048 + (n1 % 6) * 128), "s"(wq + (xc >> 2) * 1024) : "memory");
  }
  STAMP(2);
}

constexpr int SL = 32768;
constexpr int R_LDS = 5 * SL;
constexpr int XCH_OFF = 0;
constexpr int WO_OFF = 2 * SL;

DI int crow(int r, int h) { return (r & 3) + 8 * (r >> 2) + 4 * h; }
DI float swap_half(float x) {
  auto rr = __builtin_amdgcn_permlane32_swap(__float_as_uint(x), __float_as_uint(x), false, false);
  return __uint_as_float((threadIdx.x & 32) ? rr[0] : rr[1]);
}
DI void exp16(f32x16& s) {
#pragma unroll
  for (int i = 0; i < 16; ++i) s[i] = __builtin_amdgcn_exp2f(s[i]);
}
#define AE(S, i) S[i] = __builtin_amdgcn_exp2f(S[i])
#define AC(P, S, i) P[(i) & 3] = cvtpk(S[2 * (i)], S[2 * (i) + 1])
#define ABC(x) __builtin_bit_cast(bf16x8, x)
#define APHASE(QK, PV, SX, PXa, PXb, SY, CINY, PYa, PYb, QY0, QY1, OY, LY, KF0, KF1, VF0, VF1, R1, R2) do { \
  if (QK) { SY = MFMA32(KF0, QY0, CINY); PIN(SY); } SBAR(); \
  AE(SX, 0); AE(SX, 1); AE(SX, 2); AE(SX, 3); PIN(SX); SBAR(); \
  if (QK) { SY = MFMA32(KF1, QY1, SY); PIN(SY); } SBAR(); \
  R1; \
  AC(PXa, SX, 0); AC(PXa, SX, 1); AE(SX, 4); AE(SX, 5); AE(SX, 6); PIN(SX); PIN(PXa); SBAR(); \
  if (PV) { OY = MFMA32(VF0, ABC(PYa), OY); PIN(OY); } SBAR(); \
  AE(SX, 7); AC(PXa, SX, 2); AE(SX, 8); AE(SX, 9); AC(PXa, SX, 3); PIN(SX); PIN(PXa); SBAR(); \
  if (PV) { LY = MFMA16(ones, ABC(PYa), LY); PIN(LY); } SBAR(); \
  R2; \
  AE(SX, 10); AE(SX, 11); PIN(SX); SBAR(); \
  if (PV) { OY = MFMA32(VF1, ABC(PYb), OY); PIN(OY); } SBAR(); \
  AC(PXb, SX, 4); AC(PXb, SX, 5); AE(SX, 12); AE(SX, 13); AE(SX, 14); PIN(SX); PIN(PXb); SBAR(); \
  if (PV) { LY = MFMA16(ones, ABC(PYb), LY); PIN(LY); } SBAR(); \
  AE(SX, 15); AC(PXb, SX, 6); AC(PXb, SX, 7); PIN(PXb); SBAR(); \
} while (0)

template <bool MASKED>
DI void attn_exact(const char* __restrict__ kv, int t0, int t1, int qt, const bf16x8& q0, const bf16x8& q1, const bf16x8& ones,
                   f32x16& o, float& l) {
  const int lane = threadIdx.x & 63, r = lane & 31, h = lane >> 5;
  float m = -3.0e38f;
  for (int t = t0; t < t1; ++t) {
    const char* p = kv + (size_t)t * 4096 + lane * 16;
    bf16x8 k0 = gload16_now(p), k1 = gload16_now(p + 1024);
    f32x16 s = {};
    s = MFMA32(k0, q0, s); s = MFMA32(k1, q1, s);
#pragma unroll
    for (int i = 0; i < 16; ++i) {
      bool ok = true;
      if (MASKED) { const int c = crow(i, h); ok = (t == qt) ? (c <= r) : ((t == qt - 8) ? (c > r) : true); }
      m = fmaxf(m, ok ? s[i] : -3.0e38f);
    }
  }
  m = fmaxf(m, swap_half(m));
  o = (f32x16){};
  f32x4 la = {};
  for (int t = t0; t < t1; ++t) {
    const char* p = kv + (size_t)t * 4096 + lane * 16;
    bf16x8 k0 = gload16_now(p), k1 = gload16_now(p + 1024);
    bf16x8 v0 = gload16_now(p + 2048), v1 = gload16_now(p + 3072);
    f32x16 s = {};
    s = MFMA32(k0, q0, s); s = MFMA32(k1, q1, s);
#pragma unroll
    for (int i = 0; i < 16; ++i) {
      bool ok = true;
      if (MASKED) { const int c = crow(i, h); ok = (t == qt) ? (c <= r) : ((t == qt - 8) ? (c > r) : true); }
      s[i] = ok ? __builtin_amdgcn_exp2f(s[i] - m) : 0.f;
    }
    bf16x8 pa = pack8(s, 0), pb = pack8(s, 8);
    o = MFMA32(v0, pa, o); o = MFMA32(v1, pb, o);
    la = MFMA16(ones, pa, la); la = MFMA16(ones, pb, la);
  }
  l = la[0];
}
DI bool ref_unsafe(const f32x16& o, float l) {
  float z = 0.f;
#pragma unroll
  for (int i = 0; i < 16; ++i) z = __builtin_fmaf(o[i], 0.f, z);
  const bool ok = (l >= 5.421010862e-20f) && (l <= 1.8446744e19f) && (z == 0.f);
  return __any(!ok);
}

__global__ __launch_bounds__(512, 2) void k_fused(const char* __restrict__ xa, const char* __restrict__ wq,
                                                  const char* __restrict__ wk, const char* __restrict__ wv,
                                                  const char* __restrict__ woa, const char* __restrict__ wqkv,
                                                  const char* __restrict__ wob, const char* __restrict__ gw,
                                                  char* __restrict__ ws, char* __restrict__ out) {
  extern __shared__ __attribute__((aligned(16))) char lds[];
  STAMP_DECL STAMP(0); TRACE_DECL
  const int b = blockIdx.x;
  const int xcd = b & 7, idx = b >> 3;
  const int tid = threadIdx.x, lane = tid & 63;
  const int w = rfl(tid >> 6);
  const int r = lane & 31, h = lane >> 5;
  const unsigned lds0 = (unsigned)(uintptr_t)lds;
  const unsigned lo16 = lane * 16;
  const int qp = (xcd & 3) * 8 + (idx & 7), hg = (xcd >> 2) * 4 + (idx >> 3);
  QGemm qg;
  qg.init(xa, wq, qp, hg, lds0, w, lane);
  u32x2 tag;
  bf16x8 held[8];
  const int pmb = (xcd & 1) * 8 + (idx & 7), pnb = (idx >> 3) * 4 + (xcd >> 1);
  const bool isB = pnb < 12;
  if (pnb < 8) proj_b<false>(xa, wqkv + pnb * 512, ws, pmb, pnb, lds, qg, tag, held STAMP_PASS);
  else if (pnb < 12) proj_b<true>(xa, wqkv + pnb * 512, ws, pmb, pnb, lds, qg, tag, held STAMP_PASS);
  else {
    proj_kv(xa, wk, wv, gw, ws, xcd * 8 + (idx & 7), lds, qg, tag, wq STAMP_PASS);
#pragma unroll
    for (int n = 0; n < 8; ++n) held[n] = (bf16x8){};
  }
  STAMP(3);

  const unsigned hstep = pnb < 4 ? 2048 : 4096;
  char* hbase = ws + (pnb < 4 ? WS_QB : WS_KVB + (pnb >= 8 ? 2048 : 0)) +
                (size_t)(((pnb & 3) * 8 + (w >> 1) * 2) * 64 + pmb * 4 + (w & 1) * 2) * hstep;
  auto storeH = [&](int n) {
    if (isB) {
      char* dst = hbase + (size_t)((n >> 2) + ((n >> 1) & 1) * 64) * hstep;
      if (n & 1) gstore16_sc1<1024>(lo16, dst, held[n]);
      else gstore16_sc1<0>(lo16, dst, held[n]);
    }
  };
  const int pr = w & 3, half = w >> 2;
  const int hA = 4 * hg + pr;
  const int hB = (b & 7) + 8 * (b >> 6), rb = (b >> 3) & 7;
  const int qtB = rb * 8 + w;
  const int mb = (xcd & 3) * 4 + (idx & 3), nb = (xcd >> 2) * 8 + (idx >> 2);
  const char* kva = ws + WS_KVA;
  const char* kvb = ws + WS_KVB + (size_t)hB * 64 * 4096;
  bf16x8 ones;
  {
    const short v = ((((lane >> 4) & 1) == (((lane & 15) >> 2) & 1)) ? (short)0x3f80 : (short)0);
    ones = (bf16x8){v, v, v, v, v, v, v, v};
  }
  auto pieceA = [&](int k, int sl, int i) {
    const char* src = kva + (size_t)(half * 32 + 4 * k + pr) * 4096;
    const unsigned dst = lds0 + sl * SL + w * 4096;
    if (i == 0) glds16_sc1<0>(lo16, src, dst);
    else if (i == 1) glds16_sc1<1024>(lo16, src, dst);
    else if (i == 2) glds16_sc1<2048>(lo16, src, dst);
    else glds16_sc1<3072>(lo16, src, dst);
  };
  auto pieceB = [&](int n) {
    int gt = rb * 8 - 8 + 2 * w + (n >> 2);
    gt = gt < 0 ? 0 : gt;
    const char* src = kvb + (size_t)gt * 4096;
    const unsigned dst = lds0 + (2 * w + (n >> 2)) * 4096;
    if ((n & 3) == 0) glds16_sc1<0>(lo16, src, dst);
    else if ((n & 3) == 1) glds16_sc1<1024>(lo16, src, dst);
    else if ((n & 3) == 2) glds16_sc1<2048>(lo16, src, dst);
    else glds16_sc1<3072>(lo16, src, dst);
  };
  const char* pqb = ws + WS_QB + (size_t)(hB * 64 + qtB) * 2048;
  unsigned wo[2];
#pragma unroll
  for (int e = 0; e < 2; ++e) {
    const int row = 16 * w + 8 * e + (lane >> 3);
    const int c = (lane & 7) ^ (((row >> 1) & 1) << 2);
    wo[e] = row * 2048 + c * 16;
  }
  auto pieceW = [&](int st, int e) {
    const char* src = (st < 8 ? wob : woa) + (size_t)(st & 7) * 128 * 2048 + nb * 128;
    glds16<0>(wo[e], src, lds0 + WO_OFF + (st % 6) * 16384 + w * 2048 + e * 1024);
  };
#define NOP (void)0
  bf16x8 qb[2], q[2][2];
  u32x2 seen = {~tag[0], 0u};
  {
    const int g16 = lane >> 4, q4 = (lane & 15) >> 2, p4 = lane & 3;
    const int xa0 = Q_OFF + (32 * half + r) * 128 + ((h ^ ((r >> 1) & 7)) << 4);
    const int wa0 = Q_OFF + 8192 + (8 * h + q4) * 256 + (((pr * 4 + 2 * (g16 & 1) + (p4 >> 1)) ^ (q4 << 2)) << 4) + 8 * (p4 & 1);
    lds_cptr xb[2][4], wb[2];
#pragma unroll
    for (int u = 0; u < 2; ++u) {
#pragma unroll
      for (int s = 0; s < 4; ++s) { xb[u][s] = (lds_cptr)lds + u * 2 * Q_SLOT + (xa0 ^ (s << 5)); PIN(xb[u][s]); }
      wb[u] = (lds_cptr)lds + u * 2 * Q_SLOT + wa0; PIN(wb[u]);
    }
    f32x16 acc = {};
#define Q_STAGE(st, VM, PRE, POST) do { \
      asm volatile("s_waitcnt vmcnt(" #VM ") lgkmcnt(0)" : "+v"(seen)::"memory"); \
      PRE; \
      asm volatile("s_barrier" ::: "memory"); \
      bf16x8 xf[4], wf[4]; \
      _Pragma("unroll") for (int s = 0; s < 4; ++s) { \
        lds_cptr pw = wb[((st) >> 1) & 1] + ((st) & 1) * Q_SLOT + s * 4096; \
        xf[s] = lds_rd128(xb[((st) >> 1) & 1][s] + ((st) & 1) * Q_SLOT); \
        wf[s] = cat8(lds_tr(pw), lds_tr(pw + 1024)); \
      } \
      SBAR(); \
      if ((st) >= 1 && (st) + 3 < 16) qg.stage((st) + 3); \
      POST; \
      SBAR(); \
      _Pragma("unroll") for (int s = 0; s < 4; ++s) acc = MFMA32(wf[s], xf[s], acc); \
    } while (0)
    Q_STAGE(0, 9, NOP, NOP);   Q_STAGE(1, 6, NOP, NOP);
    Q_STAGE(2, 6, NOP, flag_peek(seen, lane * 8, ws + WS_FKV));
    Q_STAGE(3, 7, NOP, NOP);   Q_STAGE(4, 7, NOP, NOP);
    Q_STAGE(5, 6, if (w == 0) flag_wait(seen, lane * 8, ws + WS_FKV, tag), (pieceA(0, 0, 0), pieceA(0, 0, 1), pieceA(0, 0, 2), pieceA(0, 0, 3)));
    Q_STAGE(6, 10, NOP, NOP);  Q_STAGE(7, 10, NOP, NOP);  Q_STAGE(8, 10, NOP, NOP);
    Q_STAGE(9, 6, NOP, NOP);   Q_STAGE(10, 6, NOP, NOP);  Q_STAGE(11, 6, NOP, NOP);
    Q_STAGE(12, 6, NOP, NOP);  Q_STAGE(13, 6, NOP, NOP);  Q_STAGE(14, 3, NOP, NOP);
    Q_STAGE(15, 0, NOP, (storeH(0), storeH(1), storeH(2), storeH(3), storeH(4), storeH(5), storeH(6), storeH(7)));
#undef Q_STAGE
    STAMP(4);
    const bf16x8 mine0 = pack8s(acc, 0, QSCALE), mine1 = pack8s(acc, 8, QSCALE);
    typedef __attribute__((address_space(3))) bf16x8* lds_v8;
    *(lds_v8)((lds_ptr)lds + QX_OFF + w * 2048 + lo16) = mine0;
    *(lds_v8)((lds_ptr)lds + QX_OFF + w * 2048 + 1024 + lo16) = mine1;
    asm volatile("s_waitcnt lgkmcnt(0)\n\ts_barrier" ::: "memory");
    const bf16x8 oth0 = lds_rd128((lds_cptr)lds + QX_OFF + (w ^ 4) * 2048 + lo16);
    const bf16x8 oth1 = lds_rd128((lds_cptr)lds + QX_OFF + (w ^ 4) * 2048 + 1024 + lo16);
    q[0][0] = half ? oth0 : mine0; q[0][1] = half ? oth1 : mine1;
    q[1][0] = half ? mine0 : oth0; q[1][1] = half ? mine1 : oth1;
  }

  const f32x16 zero = {};
  f32x16 o0 = {}, o1 = {}, s0, s1;
  f32x4 l0 = {}, l1 = {};
  u32x4 p0a, p0b, p1a, p1b;
  bf16x8 ka0, ka1, kb0, kb1, va0, va1, vb0, vb1;
  lds_cptr kvbase = (lds_cptr)lds + half * 16384 + lane * 16;
#define APAIR(pp, SL0, SL1, FIRST, LAST, PA, PB, MID, D0, D1, D2, D3) do { \
    lds_cptr cur = kvbase + (SL0) * SL + ((pp) & 1) * 8192; PIN(cur);     \
    lds_cptr nxt = (SL0) == (SL1) && !((pp) & 1) ? cur + 8192 : kvbase + (SL1) * SL; \
    __builtin_amdgcn_s_setprio(PA); \
    if (FIRST) { ka0 = lds_rd128(cur); ka1 = lds_rd128(cur + 1024); s0 = MFMA32(ka0, q[0][0], zero); s0 = MFMA32(ka1, q[0][1], s0); } \
    SBAR(); \
    APHASE(1, !(FIRST), s0, p0a, p0b, s1, zero, p1a, p1b, q[1][0], q[1][1], o1, l1, ka0, ka1, vb0, vb1, \
           (kb0 = lds_rd128(cur + 4096), kb1 = lds_rd128(cur + 5120)), (va0 = lds_rd128(cur + 2048), va1 = lds_rd128(cur + 3072))); \
    D0; SBAR(); \
    APHASE(1, 1, s1, p1a, p1b, s0, zero, p0a, p0b, q[0][0], q[0][1], o0, l0, kb0, kb1, va0, va1, (void)0, (void)0); \
    D1; SBAR(); \
    ATRACE(2 * (pp)); \
    MID; \
    ATRACE(2 * (pp) + 1); \
    __builtin_amdgcn_s_setprio(PB); SBAR(); \
    APHASE(1, 1, s0, p0a, p0b, s1, zero, p1a, p1b, q[1][0], q[1][1], o1, l1, kb0, kb1, va0, va1, \
           if (!(LAST)) (ka0 = lds_rd128(nxt), ka1 = lds_rd128(nxt + 1024)), (vb0 = lds_rd128(cur + 4096 + 2048), vb1 = lds_rd128(cur + 4096 + 3072))); \
    D2; SBAR(); \
    APHASE(!(LAST), 1, s1, p1a, p1b, s0, zero, p0a, p0b, q[0][0], q[0][1], o0, l0, ka0, ka1, vb0, vb1, (void)0, (void)0); \
    D3; SBAR(); \
    if (LAST) { \
      o1 = MFMA32(vb0, ABC(p1a), o1); o1 = MFMA32(vb1, ABC(p1b), o1); \
      l1 = MFMA16(ones, ABC(p1a), l1); l1 = MFMA16(ones, ABC(p1b), l1); \
    } \
  } while (0)
#define ATRACE(i) do {} while (0)
#define DA(k, sl, i) pieceA(k, sl, i)
#define DA2(k, sl, i) (pieceA(k, sl, i), pieceA(k, sl, (i) + 1))
#define DB2(n) (pieceB(n), pieceB((n) + 1))
#define DH2(n) (storeH(n), storeH((n) + 1))
#define DW(st, e) pieceW(st, e)
#define BAR_V(n) asm volatile("s_waitcnt vmcnt(" #n ")\n\ts_barrier" ::: "memory")
  STAMP(5);
  const int fbm = 2 * rb - 2 + (lane & 3);
  const unsigned fbo = (((lane >> 2) % 3 * 4 + (hB >> 3)) * 16 + (fbm < 0 ? 2 * rb : fbm)) * 8;
  seen = (u32x2){~tag[0], 0u};
  APAIR(0, 0, 0, 1, 0, 2, 1, NOP, DA2(1, 3, 0), DA2(1, 3, 2), DA2(2, 4, 0), DA2(2, 4, 2));
  APAIR(1, 0, 3, 0, 0, 0, 3, { BAR_V(4); if (isB && tid == 0) flag_set(ws + WS_FPB + (pnb * 16 + pmb) * 8, tag);   },
        NOP, NOP, NOP, NOP);
  APAIR(2, 3, 3, 0, 0, 2, 1, NOP, NOP, NOP, NOP, NOP);
  flag_peek(seen, fbo, ws + WS_FPB);
  APAIR(3, 3, 4, 0, 0, 0, 3, { asm volatile("s_waitcnt vmcnt(0)" : "+v"(seen)::"memory"); if (w == 0) flag_wait(seen, fbo, ws + WS_FPB, tag); asm volatile("s_barrier" ::: "memory"); },
        NOP, NOP, DB2(0), DB2(2));
  APAIR(4, 4, 4, 0, 0, 2, 2, NOP, DB2(4), DB2(6), (gload16_sc1<0>(qb[0], lo16, pqb), gload16_sc1<1024>(qb[1], lo16, pqb)), DA2(3, 2, 0));
  APAIR(5, 4, 4, 0, 1, 1, 0, NOP, DA2(3, 2, 2), NOP, NOP, NOP);
  asm volatile("s_waitcnt vmcnt(0) lgkmcnt(0)\n\ts_barrier" : "+v"(qb[0]), "+v"(qb[1])::"memory");
  STAMP(6);
  {
    f32x16 o = {};
    f32x4 la = {};
    lds_cptr bb = (lds_cptr)lds + w * 4096 + lane * 16;
    f32x16 mD;
#pragma unroll
    for (int i = 0; i < 16; ++i) mD[i] = crow(i, h) <= r ? 0.f : -1.0e30f;
    if (rb != 0) {
      f32x16 sa, sb;
#pragma unroll
      for (int i = 0; i < 16; ++i) sa[i] = -1.0e30f - mD[i];
      u32x4 pa0 = {}, pa1 = {}, pb0 = {}, pb1 = {};
      bf16x8 k0, k1, k2, k3, v0, v1, v2, v3;
      k0 = lds_rd128(bb); k1 = lds_rd128(bb + 1024);
      k2 = lds_rd128(bb + 4096); k3 = lds_rd128(bb + 4096 + 1024);
      sa = MFMA32(k0, qb[0], sa); sa = MFMA32(k1, qb[1], sa);
      SBAR();
#define BRDK(KA, KB, j) (KA = lds_rd128(bb + (j) * 4096), KB = lds_rd128(bb + (j) * 4096 + 1024))
#define BRDV(VA, VB, j) (VA = lds_rd128(bb + (j) * 4096 + 2048), VB = lds_rd128(bb + (j) * 4096 + 3072))
#define B_DMA(n) do { pieceA(4 + (n) / 4, 3 + (n) / 4, (n) & 3); SBAR(); } while (0)
      __builtin_amdgcn_s_setprio(3);
      APHASE(1, 0, sa, pa0, pa1, sb, zero, pb0, pb1, qb[0], qb[1], o, la, k2, k3, v2, v3, BRDK(k0, k1, 2), BRDV(v0, v1, 0));
      B_DMA(0); B_DMA(1);
      APHASE(1, 1, sb, pb0, pb1, sa, zero, pa0, pa1, qb[0], qb[1], o, la, k0, k1, v0, v1, BRDK(k2, k3, 3), BRDV(v2, v3, 1));
      B_DMA(2); B_DMA(3);
      __builtin_amdgcn_s_setprio(2);
      APHASE(1, 1, sa, pa0, pa1, sb, zero, pb0, pb1, qb[0], qb[1], o, la, k2, k3, v2, v3, BRDK(k0, k1, 4), BRDV(v0, v1, 2));
      B_DMA(4); B_DMA(5);
      APHASE(1, 1, sb, pb0, pb1, sa, zero, pa0, pa1, qb[0], qb[1], o, la, k0, k1, v0, v1, BRDK(k2, k3, 5), BRDV(v2, v3, 3));
      B_DMA(6); B_DMA(7);
      __builtin_amdgcn_s_setprio(1);
      APHASE(1, 1, sa, pa0, pa1, sb, zero, pb0, pb1, qb[0], qb[1], o, la, k2, k3, v2, v3, BRDK(k0, k1, 6), BRDV(v0, v1, 4));
      APHASE(1, 1, sb, pb0, pb1, sa, zero, pa0, pa1, qb[0], qb[1], o, la, k0, k1, v0, v1, BRDK(k2, k3, 7), BRDV(v2, v3, 5));
      __builtin_amdgcn_s_setprio(0);
      APHASE(1, 1, sa, pa0, pa1, sb, zero, pb0, pb1, qb[0], qb[1], o, la, k2, k3, v2, v3, BRDK(k0, k1, 8), BRDV(v0, v1, 6));
      APHASE(1, 1, sb, pb0, pb1, sa, mD, pa0, pa1, qb[0], qb[1], o, la, k0, k1, v0, v1, (void)0, BRDV(v2, v3, 7));
      APHASE(0, 1, sa, pa0, pa1, sb, zero, pb0, pb1, qb[0], qb[1], o, la, k2, k3, v2, v3, (void)0, BRDV(v0, v1, 8));
      o = MFMA32(v0, ABC(pa0), o); o = MFMA32(v1, ABC(pa1), o);
      la = MFMA16(ones, ABC(pa0), la); la = MFMA16(ones, ABC(pa1), la);
    } else {
#pragma unroll
      for (int n = 0; n < 8; ++n) B_DMA(n);
      auto tile = [&](int j, const f32x16& cin) {
        lds_cptr p = bb + j * 4096;
        bf16x8 k0 = lds_rd128(p), k1 = lds_rd128(p + 1024), v0 = lds_rd128(p + 2048), v1 = lds_rd128(p + 3072);
        f32x16 s = MFMA32(k0, qb[0], cin);
        s = MFMA32(k1, qb[1], s);
        exp16(s);
        bf16x8 pa = pack8(s, 0), pb = pack8(s, 8);
        o = MFMA32(v0, pa, o); o = MFMA32(v1, pb, o);
        la = MFMA16(ones, pa, la); la = MFMA16(ones, pb, la);
      };
      for (int j = 8 - w; j < 8; ++j) tile(j, zero);
      tile(8, mD);
    }
    float l = la[0];
    if (__builtin_expect(ref_unsafe(o, l), 0)) attn_exact<true>(kvb, qtB >= 8 ? qtB - 8 : 0, qtB + 1, qtB, qb[0], qb[1], ones, o, l);
    const float sc = __builtin_amdgcn_rcpf(l);
    char* dst = ws + WS_O + (size_t)(qtB * 64 + 32 + hB) * 2048;
    gstore16_sc1<0>(lo16, dst, pack8s(o, 0, sc));
    gstore16_sc1<1024>(lo16, dst, pack8s(o, 8, sc));
  }
  asm volatile("s_waitcnt vmcnt(2) lgkmcnt(0)\n\ts_barrier" ::: "memory");
  STAMP(7);
  const int wm = w & 3, kh = w >> 2;
  const char* obase = ws + WS_O + (size_t)(mb * 4 + wm) * 64 * 2048 + kh * 4096;
  bf16x8 of[5][4];
  auto loadO1 = [&](int st, int i, bf16x8& f) {
    const char* src = obase + (size_t)(st < 8 ? 32 + 4 * st : 4 * (st - 8)) * 2048;
    if (i == 0) gload16_sc1<0>(f, lo16, src);
    else if (i == 1) gload16_sc1<1024>(f, lo16, src);
    else if (i == 2) gload16_sc1<2048>(f, lo16, src);
    else gload16_sc1<3072>(f, lo16, src);
  };
#define LO(st, i) loadO1(st, i, of[st][i])
  auto loadO = [&](int st, bf16x8 (&f)[4]) {
    const char* src = obase + (size_t)(st < 8 ? 32 + 4 * st : 4 * (st - 8)) * 2048;
    gload16_sc1<0>(f[0], lo16, src); gload16_sc1<1024>(f[1], lo16, src);
    gload16_sc1<2048>(f[2], lo16, src); gload16_sc1<3072>(f[3], lo16, src);
  };
  const unsigned fob = ((mb >> 1) * 32 + (lane & 31)) * 8;
  seen = (u32x2){~tag[0], 0u};
  unsigned warm = 0;
  APAIR(6, 2, 2, 1, 0, 3, 3, NOP, DA(6, 0, 0), DA(6, 0, 1), DA(6, 0, 2), DA(6, 0, 3));
  APAIR(7, 2, 3, 0, 0, 3, 2, NOP, DA(7, 1, 0), DA(7, 1, 1), DA(7, 1, 2), DA(7, 1, 3));
  {
    const int R = 768 + (idx & 3) * 320 + w * 40 + (lane < 40 ? lane : 39);
    const char* pw = (R < 1024 ? wob + (size_t)R * 2048 : woa + (size_t)(R - 1024) * 2048) + nb * 128;
    asm volatile("global_load_dword %0, %1, off" : "+v"(warm) : "v"(pw) : "memory");
  }
  APAIR(8, 3, 3, 0, 0, 2, 2, NOP, NOP, NOP, NOP, NOP);
  APAIR(9, 3, 4, 0, 0, 1, 1, NOP, NOP, NOP, NOP, NOP);
  APAIR(10, 4, 4, 0, 0, 1, 0, NOP, NOP, NOP, NOP, NOP);
  APAIR(11, 4, 0, 0, 0, 0, 3, { asm volatile("s_waitcnt vmcnt(0)\n\ts_barrier" : "+v"(warm)::"memory");
                                if (tid == 0) flag_set(ws + WS_FOB + (rb * 32 + hB) * 8, tag);   }, NOP, NOP, NOP, NOP);
  APAIR(12, 0, 0, 0, 0, 3, 2, NOP, DW(0, 0), DW(0, 1), DW(1, 0), DW(1, 1));
  flag_peek(seen, fob, ws + WS_FOB);
  APAIR(13, 0, 1, 0, 0, 2, 1, NOP, DW(2, 0), DW(2, 1), DW(3, 0), DW(3, 1));
  APAIR(14, 1, 1, 0, 0, 0, 3, { asm volatile("s_waitcnt vmcnt(0)" : "+v"(seen)::"memory"); if (w == 0) flag_wait(seen, fob, ws + WS_FOB, tag); asm volatile("s_barrier" ::: "memory"); },
        NOP, NOP, (LO(0, 0), LO(0, 1), DW(4, 0)), (LO(0, 2), LO(0, 3), DW(4, 1)));
  APAIR(15, 1, 1, 0, 1, 2, 1, NOP, (LO(1, 0), LO(1, 1), DW(5, 0)), (LO(1, 2), LO(1, 3), DW(5, 1)), (LO(2, 0), LO(2, 1)), (LO(2, 2), LO(2, 3)));
  STAMP(8);

  loadO(3, of[3]);
  asm volatile("s_waitcnt lgkmcnt(0)\n\ts_barrier" ::: "memory");
  {
    float* xb = (float*)(lds + XCH_OFF) + w * (5 * 256);
    const f32x16& os = half ? o0 : o1;
    const float ls = half ? l0[0] : l1[0];
#pragma unroll
    for (int g = 0; g < 4; ++g) *(f32x4*)(xb + g * 256 + lane * 4) = (f32x4){os[4 * g], os[4 * g + 1], os[4 * g + 2], os[4 * g + 3]};
    xb[4 * 256 + lane] = ls;
  }
  asm volatile("s_waitcnt lgkmcnt(0)\n\ts_barrier" ::: "memory");
  {
    const float* xb = (const float*)(lds + XCH_OFF) + (w ^ 4) * (5 * 256);
    f32x16 o = half ? o1 : o0;
    float l = half ? l1[0] : l0[0];
#pragma unroll
    for (int g = 0; g < 4; ++g) {
      f32x4 v = *(const f32x4*)(xb + g * 256 + lane * 4);
#pragma unroll
      for (int e = 0; e < 4; ++e) o[4 * g + e] += v[e];
    }
    l += xb[4 * 256 + lane];
    if (__builtin_expect(ref_unsafe(o, l), 0)) {
      if (half) attn_exact<false>(kva, 0, 64, 0, q[1][0], q[1][1], ones, o, l);
      else attn_exact<false>(kva, 0, 64, 0, q[0][0], q[0][1], ones, o, l);
    }
    const float sc = __builtin_amdgcn_rcpf(l);
    char* dst = ws + WS_O + (size_t)((2 * qp + half) * 64 + hA) * 2048;
    gstore16_sc1<0>(lo16, dst, pack8s(o, 0, sc));
    gstore16_sc1<1024>(lo16, dst, pack8s(o, 8, sc));
  }
  STAMP(9);
  {
    const int g16 = lane >> 4, q4 = (lane & 15) >> 2, p4 = lane & 3;
    const int wc = 2 * (g16 & 1) + (p4 >> 1);
    const int wa0 = (kh * 64 + 4 * h + q4) * 128 + ((wc ^ (((q4 >> 1) & 1) << 2)) << 4) + 8 * (p4 & 1);
    lds_cptr wbase[2][2] = {{(lds_cptr)lds + WO_OFF + wa0, (lds_cptr)lds + WO_OFF + (wa0 ^ 64)},
                            {(lds_cptr)lds + WO_OFF + 49152 + wa0, (lds_cptr)lds + WO_OFF + 49152 + (wa0 ^ 64)}};
    PIN(wbase[0][0]); PIN(wbase[0][1]); PIN(wbase[1][0]); PIN(wbase[1][1]);
    f32x16 accB[2] = {}, accA[2] = {};
    bf16x8 wf[4][2];
    const unsigned foa = (mb * 16 + (lane & 15)) * 8;
    u32x2 seenA = {~tag[0], 0u};
    u32x2 all0 = seenA, all1 = seenA, all2 = seenA, all3 = seenA;
    float g = 0.f;
    auto rdw = [&](bf16x8 (&f)[2], int st, int ks) {
#pragma unroll
      for (int j = 0; j < 2; ++j) {
        lds_cptr p = wbase[(st % 6) / 3][j] + ((st % 6) % 3) * 16384 + ks * 2048;
        f[j] = cat8(lds_tr(p), lds_tr(p + 1024));
      }
    };
    asm volatile("s_waitcnt vmcnt(17)" : "+v"(of[0][0]), "+v"(of[0][1]), "+v"(of[0][2]), "+v"(of[0][3])::"memory");
    rdw(wf[0], 0, 0); rdw(wf[1], 0, 1);
#define OTRACE(i) do {} while (0)
#define O_MM(ACC, f, a) do { ACC[0] = MFMA32(f[0], a, ACC[0]); SBAR(); ACC[1] = MFMA32(f[1], a, ACC[1]); SBAR(); } while (0)
#define O_STAGE(st, ACC, VM, PRE, POST) do { \
      rdw(wf[2], st, 2); SBAR(); O_MM(ACC, wf[0], of[(st) % 5][0]); \
      rdw(wf[3], st, 3); SBAR(); O_MM(ACC, wf[1], of[(st) % 5][1]); \
      if ((st) < 15) { \
        OTRACE(3 * (st)); \
        asm volatile("s_waitcnt vmcnt(" #VM ")" : "+v"(of[((st) + 1) % 5][0]), "+v"(of[((st) + 1) % 5][1]), \
                     "+v"(of[((st) + 1) % 5][2]), "+v"(of[((st) + 1) % 5][3]), "+v"(seenA)::"memory"); \
        PRE; \
        OTRACE(3 * (st) + 1); \
        asm volatile("s_barrier" ::: "memory"); \
        OTRACE(3 * (st) + 2); \
        POST; \
        SBAR(); \
        rdw(wf[0], (st) + 1, 0); \
      } \
      SBAR(); O_MM(ACC, wf[2], of[(st) % 5][2]); \
      if ((st) < 15) rdw(wf[1], (st) + 1, 1); \
      SBAR(); O_MM(ACC, wf[3], of[(st) % 5][3]); \
    } while (0)
#define OW(st) (pieceW(st, 0), pieceW(st, 1))
#define OL(st) loadO(st, of[(st) % 5])
    O_STAGE(0, accB, 11, NOP, OL(4));
    O_STAGE(1, accB, 4, NOP, { if (tid == 0) flag_set(ws + WS_FOA + (qp * 8 + hg) * 8, tag);   OW(6); OL(5); });
    O_STAGE(2, accB, 10, NOP, (OW(7), OL(6)));
    O_STAGE(3, accB, 12, NOP, (flag_peek(seenA, foa, ws + WS_FOA), OW(8), OL(7)));
    O_STAGE(4, accB, 13, NOP, OW(9));
    O_STAGE(5, accB, 8, if (w == 0) flag_wait(seenA, foa, ws + WS_FOA, tag),
            (gload4_sc1(g, (wm * 32 + r) * 4, ws + WS_G + mb * 512), OW(10), OL(8), OL(9)));
    O_STAGE(6, accB, 13, NOP, (OW(11), OL(10)));
    O_STAGE(7, accB, 10, NOP, (OW(12), OL(11)));
    O_STAGE(8, accA, 12, NOP, (OW(13), OL(12)));
    O_STAGE(9, accA, 12, NOP, (flag_peek(all0, lane * 8, ws + WS_FOA), flag_peek(all1, lane * 8, ws + WS_FOA + 512),
                                     flag_peek(all2, lane * 8, ws + WS_FOA + 1024), flag_peek(all3, lane * 8, ws + WS_FOA + 1536), OW(14), OL(13)));
    O_STAGE(10, accA, 16, NOP, (OW(15), OL(14)));
    O_STAGE(11, accA, 16, NOP, OL(15));
    O_STAGE(12, accA, 10, NOP, NOP);
    O_STAGE(13, accA, 4, NOP, NOP);
    O_STAGE(14, accA, 0, NOP, NOP);
    O_STAGE(15, accA, 0, NOP, NOP);
    asm volatile("" : "+v"(g), "+v"(all0), "+v"(all1), "+v"(all2), "+v"(all3));
    if (b == 0 && w == 0) {
      flag_wait(all0, lane * 8, ws + WS_FOA, tag); flag_wait(all1, lane * 8, ws + WS_FOA + 512, tag);
      flag_wait(all2, lane * 8, ws + WS_FOA + 1024, tag); flag_wait(all3, lane * 8, ws + WS_FOA + 1536, tag);
      if (lane == 0) flag_set(ws + WS_NONCE, tag);
    }
    STAMP(10);
    float* xw = (float*)(lds + XCH_OFF) + w * 1024 + lane * 4;
    f32x16 acc[2];
#pragma unroll
    for (int j = 0; j < 2; ++j)
#pragma unroll
      for (int i = 0; i < 16; ++i) acc[j][i] = g * accA[j][i] + (1.f - g) * accB[j][i];
    const f32x16& as = kh ? acc[0] : acc[1];
#pragma unroll
    for (int g = 0; g < 4; ++g) *(f32x4*)(xw + g * 256) = (f32x4){as[4 * g], as[4 * g + 1], as[4 * g + 2], as[4 * g + 3]};
    asm volatile("s_waitcnt lgkmcnt(0)\n\ts_barrier" ::: "memory");
    f32x16 t = kh ? acc[1] : acc[0];
    const float* xr = (const float*)(lds + XCH_OFF) + (w ^ 4) * 1024 + lane * 4;
#pragma unroll
    for (int g = 0; g < 4; ++g) {
      f32x4 v = *(const f32x4*)(xr + g * 256);
#pragma unroll
      for (int e = 0; e < 4; ++e) t[4 * g + e] += v[e];
    }
    const int row = mb * 128 + wm * 32 + r;
    char* dst = out + (size_t)row * 2048 + (nb * 64 + kh * 32) * 2;
#pragma unroll
    for (int g = 0; g < 4; g += 2) {
      u32x2 a = {cvtpk(t[4 * g], t[4 * g + 1]), cvtpk(t[4 * g + 2], t[4 * g + 3])};
      u32x2 c = {cvtpk(t[4 * g + 4], t[4 * g + 5]), cvtpk(t[4 * g + 6], t[4 * g + 7])};
      auto x = __builtin_amdgcn_permlane32_swap(a[0], c[0], false, false);
      auto y = __builtin_amdgcn_permlane32_swap(a[1], c[1], false, false);
      *(u32x4*)(dst + g * 16 + h * 16) = (u32x4){x[0], y[0], x[1], y[1]};
    }
  }
  STAMP(11);
  STAMP_OUT(0, ws); TRACE_OUT(ws);
}

extern "C" void kernel_launch(void* const* d_in, const int* in_sizes, int n_in, void* d_out, int out_size, void* d_ws,
                              size_t ws_size, hipStream_t stream) {
  const char* xa = (const char*)d_in[0];
  const char* wq = (const char*)d_in[1];
  const char* wk = (const char*)d_in[2];
  const char* wv = (const char*)d_in[3];
  const char* woa = (const char*)d_in[4];
  const char* wqkv = (const char*)d_in[6];
  const char* wob = (const char*)d_in[7];
  const char* gw = (const char*)d_in[8];
  (void)hipFuncSetAttribute((const void*)k_fused, hipFuncAttributeMaxDynamicSharedMemorySize, R_LDS);
  k_fused<<<256, 512, R_LDS, stream>>>(xa, wq, wk, wv, woa, wqkv, wob, gw, (char*)d_ws, (char*)d_out);
}
```

```cpp
#include <hip/hip_runtime.h>
#include <hip/hip_bf16.h>
#include <stdint.h>

typedef float f32x16 __attribute__((ext_vector_type(16)));
typedef float f32x4 __attribute__((ext_vector_type(4)));
typedef float f32x2 __attribute__((ext_vector_type(2)));
typedef short bf16x8 __attribute__((ext_vector_type(8)));
typedef short s16x4 __attribute__((ext_vector_type(4)));
typedef __bf16 bf16x2 __attribute__((ext_vector_type(2)));
typedef unsigned u32x4 __attribute__((ext_vector_type(4)));
typedef unsigned u32x2 __attribute__((ext_vector_type(2)));
typedef __attribute__((address_space(3))) const char* lds_cptr;
typedef __attribute__((address_space(3))) char* lds_ptr;

#define DI __device__ __forceinline__
#define SBAR() __builtin_amdgcn_sched_barrier(0)
#define PIN(x) asm volatile("" : "+v"(x))
#define MFMA32(a, b, c) __builtin_amdgcn_mfma_f32_32x32x16_bf16(a, b, c, 0, 0, 0)
#define MFMA16(a, b, c) __builtin_amdgcn_mfma_f32_16x16x32_bf16(a, b, c, 0, 0, 0)

constexpr int SEQ = 2048, DM = 1024;
constexpr float QSCALE = 0.17677669529663687f * 1.4426950408889634f;

constexpr size_t WS_KVA = 4u << 20;
constexpr size_t WS_G = WS_KVA + (256u << 10);
constexpr size_t WS_NONCE = WS_G + 8192;
constexpr size_t WS_FKV = WS_NONCE + 1024;
constexpr size_t WS_FPB = WS_FKV + 1024;
constexpr size_t WS_FOA = WS_FPB + 2048;
constexpr size_t WS_FOB = WS_FOA + 2048;
constexpr size_t WS_QB = 5u << 20;
constexpr size_t WS_KVB = 9u << 20;
constexpr size_t WS_O = 17u << 20;

#define TRACE(i) do {} while (0)
#define TRACE_DECL
#define TRACE_OUT(wsp) do {} while (0)
#define STAMP_ARG
#define STAMP_PASS
#define STAMP_DECL
#define STAMP(i) do {} while (0)
#define STAMP_OUT(k, wsp) do {} while (0)
#define PT(i) do {} while (0)
#define PTV(i) do {} while (0)
#define PT_ARG
#define PT_PASS
DI unsigned rfl(unsigned x) { return __builtin_amdgcn_readfirstlane(x); }
DI unsigned cvtpk(float lo, float hi) {
  f32x2 v = {lo, hi};
  bf16x2 b = __builtin_convertvector(v, bf16x2);
  return __builtin_bit_cast(unsigned, b);
}
DI bf16x8 pack8(const f32x16& a, int o) {
  u32x4 r = {cvtpk(a[o], a[o + 1]), cvtpk(a[o + 2], a[o + 3]), cvtpk(a[o + 4], a[o + 5]), cvtpk(a[o + 6], a[o + 7])};
  return __builtin_bit_cast(bf16x8, r);
}
DI bf16x8 pack8s(const f32x16& a, int o, float s) {
  u32x4 r = {cvtpk(a[o] * s, a[o + 1] * s), cvtpk(a[o + 2] * s, a[o + 3] * s), cvtpk(a[o + 4] * s, a[o + 5] * s),
             cvtpk(a[o + 6] * s, a[o + 7] * s)};
  return __builtin_bit_cast(bf16x8, r);
}
template <int OFF>
DI void glds16(unsigned voff, const void* sbase, unsigned lds_base) {
  asm volatile("s_mov_b32 m0, %2\n\ts_nop 0\n\tglobal_load_lds_dwordx4 %0, %1 offset:%3"
               :: "v"(voff), "s"(sbase), "s"(rfl(lds_base)), "i"(OFF) : "memory");
}
template <int OFF>
DI void glds16_sc1(unsigned voff, const void* sbase, unsigned lds_base) {
  asm volatile("s_mov_b32 m0, %2\n\ts_nop 0\n\tglobal_load_lds_dwordx4 %0, %1 offset:%3 sc1"
               :: "v"(voff), "s"(sbase), "s"(rfl(lds_base)), "i"(OFF) : "memory");
}
DI void glds16v(const char* vaddr, unsigned lds_base) {
  asm volatile("s_mov_b32 m0, %1\n\ts_nop 0\n\tglobal_load_lds_dwordx4 %0, off" :: "v"(vaddr), "s"(rfl(lds_base)) : "memory");
}
DI bf16x8 lds_rd128(lds_cptr p) { return *(const __attribute__((address_space(3))) bf16x8*)p; }
DI s16x4 lds_tr(lds_cptr p) {
  typedef short v4i16_t __attribute__((ext_vector_type(4)));
  return __builtin_bit_cast(s16x4, __builtin_amdgcn_ds_read_tr16_b64_v4i16((__attribute__((address_space(3))) v4i16_t*)p));
}
DI bf16x8 cat8(s16x4 lo, s16x4 hi) { return (bf16x8){lo[0], lo[1], lo[2], lo[3], hi[0], hi[1], hi[2], hi[3]}; }
DI float bf2f(unsigned short b) { return __uint_as_float((unsigned)b << 16); }

template <int OFF>
DI void gload16_sc1(bf16x8& d, unsigned voff, const void* sbase) {
  asm volatile("global_load_dwordx4 %0, %1, %2 offset:%3 sc1" : "=v"(d) : "v"(voff), "s"(sbase), "i"(OFF) : "memory");
}
template <int OFF>
DI void gstore16_sc1(unsigned voff, void* sbase, const bf16x8& d) {
  asm volatile("global_store_dwordx4 %0, %1, %2 offset:%3 sc1\n\ts_nop 1" :: "v"(voff), "v"(d), "s"(sbase), "i"(OFF) : "memory");
}
DI void gload4_sc1(float& d, unsigned voff, const void* sbase) {
  asm volatile("global_load_dword %0, %1, %2 sc1" : "=v"(d) : "v"(voff), "s"(sbase) : "memory");
}
DI bf16x8 gload16_now(const char* p) {
  bf16x8 d;
  asm volatile("global_load_dwordx4 %0, %1, off sc1\n\ts_waitcnt vmcnt(0)" : "=&v"(d) : "v"(p) : "memory");
  return d;
}
typedef __attribute__((address_space(1))) unsigned long long gu64;
DI void flag_set(char* p, u32x2 tag) {
  asm volatile("global_store_dwordx2 %0, %1, %2 sc1\n\ts_nop 1" :: "v"(0u), "v"(tag), "s"(p) : "memory");
}
DI void flag_peek(u32x2& d, unsigned voff, const void* sbase) {
  asm volatile("global_load_dwordx2 %0, %1, %2 sc1" : "+v"(d) : "v"(voff), "s"(sbase) : "memory");
}
DI void flag_wait(u32x2 seen, unsigned voff, const char* sbase, u32x2 tag) {
  if (__builtin_expect(__all(seen[0] == tag[0] && seen[1] == tag[1]), 1)) return;
  const unsigned long long want = ((unsigned long long)tag[1] << 32) | tag[0];
  for (unsigned spins = 0; spins < (1u << 21); ++spins) {
    if (__all(__hip_atomic_load((gu64*)(sbase + voff), __ATOMIC_RELAXED, __HIP_MEMORY_SCOPE_AGENT) == want)) return;
    __builtin_amdgcn_s_sleep(4);
  }
}

DI float wave_sum63(float z) {
#define DPP_ADD(ctrl, rmask) z += __uint_as_float(__builtin_amdgcn_update_dpp(0u, __float_as_uint(z), ctrl, rmask, 0xf, true))
  DPP_ADD(0x111, 0xf); DPP_ADD(0x112, 0xf); DPP_ADD(0x114, 0xf); DPP_ADD(0x118, 0xf);
  DPP_ADD(0x142, 0xa); DPP_ADD(0x143, 0xc);
#undef DPP_ADD
  return z;
}

DI void nonce_load(u32x2& nonce, const char* ws) {
  asm volatile("global_load_dwordx2 %0, %1, %2 sc1" : "=v"(nonce) : "v"(0u), "s"(ws + WS_NONCE) : "memory");
}
DI u32x2 nonce_tag(u32x2 nonce) {
  u32x2 tag;
  tag[0] = nonce[0] + 1u;
  tag[1] = nonce[1] + (tag[0] == 0u ? 1u : 0u);
  return tag;
}

constexpr int P_SLOT = 49152;
constexpr int P_XOFF = 0, P_WOFF = 16384;
constexpr int QX_OFF = 0;
struct PDma {
  const char* xsrc; const char* wp[4]; unsigned xo[2], wstep[2], lds0; int w;
  DI void init(const char* xa, const char* wq, const char* wqkv, int mb, int j, unsigned lds0_, int w_, int lane) {
    lds0 = lds0_; w = w_;
    xsrc = xa + (size_t)mb * 128 * 2048;
#pragma unroll
    for (int e = 0; e < 2; ++e) {
      const int row = 16 * w + 8 * e + (lane >> 3);
      xo[e] = row * 2048 + (((lane & 7) ^ (4 * e + (lane >> 4))) << 4);
    }
#pragma unroll
    for (int e = 0; e < 4; ++e) {
      const int krow = 8 * w + 2 * e + (lane >> 5);
      const int c = (lane & 31) ^ ((krow & 3) << 2);
      wp[e] = c < 24 ? wqkv + (size_t)krow * 6144 + j * 384 + c * 16 : wq + (size_t)krow * 2048 + j * 128 + (c - 24) * 16;
      if (e < 2) wstep[e] = c < 24 ? 64 * 6144 : 64 * 2048;
    }
  }
  DI void piece(int pc, int st, int slot) {
    const unsigned lb = lds0 + slot * P_SLOT;
    if (pc < 2) glds16<0>(xo[pc], xsrc + st * 128, lb + P_XOFF + w * 2048 + pc * 1024);
    else { glds16v(wp[pc - 2], lb + P_WOFF + w * 4096 + (pc - 2) * 1024); wp[pc - 2] += wstep[pc & 1]; }
  }
  DI void stage(int st, int slot) {
#pragma unroll
    for (int pc = 0; pc < 6; ++pc) piece(pc, st, slot);
  }
};
struct PFrag { bf16x8 x[2]; bf16x8 w[2]; };

template <bool VT, class Tail, class Peek, class Check, class Hook>
DI void proj_loop(PDma& dma, char* lds, f32x16 (&acc)[2][2], Tail tail, Peek peek, Check check, Hook hook PT_ARG) {
  const int tid = threadIdx.x, lane = tid & 63;
  const int w = dma.w;
  const int wm = w & 1, wn = w >> 1;
  const int r = lane & 31, h = lane >> 5;
  const int t3 = (r >> 1) & 7;
  const int xa0 = P_XOFF + (wm * 64 + r) * 128 + ((h ^ t3) << 4);
  const int g16 = lane >> 4, q4 = (lane & 15) >> 2, p4 = lane & 3;
  const int wc = wn * 8 + 2 * (g16 & 1) + (p4 >> 1);
  const int wa0 = P_WOFF + (8 * h + q4) * 512 + ((wc ^ (q4 << 2)) << 4) + 8 * (p4 & 1);
  auto rdx = [&](PFrag& f, int i, int slot, int s) { f.x[i] = lds_rd128((lds_cptr)lds + slot * P_SLOT + (xa0 ^ (s << 5)) + i * 4096); };
  auto rdw = [&](PFrag& f, int j, int slot, int s) {
    lds_cptr p = (lds_cptr)lds + slot * P_SLOT + (wa0 ^ (j << 6)) + s * 8192;
    f.w[j] = cat8(lds_tr(p), lds_tr(p + 2048));
  };
  auto mm = [&](const PFrag& f, int i, int j) {
    if constexpr (VT) acc[i][j] = MFMA32(f.x[i], f.w[j], acc[i][j]);
    else acc[i][j] = MFMA32(f.w[j], f.x[i], acc[i][j]);
  };
  auto win = [&](int j, int pc, int slot) {
    if (j == -1) dma.piece(pc, 2, 2);
    else if (j <= 12) dma.piece(pc, j + 3, slot);
    else if (j <= 14 && pc < 4) tail((j - 13) * 4 + pc);
  };
  auto step = [&](const PFrag& c, PFrag& n, int rslot, int rs, bool rd, int pa, int wj, int dslot) {
    mm(c, 0, 0); SBAR();
    if (rd) { rdw(n, 0, rslot, rs); rdx(n, 0, rslot, rs); }
    SBAR();
    mm(c, 0, 1); SBAR();
    if (rd) { rdw(n, 1, rslot, rs); rdx(n, 1, rslot, rs); }
    SBAR();
    mm(c, 1, 0); SBAR();
    if (pa >= 0) win(wj, pa, dslot);
    SBAR();
    mm(c, 1, 1); SBAR();
    if (pa >= 0) win(wj, pa + 1, dslot);
    SBAR();
  };
  PFrag f0, f1;
  rdw(f0, 0, 0, 0); rdx(f0, 0, 0, 0); rdw(f0, 1, 0, 0); rdx(f0, 1, 0, 0);
  auto iter = [&](int it, int slot, int pslot, int nslot, bool hk) {
    step(f0, f1, slot, 1, true, 2, it - 1, pslot);
    step(f1, f0, slot, 2, true, 4, it - 1, pslot);
    step(f0, f1, slot, 3, true, -1, 0, 0);
    PTV(16 + 2 * it);
    asm volatile("s_waitcnt vmcnt(6) lgkmcnt(0)\n\ts_barrier" ::: "memory");
    PTV(17 + 2 * it);
    if (hk) hook(it);
    SBAR();
    step(f1, f0, nslot, 0, true, 0, it, slot);
  };
  iter(0, 0, 0, 1, true); iter(1, 1, 0, 2, true); iter(2, 2, 1, 0, true); iter(3, 0, 2, 1, true);
  iter(4, 1, 0, 2, true); iter(5, 2, 1, 0, true); iter(6, 0, 2, 1, true);
  int slot = 1, pslot = 0;
  for (int it = 7; it < 13; ++it) {
    const int nslot = slot == 2 ? 0 : slot + 1;
    iter(it, slot, pslot, nslot, false);
    pslot = slot;
    slot = nslot;
  }
  peek();
  step(f0, f1, 1, 1, true, 2, 12, 0);
  step(f1, f0, 1, 2, true, 4, 12, 0);
  step(f0, f1, 1, 3, true, -1, 0, 0);
  check();
  SBAR();
  step(f1, f0, 2, 0, true, 0, 13, 0);
  step(f0, f1, 2, 1, true, 2, 13, 0);
  step(f1, f0, 2, 2, true, -1, 0, 0);
  step(f0, f1, 2, 3, true, -1, 0, 0);
  asm volatile("s_waitcnt vmcnt(4) lgkmcnt(0)\n\ts_barrier" ::: "memory");
  SBAR();
  step(f1, f0, 0, 0, true, 0, 14, 0);
  step(f0, f1, 0, 1, true, 2, 14, 0);
  step(f1, f0, 0, 2, true, -1, 0, 0);
  step(f0, f1, 0, 3, true, -1, 0, 0);
  asm volatile("s_waitcnt lgkmcnt(0)\n\ts_barrier" ::: "memory");
  SBAR();
  step(f1, f0, 0, 0, false, -1, 0, 0);
}

constexpr int KVC = 147456;
DI void pre_kv(const char* __restrict__ xg, const char* __restrict__ wsel, char* __restrict__ ws, int rt, int t, char* lds, PDma& dma,
               u32x2& tag, f32x16& acc, lds_cptr& xlast, lds_cptr& wlast PT_ARG) {
  const int tid = threadIdx.x, lane = tid & 63;
  const int w = dma.w;
  const int r = lane & 31, h = lane >> 5;
  const unsigned lds0 = dma.lds0;
  u32x2 nonce;
  nonce_load(nonce, ws);
  const int xrow = 4 * w + (lane >> 4);
  const unsigned xo = xrow * 2048 + (((lane & 15) ^ (xrow & 15)) << 4), wo = w * 1024 + lane * 16;
  const char* xs = xg + (size_t)rt * 32 * 2048;
#define KV_OFF(u) ((u) == 0 || (u) == 7 ? KVC : P_SLOT + ((u) - 1) * 16384)
  auto sub = [&](int u) {
    const unsigned lb = lds0 + KV_OFF(u) + w * 1024;
    glds16<0>(xo, xs + u * 256, lb);
    glds16<0>(wo, wsel + u * 8192, lb + 8192);
  };
  sub(0); sub(1); sub(2); sub(3); sub(4); sub(5); sub(6);
  dma.stage(0, 0);
  PT(0);
  const int g16 = lane >> 4, q4 = (lane & 15) >> 2, p4 = lane & 3;
  lds_cptr xa = (lds_cptr)lds + P_SLOT + r * 256 + (((2 * w + h) ^ (r & 15)) << 4);
  lds_cptr wa = (lds_cptr)lds + P_SLOT + 8192 + (16 * w + 8 * h + q4) * 64 + 32 * (g16 & 1) + 8 * p4;
  lds_cptr xc = xa + (KVC - P_SLOT), wc = wa + (KVC - P_SLOT);
  PIN(xa); PIN(wa); PIN(xc); PIN(wc);
  acc = (f32x16){};
  bf16x8 xf[4], wf[4];
#define KV_RD(i, u) do { \
    lds_cptr px_ = (u) == 0 ? xc : xa + ((u) - 1) * 16384; \
    lds_cptr pw_ = (u) == 0 ? wc : wa + ((u) - 1) * 16384; \
    xf[i] = lds_rd128(px_); wf[i] = cat8(lds_tr(pw_), lds_tr(pw_ + 256)); \
  } while (0)
#define KV_MM(i) do { if (t) acc = MFMA32(xf[i], wf[i], acc); else acc = MFMA32(wf[i], xf[i], acc); } while (0)
  asm volatile("s_waitcnt vmcnt(12)\n\ts_barrier" : "+v"(nonce)::"memory");
  PT(1);
  KV_RD(0, 0); KV_RD(1, 1); KV_RD(2, 2); KV_RD(3, 3);
  KV_MM(0); KV_MM(1); KV_MM(2); KV_MM(3);
  asm volatile("s_waitcnt vmcnt(6) lgkmcnt(0)\n\ts_barrier" ::: "memory");
  PT(2);
  KV_RD(0, 4); KV_RD(1, 5); KV_RD(2, 6);
  SBAR();
  sub(7); dma.stage(1, 1);
  SBAR();
  KV_MM(0); KV_MM(1); KV_MM(2);
  asm volatile("s_waitcnt vmcnt(8) lgkmcnt(0)\n\ts_barrier" ::: "memory");
  PT(3);
  dma.piece(0, 2, 2); dma.piece(1, 2, 2);
#undef KV_RD
#undef KV_MM
#undef KV_OFF
  tag = nonce_tag(nonce);
  xlast = xc; wlast = wc;
  PT(9);
}

DI void pre_gate(const char* __restrict__ xg, const char* __restrict__ gw, char* __restrict__ ws, int row0, PDma& dma, u32x2& tag PT_ARG) {
  const int tid = threadIdx.x, lane = tid & 63;
  const int w = dma.w;
  u32x4 gx[2][2], gv[2];
  u32x2 nonce;
  const char* px = xg + (size_t)(row0 + 2 * w) * 2048;
  asm volatile("global_load_dwordx2 %6, %10, %11 sc1\n\t"
               "global_load_dwordx4 %0, %7, %8\n\tglobal_load_dwordx4 %1, %7, %8 offset:16\n\t"
               "global_load_dwordx4 %2, %7, %8 offset:2048\n\tglobal_load_dwordx4 %3, %7, %8 offset:2064\n\t"
               "global_load_dwordx4 %4, %7, %9\n\tglobal_load_dwordx4 %5, %7, %9 offset:16"
               : "=&v"(gx[0][0]), "=&v"(gx[0][1]), "=&v"(gx[1][0]), "=&v"(gx[1][1]), "=&v"(gv[0]), "=&v"(gv[1]), "=&v"(nonce)
               : "v"(lane * 32), "s"(px), "s"(gw), "v"(0u), "s"(ws + WS_NONCE) : "memory");
  dma.stage(0, 0);
  PT(0);
  asm volatile("s_barrier" ::: "memory");
  PT(1);
  dma.stage(1, 1); dma.piece(0, 2, 2); dma.piece(1, 2, 2);
  PT(2);
  asm volatile("s_waitcnt vmcnt(8)\n\ts_barrier"
               : "+v"(gx[0][0]), "+v"(gx[0][1]), "+v"(gx[1][0]), "+v"(gx[1][1]), "+v"(gv[0]), "+v"(gv[1]), "+v"(nonce)::"memory");
  PT(3);
  tag = nonce_tag(nonce);
#pragma unroll
  for (int i = 0; i < 2; ++i) {
    float z = 0.f;
#pragma unroll
    for (int e = 0; e < 4; ++e) {
      z += __uint_as_float(gx[i][0][e] << 16) * __uint_as_float(gv[0][e] << 16) + __uint_as_float(gx[i][0][e] & 0xffff0000u) * __uint_as_float(gv[0][e] & 0xffff0000u);
      z += __uint_as_float(gx[i][1][e] << 16) * __uint_as_float(gv[1][e] << 16) + __uint_as_float(gx[i][1][e] & 0xffff0000u) * __uint_as_float(gv[1][e] & 0xffff0000u);
    }
    z = wave_sum63(z);
    const float g = 1.f / (1.f + __expf(-z));
    if (lane == 63) asm volatile("global_store_dword %0, %1, %2 sc1\n\ts_nop 0" :: "v"(0u), "v"(g), "s"(ws + WS_G + (row0 + 2 * w + i) * 4) : "memory");
  }
}

constexpr int SL = 32768;
constexpr int R_LDS = 5 * SL;
constexpr int XCH_OFF = 0;
constexpr int WO_OFF = 2 * SL;

DI int crow(int r, int h) { return (r & 3) + 8 * (r >> 2) + 4 * h; }
DI float swap_half(float x) {
  auto rr = __builtin_amdgcn_permlane32_swap(__float_as_uint(x), __float_as_uint(x), false, false);
  return __uint_as_float((threadIdx.x & 32) ? rr[0] : rr[1]);
}
DI void exp16(f32x16& s) {
#pragma unroll
  for (int i = 0; i < 16; ++i) s[i] = __builtin_amdgcn_exp2f(s[i]);
}
#define AE(S, i) S[i] = __builtin_amdgcn_exp2f(S[i])
#define AC(P, S, i) P[(i) & 3] = cvtpk(S[2 * (i)], S[2 * (i) + 1])
#define ABC(x) __builtin_bit_cast(bf16x8, x)
#define APHASE(QK, PV, SX, PXa, PXb, SY, CINY, PYa, PYb, QY0, QY1, OY, LY, KF0, KF1, VF0, VF1, R1, R2) do { \
  if (QK) { SY = MFMA32(KF0, QY0, CINY); PIN(SY); } SBAR(); \
  AE(SX, 0); AE(SX, 1); AE(SX, 2); AE(SX, 3); PIN(SX); SBAR(); \
  if (QK) { SY = MFMA32(KF1, QY1, SY); PIN(SY); } SBAR(); \
  R1; \
  AC(PXa, SX, 0); AC(PXa, SX, 1); AE(SX, 4); AE(SX, 5); AE(SX, 6); PIN(SX); PIN(PXa); SBAR(); \
  if (PV) { OY = MFMA32(VF0, ABC(PYa), OY); PIN(OY); } SBAR(); \
  AE(SX, 7); AC(PXa, SX, 2); AE(SX, 8); AE(SX, 9); AC(PXa, SX, 3); PIN(SX); PIN(PXa); SBAR(); \
  if (PV) { LY = MFMA16(ones, ABC(PYa), LY); PIN(LY); } SBAR(); \
  R2; \
  AE(SX, 10); AE(SX, 11); PIN(SX); SBAR(); \
  if (PV) { OY = MFMA32(VF1, ABC(PYb), OY); PIN(OY); } SBAR(); \
  AC(PXb, SX, 4); AC(PXb, SX, 5); AE(SX, 12); AE(SX, 13); AE(SX, 14); PIN(SX); PIN(PXb); SBAR(); \
  if (PV) { LY = MFMA16(ones, ABC(PYb), LY); PIN(LY); } SBAR(); \
  AE(SX, 15); AC(PXb, SX, 6); AC(PXb, SX, 7); PIN(PXb); SBAR(); \
} while (0)

template <bool MASKED>
DI void attn_exact(const char* __restrict__ kv, int t0, int t1, int qt, const bf16x8& q0, const bf16x8& q1, const bf16x8& ones,
                   f32x16& o, float& l) {
  const int lane = threadIdx.x & 63, r = lane & 31, h = lane >> 5;
  float m = -3.0e38f;
  for (int t = t0; t < t1; ++t) {
    const char* p = kv + (size_t)t * 4096 + lane * 16;
    bf16x8 k0 = gload16_now(p), k1 = gload16_now(p + 1024);
    f32x16 s = {};
    s = MFMA32(k0, q0, s); s = MFMA32(k1, q1, s);
#pragma unroll
    for (int i = 0; i < 16; ++i) {
      bool ok = true;
      if (MASKED) { const int c = crow(i, h); ok = (t == qt) ? (c <= r) : ((t == qt - 8) ? (c > r) : true); }
      m = fmaxf(m, ok ? s[i] : -3.0e38f);
    }
  }
  m = fmaxf(m, swap_half(m));
  o = (f32x16){};
  f32x4 la = {};
  for (int t = t0; t < t1; ++t) {
    const char* p = kv + (size_t)t * 4096 + lane * 16;
    bf16x8 k0 = gload16_now(p), k1 = gload16_now(p + 1024);
    bf16x8 v0 = gload16_now(p + 2048), v1 = gload16_now(p + 3072);
    f32x16 s = {};
    s = MFMA32(k0, q0, s); s = MFMA32(k1, q1, s);
#pragma unroll
    for (int i = 0; i < 16; ++i) {
      bool ok = true;
      if (MASKED) { const int c = crow(i, h); ok = (t == qt) ? (c <= r) : ((t == qt - 8) ? (c > r) : true); }
      s[i] = ok ? __builtin_amdgcn_exp2f(s[i] - m) : 0.f;
    }
    bf16x8 pa = pack8(s, 0), pb = pack8(s, 8);
    o = MFMA32(v0, pa, o); o = MFMA32(v1, pb, o);
    la = MFMA16(ones, pa, la); la = MFMA16(ones, pb, la);
  }
  l = la[0];
}
DI bool ref_unsafe(const f32x16& o, float l) {
  float z = 0.f;
#pragma unroll
  for (int i = 0; i < 16; ++i) z = __builtin_fmaf(o[i], 0.f, z);
  const bool ok = (l >= 5.421010862e-20f) && (l <= 1.8446744e19f) && (z == 0.f);
  return __any(!ok);
}

__global__ __launch_bounds__(512, 2) void k_fused(const char* __restrict__ xa, const char* __restrict__ wq,
                                                  const char* __restrict__ wk, const char* __restrict__ wv,
                                                  const char* __restrict__ woa, const char* __restrict__ wqkv,
                                                  const char* __restrict__ wob, const char* __restrict__ gw,
                                                  char* __restrict__ ws, char* __restrict__ out) {
  extern __shared__ __attribute__((aligned(16))) char lds[];
  STAMP_DECL STAMP(0); TRACE_DECL
  const int b = blockIdx.x;
  const int xcd = b & 7, idx = b >> 3;
  const int tid = threadIdx.x, lane = tid & 63;
  const int w = rfl(tid >> 6);
  const int r = lane & 31, h = lane >> 5;
  const unsigned lds0 = (unsigned)(uintptr_t)lds;
  const unsigned lo16 = lane * 16;
  const int pmb = (xcd & 1) * 8 + (idx & 7), pj = (xcd >> 1) * 4 + (idx >> 3);
  const int pr = w & 3, half = w >> 2;
  const char* kva = ws + WS_KVA;
  auto pieceA = [&](int k, int sl, int i) {
    const char* src = kva + (size_t)(half * 32 + 4 * k + pr) * 4096;
    const unsigned dst = lds0 + sl * SL + w * 4096;
    if (i == 0) glds16_sc1<0>(lo16, src, dst);
    else if (i == 1) glds16_sc1<1024>(lo16, src, dst);
    else if (i == 2) glds16_sc1<2048>(lo16, src, dst);
    else glds16_sc1<3072>(lo16, src, dst);
  };
  PDma dma;
  dma.init(xa, wq, wqkv, pmb, pj, lds0, w, lane);
  u32x2 tag;
  const bool evenj = (pj & 1) == 0;
  const int kvrt = pmb * 4 + ((pj >> 1) & 3), kvt = pj >> 3;
  PT(11);
  f32x16 kvacc = {};
  lds_cptr kvxl = (lds_cptr)lds, kvwl = (lds_cptr)lds;
  if (evenj) pre_kv(xa, kvt ? wv : wk, ws, kvrt, kvt, lds, dma, tag, kvacc, kvxl, kvwl PT_PASS);
  else pre_gate(xa, gw, ws, pmb * 128 + (pj >> 1) * 16, dma, tag PT_PASS);
  PT(12);
  STAMP(1);
  const int wn = w >> 1, pwm = w & 1;
  const int ct = 6 * pj + 2 * wn;
  const bool hasB = wn < 3;
  bf16x8 held[8];
  {
    f32x16 acc[2][2] = {};
    u32x2 kv0 = {~tag[0], 0u}, kv1 = kv0;
    auto tail = [&](int n) { pieceA(n >> 2, 2 + (n >> 2), n & 3); };
    auto peek = [&]() { flag_peek(kv0, lane * 8, ws + WS_FKV); flag_peek(kv1, lane * 8, ws + WS_FKV + 512); };
    auto check = [&]() {
      asm volatile("s_waitcnt vmcnt(4) lgkmcnt(0)" : "+v"(kv0), "+v"(kv1)::"memory");
      if (w == 0) { flag_wait(kv0, lane * 8, ws + WS_FKV, tag); flag_wait(kv1, lane * 8, ws + WS_FKV + 512, tag); }
      asm volatile("s_barrier" ::: "memory");
    };
    float* red = (float*)(lds + KVC) + lane * 4;
    auto put = [&](int s4) {
#pragma unroll
      for (int g = 0; g < 4; ++g) *(f32x4*)(red + (s4 * 4 + g) * 256) = (f32x4){kvacc[4 * g], kvacc[4 * g + 1], kvacc[4 * g + 2], kvacc[4 * g + 3]};
    };
    auto get = [&](int s4) {
#pragma unroll
      for (int g = 0; g < 4; ++g) {
        const f32x4 a = *(const f32x4*)(red + (s4 * 4 + g) * 256);
#pragma unroll
        for (int e = 0; e < 4; ++e) kvacc[4 * g + e] += a[e];
      }
    };
    auto hook = [&](int it) {
      if (evenj) {
        if (it == 0) {
          const bf16x8 xf = lds_rd128(kvxl), wf = cat8(lds_tr(kvwl), lds_tr(kvwl + 256));
          if (kvt) kvacc = MFMA32(xf, wf, kvacc); else kvacc = MFMA32(wf, xf, kvacc);
        }
        else if (it == 1) { if (w >= 4) put(w - 4); }
        else if (it == 2) { if (w < 4) get(w); }
        else if (it == 3) { if (w >= 1 && w < 4) put(w); }
        else if (it == 4) {
          if (w == 0) {
            get(1); get(2); get(3);
            char* dst = ws + WS_KVA + (size_t)kvrt * 4096 + kvt * 2048;
            gstore16_sc1<0>(lo16, dst, pack8(kvacc, 0));
            gstore16_sc1<1024>(lo16, dst, pack8(kvacc, 8));
          }
        } else if (it == 6) { if (tid == 0) flag_set(ws + WS_FKV + (kvrt * 2 + kvt) * 8, tag); }
      }
    };
    if (hasB && ct >= 64) proj_loop<true>(dma, lds, acc, tail, peek, check, hook PT_PASS);
    else proj_loop<false>(dma, lds, acc, tail, peek, check, hook PT_PASS);
    STAMP(2);
    const float sc = (!hasB || ct < 32) ? QSCALE : 1.f;
#pragma unroll
    for (int i = 0; i < 2; ++i)
#pragma unroll
      for (int j = 0; j < 2; ++j) {
        held[(i * 2 + j) * 2] = pack8s(acc[i][j], 0, sc);
        held[(i * 2 + j) * 2 + 1] = pack8s(acc[i][j], 8, sc);
      }
  }
  bf16x8 qb[2], q[2][2];
  typedef __attribute__((address_space(3))) bf16x8* lds_v8;
  if (hasB) {
    const unsigned hstep = ct < 32 ? 2048 : 4096;
    char* hbase = ws + (ct < 32 ? WS_QB : WS_KVB + (ct >= 64 ? 2048 : 0)) + (size_t)((ct & 31) * 64 + pmb * 4 + pwm * 2) * hstep;
#pragma unroll
    for (int n = 0; n < 8; ++n) {
      char* dst = hbase + (size_t)((n >> 2) + ((n >> 1) & 1) * 64) * hstep;
      if (n & 1) gstore16_sc1<1024>(lo16, dst, held[n]);
      else gstore16_sc1<0>(lo16, dst, held[n]);
    }
    asm volatile("s_waitcnt vmcnt(12)" ::: "memory");
  } else {
#pragma unroll
    for (int n = 0; n < 8; ++n)
      *(lds_v8)((lds_ptr)lds + QX_OFF + (((pwm * 2 + (n >> 2)) * 2 + ((n >> 1) & 1)) * 2 + (n & 1)) * 1024 + lo16) = held[n];
    asm volatile("s_waitcnt vmcnt(4)" ::: "memory");
  }
  asm volatile("s_waitcnt lgkmcnt(0)\n\ts_barrier" ::: "memory");
#pragma unroll
  for (int u = 0; u < 2; ++u)
#pragma unroll
    for (int s2 = 0; s2 < 2; ++s2) q[u][s2] = lds_rd128((lds_cptr)lds + QX_OFF + ((pr * 2 + u) * 2 + s2) * 1024 + lo16);
  STAMP(3);

  const int qt = pmb * 4 + pr, hA = 2 * pj;
  const int hB = (b & 7) + 8 * (b >> 6), rb = (b >> 3) & 7;
  const int qtB = rb * 8 + w;
  const int mb = (xcd & 3) * 4 + (idx & 3), nb = (xcd >> 2) * 8 + (idx >> 2);
  const char* kvb = ws + WS_KVB + (size_t)hB * 64 * 4096;
  bf16x8 ones;
  {
    const short v = ((((lane >> 4) & 1) == (((lane & 15) >> 2) & 1)) ? (short)0x3f80 : (short)0);
    ones = (bf16x8){v, v, v, v, v, v, v, v};
  }
  auto pieceB = [&](int n) {
    int gt = rb * 8 - 8 + 2 * w + (n >> 2);
    gt = gt < 0 ? 0 : gt;
    const char* src = kvb + (size_t)gt * 4096;
    const unsigned dst = lds0 + (2 * w + (n >> 2)) * 4096;
    if ((n & 3) == 0) glds16_sc1<0>(lo16, src, dst);
    else if ((n & 3) == 1) glds16_sc1<1024>(lo16, src, dst);
    else if ((n & 3) == 2) glds16_sc1<2048>(lo16, src, dst);
    else glds16_sc1<3072>(lo16, src, dst);
  };
  const char* pqb = ws + WS_QB + (size_t)(hB * 64 + qtB) * 2048;
  unsigned wo[2];
#pragma unroll
  for (int e = 0; e < 2; ++e) {
    const int row = 16 * w + 8 * e + (lane >> 3);
    const int c = (lane & 7) ^ (((row >> 1) & 1) << 2);
    wo[e] = row * 2048 + c * 16;
  }
  auto pieceW = [&](int st, int e) {
    const char* src = (st < 8 ? wob : woa) + (size_t)(st & 7) * 128 * 2048 + nb * 128;
    glds16<0>(wo[e], src, lds0 + WO_OFF + (st % 6) * 16384 + w * 2048 + e * 1024);
  };
#define NOP (void)0
  u32x2 seen;

  const f32x16 zero = {};
  f32x16 o0 = {}, o1 = {}, s0, s1;
  f32x4 l0 = {}, l1 = {};
  u32x4 p0a, p0b, p1a, p1b;
  bf16x8 ka0, ka1, kb0, kb1, va0, va1, vb0, vb1;
  lds_cptr kvbase = (lds_cptr)lds + half * 16384 + lane * 16;
#define APAIR(pp, SL0, SL1, FIRST, LAST, PA, PB, MID, D0, D1, D2, D3) do { \
    lds_cptr cur = kvbase + (SL0) * SL + ((pp) & 1) * 8192; PIN(cur);     \
    lds_cptr nxt = (SL0) == (SL1) && !((pp) & 1) ? cur + 8192 : kvbase + (SL1) * SL; \
    __builtin_amdgcn_s_setprio(PA); \
    if (FIRST) { ka0 = lds_rd128(cur); ka1 = lds_rd128(cur + 1024); s0 = MFMA32(ka0, q[0][0], zero); s0 = MFMA32(ka1, q[0][1], s0); } \
    SBAR(); \
    APHASE(1, !(FIRST), s0, p0a, p0b, s1, zero, p1a, p1b, q[1][0], q[1][1], o1, l1, ka0, ka1, vb0, vb1, \
           (kb0 = lds_rd128(cur + 4096), kb1 = lds_rd128(cur + 5120)), (va0 = lds_rd128(cur + 2048), va1 = lds_rd128(cur + 3072))); \
    D0; SBAR(); \
    APHASE(1, 1, s1, p1a, p1b, s0, zero, p0a, p0b, q[0][0], q[0][1], o0, l0, kb0, kb1, va0, va1, (void)0, (void)0); \
    D1; SBAR(); \
    ATRACE(2 * (pp)); \
    MID; \
    ATRACE(2 * (pp) + 1); \
    __builtin_amdgcn_s_setprio(PB); SBAR(); \
    APHASE(1, 1, s0, p0a, p0b, s1, zero, p1a, p1b, q[1][0], q[1][1], o1, l1, kb0, kb1, va0, va1, \
           if (!(LAST)) (ka0 = lds_rd128(nxt), ka1 = lds_rd128(nxt + 1024)), (vb0 = lds_rd128(cur + 4096 + 2048), vb1 = lds_rd128(cur + 4096 + 3072))); \
    D2; SBAR(); \
    APHASE(!(LAST), 1, s1, p1a, p1b, s0, zero, p0a, p0b, q[0][0], q[0][1], o0, l0, ka0, ka1, vb0, vb1, (void)0, (void)0); \
    D3; SBAR(); \
    if (LAST) { \
      o1 = MFMA32(vb0, ABC(p1a), o1); o1 = MFMA32(vb1, ABC(p1b), o1); \
      l1 = MFMA16(ones, ABC(p1a), l1); l1 = MFMA16(ones, ABC(p1b), l1); \
    } \
  } while (0)
#define ATRACE(i) do {} while (0)
#define DA(k, sl, i) pieceA(k, sl, i)
#define DA2(k, sl, i) (pieceA(k, sl, i), pieceA(k, sl, (i) + 1))
#define DB2(n) (pieceB(n), pieceB((n) + 1))
#define DW(st, e) pieceW(st, e)
#define BAR_V(n) asm volatile("s_waitcnt vmcnt(" #n ")\n\ts_barrier" ::: "memory")
  STAMP(4); STAMP(5);
  const int fbm = 2 * rb - 2 + (lane & 3);
  const unsigned fbo = ((fbm < 0 ? 2 * rb : fbm) * 16 + ((lane >> 2) % 3 * 32 + hB) / 6) * 8;
  seen = (u32x2){~tag[0], 0u};
  APAIR(0, 2, 2, 1, 0, 2, 1, NOP, DA(2, 4, 0), DA(2, 4, 1), DA(2, 4, 2), DA(2, 4, 3));
  APAIR(1, 2, 3, 0, 0, 0, 3, { BAR_V(4); if (tid == 0) flag_set(ws + WS_FPB + (pmb * 16 + pj) * 8, tag);   },
        NOP, NOP, NOP, NOP);
  APAIR(2, 3, 3, 0, 0, 2, 1, NOP, NOP, NOP, NOP, NOP);
  flag_peek(seen, fbo, ws + WS_FPB);
  APAIR(3, 3, 4, 0, 0, 0, 3, { asm volatile("s_waitcnt vmcnt(0)" : "+v"(seen)::"memory"); if (w == 0) flag_wait(seen, fbo, ws + WS_FPB, tag); asm volatile("s_barrier" ::: "memory"); },
        NOP, NOP, DB2(0), DB2(2));
  APAIR(4, 4, 4, 0, 0, 2, 2, NOP, DB2(4), DB2(6), (gload16_sc1<0>(qb[0], lo16, pqb), gload16_sc1<1024>(qb[1], lo16, pqb)), DA2(3, 2, 0));
  APAIR(5, 4, 4, 0, 1, 1, 0, NOP, DA2(3, 2, 2), NOP, NOP, NOP);
  asm volatile("s_waitcnt vmcnt(0) lgkmcnt(0)\n\ts_barrier" : "+v"(qb[0]), "+v"(qb[1])::"memory");
  STAMP(6);
  {
    f32x16 o = {};
    f32x4 la = {};
    lds_cptr bb = (lds_cptr)lds + w * 4096 + lane * 16;
    f32x16 mD;
#pragma unroll
    for (int i = 0; i < 16; ++i) mD[i] = crow(i, h) <= r ? 0.f : -1.0e30f;
    if (rb != 0) {
      f32x16 sa, sb;
#pragma unroll
      for (int i = 0; i < 16; ++i) sa[i] = -1.0e30f - mD[i];
      u32x4 pa0 = {}, pa1 = {}, pb0 = {}, pb1 = {};
      bf16x8 k0, k1, k2, k3, v0, v1, v2, v3;
      k0 = lds_rd128(bb); k1 = lds_rd128(bb + 1024);
      k2 = lds_rd128(bb + 4096); k3 = lds_rd128(bb + 4096 + 1024);
      sa = MFMA32(k0, qb[0], sa); sa = MFMA32(k1, qb[1], sa);
      SBAR();
#define BRDK(KA, KB, j) (KA = lds_rd128(bb + (j) * 4096), KB = lds_rd128(bb + (j) * 4096 + 1024))
#define BRDV(VA, VB, j) (VA = lds_rd128(bb + (j) * 4096 + 2048), VB = lds_rd128(bb + (j) * 4096 + 3072))
#define B_DMA(n) do { pieceA(4 + (n) / 4, 3 + (n) / 4, (n) & 3); SBAR(); } while (0)
      __builtin_amdgcn_s_setprio(3);
      APHASE(1, 0, sa, pa0, pa1, sb, zero, pb0, pb1, qb[0], qb[1], o, la, k2, k3, v2, v3, BRDK(k0, k1, 2), BRDV(v0, v1, 0));
      B_DMA(0); B_DMA(1);
      APHASE(1, 1, sb, pb0, pb1, sa, zero, pa0, pa1, qb[0], qb[1], o, la, k0, k1, v0, v1, BRDK(k2, k3, 3), BRDV(v2, v3, 1));
      B_DMA(2); B_DMA(3);
      __builtin_amdgcn_s_setprio(2);
      APHASE(1, 1, sa, pa0, pa1, sb, zero, pb0, pb1, qb[0], qb[1], o, la, k2, k3, v2, v3, BRDK(k0, k1, 4), BRDV(v0, v1, 2));
      B_DMA(4); B_DMA(5);
      APHASE(1, 1, sb, pb0, pb1, sa, zero, pa0, pa1, qb[0], qb[1], o, la, k0, k1, v0, v1, BRDK(k2, k3, 5), BRDV(v2, v3, 3));
      B_DMA(6); B_DMA(7);
      __builtin_amdgcn_s_setprio(1);
      APHASE(1, 1, sa, pa0, pa1, sb, zero, pb0, pb1, qb[0], qb[1], o, la, k2, k3, v2, v3, BRDK(k0, k1, 6), BRDV(v0, v1, 4));
      APHASE(1, 1, sb, pb0, pb1, sa, zero, pa0, pa1, qb[0], qb[1], o, la, k0, k1, v0, v1, BRDK(k2, k3, 7), BRDV(v2, v3, 5));
      __builtin_amdgcn_s_setprio(0);
      APHASE(1, 1, sa, pa0, pa1, sb, zero, pb0, pb1, qb[0], qb[1], o, la, k2, k3, v2, v3, BRDK(k0, k1, 8), BRDV(v0, v1, 6));
      APHASE(1, 1, sb, pb0, pb1, sa, mD, pa0, pa1, qb[0], qb[1], o, la, k0, k1, v0, v1, (void)0, BRDV(v2, v3, 7));
      APHASE(0, 1, sa, pa0, pa1, sb, zero, pb0, pb1, qb[0], qb[1], o, la, k2, k3, v2, v3, (void)0, BRDV(v0, v1, 8));
      o = MFMA32(v0, ABC(pa0), o); o = MFMA32(v1, ABC(pa1), o);
      la = MFMA16(ones, ABC(pa0), la); la = MFMA16(ones, ABC(pa1), la);
    } else {
#pragma unroll
      for (int n = 0; n < 8; ++n) B_DMA(n);
      auto tile = [&](int j, const f32x16& cin) {
        lds_cptr p = bb + j * 4096;
        bf16x8 k0 = lds_rd128(p), k1 = lds_rd128(p + 1024), v0 = lds_rd128(p + 2048), v1 = lds_rd128(p + 3072);
        f32x16 s = MFMA32(k0, qb[0], cin);
        s = MFMA32(k1, qb[1], s);
        exp16(s);
        bf16x8 pa = pack8(s, 0), pb = pack8(s, 8);
        o = MFMA32(v0, pa, o); o = MFMA32(v1, pb, o);
        la = MFMA16(ones, pa, la); la = MFMA16(ones, pb, la);
      };
      for (int j = 8 - w; j < 8; ++j) tile(j, zero);
      tile(8, mD);
    }
    float l = la[0];
    if (__builtin_expect(ref_unsafe(o, l), 0)) attn_exact<true>(kvb, qtB >= 8 ? qtB - 8 : 0, qtB + 1, qtB, qb[0], qb[1], ones, o, l);
    const float sc = __builtin_amdgcn_rcpf(l);
    char* dst = ws + WS_O + (size_t)(qtB * 64 + 32 + hB) * 2048;
    gstore16_sc1<0>(lo16, dst, pack8s(o, 0, sc));
    gstore16_sc1<1024>(lo16, dst, pack8s(o, 8, sc));
  }
  asm volatile("s_waitcnt vmcnt(2) lgkmcnt(0)\n\ts_barrier" ::: "memory");
  STAMP(7);
  const int wm = w & 3, kh = w >> 2;
  const char* obase = ws + WS_O + (size_t)(mb * 4 + wm) * 64 * 2048 + kh * 4096;
  bf16x8 of[5][4];
  auto loadO1 = [&](int st, int i, bf16x8& f) {
    const char* src = obase + (size_t)(st < 8 ? 32 + 4 * st : 4 * (st - 8)) * 2048;
    if (i == 0) gload16_sc1<0>(f, lo16, src);
    else if (i == 1) gload16_sc1<1024>(f, lo16, src);
    else if (i == 2) gload16_sc1<2048>(f, lo16, src);
    else gload16_sc1<3072>(f, lo16, src);
  };
#define LO(st, i) loadO1(st, i, of[st][i])
  auto loadO = [&](int st, bf16x8 (&f)[4]) {
    const char* src = obase + (size_t)(st < 8 ? 32 + 4 * st : 4 * (st - 8)) * 2048;
    gload16_sc1<0>(f[0], lo16, src); gload16_sc1<1024>(f[1], lo16, src);
    gload16_sc1<2048>(f[2], lo16, src); gload16_sc1<3072>(f[3], lo16, src);
  };
  const unsigned fob = ((mb >> 1) * 32 + (lane & 31)) * 8;
  seen = (u32x2){~tag[0], 0u};
  unsigned warm = 0;
  APAIR(6, 2, 2, 1, 0, 3, 3, NOP, DA(6, 0, 0), DA(6, 0, 1), DA(6, 0, 2), DA(6, 0, 3));
  APAIR(7, 2, 3, 0, 0, 3, 2, NOP, DA(7, 1, 0), DA(7, 1, 1), DA(7, 1, 2), DA(7, 1, 3));
  {
    const int R = 768 + (idx & 3) * 320 + w * 40 + (lane < 40 ? lane : 39);
    const char* pw = (R < 1024 ? wob + (size_t)R * 2048 : woa + (size_t)(R - 1024) * 2048) + nb * 128;
    asm volatile("global_load_dword %0, %1, off" : "+v"(warm) : "v"(pw) : "memory");
  }
  APAIR(8, 3, 3, 0, 0, 2, 2, NOP, NOP, NOP, NOP, NOP);
  APAIR(9, 3, 4, 0, 0, 1, 1, NOP, NOP, NOP, NOP, NOP);
  APAIR(10, 4, 4, 0, 0, 1, 0, NOP, NOP, NOP, NOP, NOP);
  APAIR(11, 4, 0, 0, 0, 0, 3, { asm volatile("s_waitcnt vmcnt(0)\n\ts_barrier" : "+v"(warm)::"memory");
                                if (tid == 0) flag_set(ws + WS_FOB + (rb * 32 + hB) * 8, tag);   }, NOP, NOP, NOP, NOP);
  APAIR(12, 0, 0, 0, 0, 3, 2, NOP, DW(0, 0), DW(0, 1), DW(1, 0), DW(1, 1));
  flag_peek(seen, fob, ws + WS_FOB);
  APAIR(13, 0, 1, 0, 0, 2, 1, NOP, DW(2, 0), DW(2, 1), DW(3, 0), DW(3, 1));
  APAIR(14, 1, 1, 0, 0, 0, 3, { asm volatile("s_waitcnt vmcnt(0)" : "+v"(seen)::"memory"); if (w == 0) flag_wait(seen, fob, ws + WS_FOB, tag); asm volatile("s_barrier" ::: "memory"); },
        NOP, NOP, (LO(0, 0), LO(0, 1), DW(4, 0)), (LO(0, 2), LO(0, 3), DW(4, 1)));
  APAIR(15, 1, 1, 0, 1, 2, 1, NOP, (LO(1, 0), LO(1, 1), DW(5, 0)), (LO(1, 2), LO(1, 3), DW(5, 1)), (LO(2, 0), LO(2, 1)), (LO(2, 2), LO(2, 3)));
  STAMP(8);

  loadO(3, of[3]);
  asm volatile("s_waitcnt lgkmcnt(0)\n\ts_barrier" ::: "memory");
  {
    float* xb = (float*)(lds + XCH_OFF) + w * (5 * 256);
    const f32x16& os = half ? o0 : o1;
    const float ls = half ? l0[0] : l1[0];
#pragma unroll
    for (int g = 0; g < 4; ++g) *(f32x4*)(xb + g * 256 + lane * 4) = (f32x4){os[4 * g], os[4 * g + 1], os[4 * g + 2], os[4 * g + 3]};
    xb[4 * 256 + lane] = ls;
  }
  asm volatile("s_waitcnt lgkmcnt(0)\n\ts_barrier" ::: "memory");
  {
    const float* xb = (const float*)(lds + XCH_OFF) + (w ^ 4) * (5 * 256);
    f32x16 o = half ? o1 : o0;
    float l = half ? l1[0] : l0[0];
#pragma unroll
    for (int g = 0; g < 4; ++g) {
      f32x4 v = *(const f32x4*)(xb + g * 256 + lane * 4);
#pragma unroll
      for (int e = 0; e < 4; ++e) o[4 * g + e] += v[e];
    }
    l += xb[4 * 256 + lane];
    if (__builtin_expect(ref_unsafe(o, l), 0)) {
      if (half) attn_exact<false>(kva, 0, 64, 0, q[1][0], q[1][1], ones, o, l);
      else attn_exact<false>(kva, 0, 64, 0, q[0][0], q[0][1], ones, o, l);
    }
    const float sc = __builtin_amdgcn_rcpf(l);
    char* dst = ws + WS_O + (size_t)(qt * 64 + hA + half) * 2048;
    gstore16_sc1<0>(lo16, dst, pack8s(o, 0, sc));
    gstore16_sc1<1024>(lo16, dst, pack8s(o, 8, sc));
  }
  STAMP(9);
  {
    const int g16 = lane >> 4, q4 = (lane & 15) >> 2, p4 = lane & 3;
    const int wc = 2 * (g16 & 1) + (p4 >> 1);
    const int wa0 = (kh * 64 + 4 * h + q4) * 128 + ((wc ^ (((q4 >> 1) & 1) << 2)) << 4) + 8 * (p4 & 1);
    lds_cptr wbase[2][2] = {{(lds_cptr)lds + WO_OFF + wa0, (lds_cptr)lds + WO_OFF + (wa0 ^ 64)},
                            {(lds_cptr)lds + WO_OFF + 49152 + wa0, (lds_cptr)lds + WO_OFF + 49152 + (wa0 ^ 64)}};
    PIN(wbase[0][0]); PIN(wbase[0][1]); PIN(wbase[1][0]); PIN(wbase[1][1]);
    f32x16 accB[2] = {}, accA[2] = {};
    bf16x8 wf[4][2];
    const unsigned foa = (mb * 16 + (lane & 15)) * 8;
    u32x2 seenA = {~tag[0], 0u};
    u32x2 all0 = seenA, all1 = seenA, all2 = seenA, all3 = seenA;
    float g = 0.f;
    auto rdw = [&](bf16x8 (&f)[2], int st, int ks) {
#pragma unroll
      for (int j = 0; j < 2; ++j) {
        lds_cptr p = wbase[(st % 6) / 3][j] + ((st % 6) % 3) * 16384 + ks * 2048;
        f[j] = cat8(lds_tr(p), lds_tr(p + 1024));
      }
    };
    asm volatile("s_waitcnt vmcnt(17)" : "+v"(of[0][0]), "+v"(of[0][1]), "+v"(of[0][2]), "+v"(of[0][3])::"memory");
    rdw(wf[0], 0, 0); rdw(wf[1], 0, 1);
#define OTRACE(i) do {} while (0)
#define O_MM(ACC, f, a) do { ACC[0] = MFMA32(f[0], a, ACC[0]); SBAR(); ACC[1] = MFMA32(f[1], a, ACC[1]); SBAR(); } while (0)
#define O_STAGE(st, ACC, VM, PRE, POST) do { \
      rdw(wf[2], st, 2); SBAR(); O_MM(ACC, wf[0], of[(st) % 5][0]); \
      rdw(wf[3], st, 3); SBAR(); O_MM(ACC, wf[1], of[(st) % 5][1]); \
      if ((st) < 15) { \
        OTRACE(3 * (st)); \
        asm volatile("s_waitcnt vmcnt(" #VM ")" : "+v"(of[((st) + 1) % 5][0]), "+v"(of[((st) + 1) % 5][1]), \
                     "+v"(of[((st) + 1) % 5][2]), "+v"(of[((st) + 1) % 5][3]), "+v"(seenA)::"memory"); \
        PRE; \
        OTRACE(3 * (st) + 1); \
        asm volatile("s_barrier" ::: "memory"); \
        OTRACE(3 * (st) + 2); \
        POST; \
        SBAR(); \
        rdw(wf[0], (st) + 1, 0); \
      } \
      SBAR(); O_MM(ACC, wf[2], of[(st) % 5][2]); \
      if ((st) < 15) rdw(wf[1], (st) + 1, 1); \
      SBAR(); O_MM(ACC, wf[3], of[(st) % 5][3]); \
    } while (0)
#define OW(st) (pieceW(st, 0), pieceW(st, 1))
#define OL(st) loadO(st, of[(st) % 5])
    O_STAGE(0, accB, 11, NOP, OL(4));
    O_STAGE(1, accB, 4, NOP, { if (tid == 0) flag_set(ws + WS_FOA + (pmb * 16 + pj) * 8, tag);   OW(6); OL(5); });
    O_STAGE(2, accB, 10, NOP, (OW(7), OL(6)));
    O_STAGE(3, accB, 12, NOP, (flag_peek(seenA, foa, ws + WS_FOA), OW(8), OL(7)));
    O_STAGE(4, accB, 13, NOP, OW(9));
    O_STAGE(5, accB, 8, if (w == 0) flag_wait(seenA, foa, ws + WS_FOA, tag),
            (gload4_sc1(g, (wm * 32 + r) * 4, ws + WS_G + mb * 512), OW(10), OL(8), OL(9)));
    O_STAGE(6, accB, 13, NOP, (OW(11), OL(10)));
    O_STAGE(7, accB, 10, NOP, (OW(12), OL(11)));
    O_STAGE(8, accA, 12, NOP, (OW(13), OL(12)));
    O_STAGE(9, accA, 12, NOP, (flag_peek(all0, lane * 8, ws + WS_FOA), flag_peek(all1, lane * 8, ws + WS_FOA + 512),
                                     flag_peek(all2, lane * 8, ws + WS_FOA + 1024), flag_peek(all3, lane * 8, ws + WS_FOA + 1536), OW(14), OL(13)));
    O_STAGE(10, accA, 16, NOP, (OW(15), OL(14)));
    O_STAGE(11, accA, 16, NOP, OL(15));
    O_STAGE(12, accA, 10, NOP, NOP);
    O_STAGE(13, accA, 4, NOP, NOP);
    O_STAGE(14, accA, 0, NOP, NOP);
    O_STAGE(15, accA, 0, NOP, NOP);
    asm volatile("" : "+v"(g), "+v"(all0), "+v"(all1), "+v"(all2), "+v"(all3));
    if (b == 0 && w == 0) {
      flag_wait(all0, lane * 8, ws + WS_FOA, tag); flag_wait(all1, lane * 8, ws + WS_FOA + 512, tag);
      flag_wait(all2, lane * 8, ws + WS_FOA + 1024, tag); flag_wait(all3, lane * 8, ws + WS_FOA + 1536, tag);
      if (lane == 0) flag_set(ws + WS_NONCE, tag);
    }
    STAMP(10);
    float* xw = (float*)(lds + XCH_OFF) + w * 1024 + lane * 4;
    f32x16 acc[2];
#pragma unroll
    for (int j = 0; j < 2; ++j)
#pragma unroll
      for (int i = 0; i < 16; ++i) acc[j][i] = g * accA[j][i] + (1.f - g) * accB[j][i];
    const f32x16& as = kh ? acc[0] : acc[1];
#pragma unroll
    for (int g = 0; g < 4; ++g) *(f32x4*)(xw + g * 256) = (f32x4){as[4 * g], as[4 * g + 1], as[4 * g + 2], as[4 * g + 3]};
    asm volatile("s_waitcnt lgkmcnt(0)\n\ts_barrier" ::: "memory");
    f32x16 t = kh ? acc[1] : acc[0];
    const float* xr = (const float*)(lds + XCH_OFF) + (w ^ 4) * 1024 + lane * 4;
#pragma unroll
    for (int g = 0; g < 4; ++g) {
      f32x4 v = *(const f32x4*)(xr + g * 256);
#pragma unroll
      for (int e = 0; e < 4; ++e) t[4 * g + e] += v[e];
    }
    const int row = mb * 128 + wm * 32 + r;
    char* dst = out + (size_t)row * 2048 + (nb * 64 + kh * 32) * 2;
#pragma unroll
    for (int g = 0; g < 4; g += 2) {
      u32x2 a = {cvtpk(t[4 * g], t[4 * g + 1]), cvtpk(t[4 * g + 2], t[4 * g + 3])};
      u32x2 c = {cvtpk(t[4 * g + 4], t[4 * g + 5]), cvtpk(t[4 * g + 6], t[4 * g + 7])};
      auto x = __builtin_amdgcn_permlane32_swap(a[0], c[0], false, false);
      auto y = __builtin_amdgcn_permlane32_swap(a[1], c[1], false, false);
      *(u32x4*)(dst + g * 16 + h * 16) = (u32x4){x[0], y[0], x[1], y[1]};
    }
  }
  STAMP(11);
  STAMP_OUT(0, ws); TRACE_OUT(ws);
}

extern "C" void kernel_launch(void* const* d_in, const int* in_sizes, int n_in, void* d_out, int out_size, void* d_ws,
                              size_t ws_size, hipStream_t stream) {
  const char* xa = (const char*)d_in[0];
  const char* wq = (const char*)d_in[1];
  const char* wk = (const char*)d_in[2];
  const char* wv = (const char*)d_in[3];
  const char* woa = (const char*)d_in[4];
  const char* wqkv = (const char*)d_in[6];
  const char* wob = (const char*)d_in[7];
  const char* gw = (const char*)d_in[8];
  (void)hipFuncSetAttribute((const void*)k_fused, hipFuncAttributeMaxDynamicSharedMemorySize, R_LDS);
  k_fused<<<256, 512, R_LDS, stream>>>(xa, wq, wk, wv, woa, wqkv, wob, gw, (char*)d_ws, (char*)d_out);
}
```

```cpp
#include <hip/hip_runtime.h>
#include <hip/hip_bf16.h>
#include <stdint.h>

typedef float f32x16 __attribute__((ext_vector_type(16)));
typedef float f32x4 __attribute__((ext_vector_type(4)));
typedef float f32x2 __attribute__((ext_vector_type(2)));
typedef short bf16x8 __attribute__((ext_vector_type(8)));
typedef short s16x4 __attribute__((ext_vector_type(4)));
typedef __bf16 bf16x2 __attribute__((ext_vector_type(2)));
typedef unsigned u32x4 __attribute__((ext_vector_type(4)));
typedef unsigned u32x2 __attribute__((ext_vector_type(2)));
typedef __attribute__((address_space(3))) const char* lds_cptr;
typedef __attribute__((address_space(3))) char* lds_ptr;

#define DI __device__ __forceinline__
#define SBAR() __builtin_amdgcn_sched_barrier(0)
#define PIN(x) asm volatile("" : "+v"(x))
#define MFMA32(a, b, c) __builtin_amdgcn_mfma_f32_32x32x16_bf16(a, b, c, 0, 0, 0)
#define MFMA16(a, b, c) __builtin_amdgcn_mfma_f32_16x16x32_bf16(a, b, c, 0, 0, 0)

constexpr int SEQ = 2048, DM = 1024;
constexpr float QSCALE = 0.17677669529663687f * 1.4426950408889634f;

constexpr size_t WS_KVA = 4u << 20;
constexpr size_t WS_G = WS_KVA + (256u << 10);
constexpr size_t WS_NONCE = WS_G + 8192;
constexpr size_t WS_FKV = WS_NONCE + 1024;
constexpr size_t WS_FPB = WS_FKV + 1024;
constexpr size_t WS_FOA = WS_FPB + 2048;
constexpr size_t WS_FOB = WS_FOA + 2048;
constexpr size_t WS_QB = 5u << 20;
constexpr size_t WS_KVB = 9u << 20;
constexpr size_t WS_O = 17u << 20;

#define TRACE(i) do {} while (0)
#define TRACE_DECL
#define TRACE_OUT(wsp) do {} while (0)
#define STAMP_ARG
#define STAMP_PASS
#define STAMP_DECL
#define STAMP(i) do {} while (0)
#define STAMP_OUT(k, wsp) do {} while (0)
#define PT(i) do {} while (0)
#define PTV(i) do {} while (0)
#define PT_ARG
#define PT_PASS
DI unsigned rfl(unsigned x) { return __builtin_amdgcn_readfirstlane(x); }
DI unsigned cvtpk(float lo, float hi) {
  f32x2 v = {lo, hi};
  bf16x2 b = __builtin_convertvector(v, bf16x2);
  return __builtin_bit_cast(unsigned, b);
}
DI bf16x8 pack8(const f32x16& a, int o) {
  u32x4 r = {cvtpk(a[o], a[o + 1]), cvtpk(a[o + 2], a[o + 3]), cvtpk(a[o + 4], a[o + 5]), cvtpk(a[o + 6], a[o + 7])};
  return __builtin_bit_cast(bf16x8, r);
}
DI bf16x8 pack8s(const f32x16& a, int o, float s) {
  u32x4 r = {cvtpk(a[o] * s, a[o + 1] * s), cvtpk(a[o + 2] * s, a[o + 3] * s), cvtpk(a[o + 4] * s, a[o + 5] * s),
             cvtpk(a[o + 6] * s, a[o + 7] * s)};
  return __builtin_bit_cast(bf16x8, r);
}
template <int OFF>
DI void glds16(unsigned voff, const void* sbase, unsigned lds_base) {
  asm volatile("s_mov_b32 m0, %2\n\ts_nop 0\n\tglobal_load_lds_dwordx4 %0, %1 offset:%3"
               :: "v"(voff), "s"(sbase), "s"(rfl(lds_base)), "i"(OFF) : "memory");
}
template <int OFF>
DI void glds16_sc1(unsigned voff, const void* sbase, unsigned lds_base) {
  asm volatile("s_mov_b32 m0, %2\n\ts_nop 0\n\tglobal_load_lds_dwordx4 %0, %1 offset:%3 sc1"
               :: "v"(voff), "s"(sbase), "s"(rfl(lds_base)), "i"(OFF) : "memory");
}
DI void glds16v(const char* vaddr, unsigned lds_base) {
  asm volatile("s_mov_b32 m0, %1\n\ts_nop 0\n\tglobal_load_lds_dwordx4 %0, off" :: "v"(vaddr), "s"(rfl(lds_base)) : "memory");
}
DI bf16x8 lds_rd128(lds_cptr p) { return *(const __attribute__((address_space(3))) bf16x8*)p; }
DI s16x4 lds_tr(lds_cptr p) {
  typedef short v4i16_t __attribute__((ext_vector_type(4)));
  return __builtin_bit_cast(s16x4, __builtin_amdgcn_ds_read_tr16_b64_v4i16((__attribute__((address_space(3))) v4i16_t*)p));
}
DI bf16x8 cat8(s16x4 lo, s16x4 hi) { return (bf16x8){lo[0], lo[1], lo[2], lo[3], hi[0], hi[1], hi[2], hi[3]}; }
DI float bf2f(unsigned short b) { return __uint_as_float((unsigned)b << 16); }

template <int OFF>
DI void gload16_sc1(bf16x8& d, unsigned voff, const void* sbase) {
  asm volatile("global_load_dwordx4 %0, %1, %2 offset:%3 sc1" : "=v"(d) : "v"(voff), "s"(sbase), "i"(OFF) : "memory");
}
template <int OFF>
DI void gstore16_sc1(unsigned voff, void* sbase, const bf16x8& d) {
  asm volatile("global_store_dwordx4 %0, %1, %2 offset:%3 sc1\n\ts_nop 1" :: "v"(voff), "v"(d), "s"(sbase), "i"(OFF) : "memory");
}
DI void gload4_sc1(float& d, unsigned voff, const void* sbase) {
  asm volatile("global_load_dword %0, %1, %2 sc1" : "=v"(d) : "v"(voff), "s"(sbase) : "memory");
}
DI bf16x8 gload16_now(const char* p) {
  bf16x8 d;
  asm volatile("global_load_dwordx4 %0, %1, off sc1\n\ts_waitcnt vmcnt(0)" : "=&v"(d) : "v"(p) : "memory");
  return d;
}
typedef __attribute__((address_space(1))) unsigned long long gu64;
DI void flag_set(char* p, u32x2 tag) {
  asm volatile("global_store_dwordx2 %0, %1, %2 sc1\n\ts_nop 1" :: "v"(0u), "v"(tag), "s"(p) : "memory");
}
DI void flag_peek(u32x2& d, unsigned voff, const void* sbase) {
  asm volatile("global_load_dwordx2 %0, %1, %2 sc1" : "+v"(d) : "v"(voff), "s"(sbase) : "memory");
}
DI void flag_wait(u32x2 seen, unsigned voff, const char* sbase, u32x2 tag) {
  if (__builtin_expect(__all(seen[0] == tag[0] && seen[1] == tag[1]), 1)) return;
  const unsigned long long want = ((unsigned long long)tag[1] << 32) | tag[0];
  for (unsigned spins = 0; spins < (1u << 21); ++spins) {
    if (__all(__hip_atomic_load((gu64*)(sbase + voff), __ATOMIC_RELAXED, __HIP_MEMORY_SCOPE_AGENT) == want)) return;
    __builtin_amdgcn_s_sleep(4);
  }
}

DI float dot2bf(unsigned a, unsigned b, float c) {
  return __builtin_amdgcn_fdot2_f32_bf16(__builtin_bit_cast(bf16x2, a), __builtin_bit_cast(bf16x2, b), c, false);
}
DI float wave_sum63(float z) {
#define DPP_ADD(ctrl, rmask) z += __uint_as_float(__builtin_amdgcn_update_dpp(0u, __float_as_uint(z), ctrl, rmask, 0xf, true))
  DPP_ADD(0x111, 0xf); DPP_ADD(0x112, 0xf); DPP_ADD(0x114, 0xf); DPP_ADD(0x118, 0xf);
  DPP_ADD(0x142, 0xa); DPP_ADD(0x143, 0xc);
#undef DPP_ADD
  return z;
}

DI void nonce_load(u32x2& nonce, const char* ws) {
  asm volatile("global_load_dwordx2 %0, %1, %2 sc1" : "=v"(nonce) : "v"(0u), "s"(ws + WS_NONCE) : "memory");
}
DI u32x2 nonce_tag(u32x2 nonce) {
  u32x2 tag;
  tag[0] = nonce[0] + 1u;
  tag[1] = nonce[1] + (tag[0] == 0u ? 1u : 0u);
  return tag;
}

constexpr int P_SLOT = 49152;
constexpr int P_XOFF = 0, P_WOFF = 16384;
constexpr int QX_OFF = 0;
constexpr int KVC = 147456;
struct PDma {
  const char* xsrc; const char* wp[4]; unsigned xo[2], wstep[2], lds0; int w;
  DI void init(const char* xa, const char* wq, const char* wqkv, int mb, int j, unsigned lds0_, int w_, int lane) {
    lds0 = lds0_; w = w_;
    xsrc = xa + (size_t)mb * 128 * 2048;
#pragma unroll
    for (int e = 0; e < 2; ++e) {
      const int row = 16 * w + 8 * e + (lane >> 3);
      xo[e] = row * 2048 + (((lane & 7) ^ (4 * e + (lane >> 4))) << 4);
    }
#pragma unroll
    for (int e = 0; e < 4; ++e) {
      const int krow = 8 * w + 2 * e + (lane >> 5);
      const int c = (lane & 31) ^ ((krow & 3) << 2);
      wp[e] = c < 24 ? wqkv + (size_t)krow * 6144 + j * 384 + c * 16 : wq + (size_t)krow * 2048 + j * 128 + (c - 24) * 16;
      if (e < 2) wstep[e] = c < 24 ? 64 * 6144 : 64 * 2048;
    }
  }
  DI void piece(int pc, int st, int slot) {
    const unsigned lb = lds0 + slot * P_SLOT;
    if (pc < 2) glds16<0>(xo[pc], xsrc + st * 128, lb + P_XOFF + w * 2048 + pc * 1024);
    else { glds16v(wp[pc - 2], lb + P_WOFF + w * 4096 + (pc - 2) * 1024); wp[pc - 2] += wstep[pc & 1]; }
  }
  DI void stage(int st, int slot) {
#pragma unroll
    for (int pc = 0; pc < 6; ++pc) piece(pc, st, slot);
  }
};
struct PFrag { bf16x8 x[2]; bf16x8 w[2]; };

template <bool VT, class Tail, class Peek, class Check, class Hook>
DI void proj_loop(PDma& dma, char* lds, f32x16 (&acc)[2][2], Tail tail, Peek peek, Check check, Hook hook PT_ARG) {
  const int tid = threadIdx.x, lane = tid & 63;
  const int w = dma.w;
  const int wm = w & 1, wn = w >> 1;
  const int r = lane & 31, h = lane >> 5;
  const int t3 = (r >> 1) & 7;
  const int xa0 = P_XOFF + (wm * 64 + r) * 128 + ((h ^ t3) << 4);
  const int g16 = lane >> 4, q4 = (lane & 15) >> 2, p4 = lane & 3;
  const int wc = wn * 8 + 2 * (g16 & 1) + (p4 >> 1);
  const int wa0 = P_WOFF + (8 * h + q4) * 512 + ((wc ^ (q4 << 2)) << 4) + 8 * (p4 & 1);
  auto rdx = [&](PFrag& f, int i, int slot, int s) { f.x[i] = lds_rd128((lds_cptr)lds + slot * P_SLOT + (xa0 ^ (s << 5)) + i * 4096); };
  auto rdw = [&](PFrag& f, int j, int slot, int s) {
    lds_cptr p = (lds_cptr)lds + slot * P_SLOT + (wa0 ^ (j << 6)) + s * 8192;
    f.w[j] = cat8(lds_tr(p), lds_tr(p + 2048));
  };
  auto mm = [&](const PFrag& f, int i, int j) {
    if constexpr (VT) acc[i][j] = MFMA32(f.x[i], f.w[j], acc[i][j]);
    else acc[i][j] = MFMA32(f.w[j], f.x[i], acc[i][j]);
  };
  auto win = [&](int j, int pc, int slot) {
    if (j == -1) dma.piece(pc, 2, 2);
    else if (j <= 12) dma.piece(pc, j + 3, slot);
    else if (j <= 14 && pc < 4) tail((j - 13) * 4 + pc);
  };
  auto step = [&](const PFrag& c, PFrag& n, int rslot, int rs, bool rd, int pa, int wj, int dslot) {
    mm(c, 0, 0); SBAR();
    if (rd) { rdw(n, 0, rslot, rs); rdx(n, 0, rslot, rs); }
    SBAR();
    mm(c, 0, 1); SBAR();
    if (rd) { rdw(n, 1, rslot, rs); rdx(n, 1, rslot, rs); }
    SBAR();
    mm(c, 1, 0); SBAR();
    if (pa >= 0) win(wj, pa, dslot);
    SBAR();
    mm(c, 1, 1); SBAR();
    if (pa >= 0) win(wj, pa + 1, dslot);
    SBAR();
  };
  PFrag f0, f1;
  rdw(f0, 0, 0, 0); rdx(f0, 0, 0, 0); rdw(f0, 1, 0, 0); rdx(f0, 1, 0, 0);
  auto iter = [&](int it, int slot, int pslot, int nslot, bool hk) {
    step(f0, f1, slot, 1, true, 2, it - 1, pslot);
    step(f1, f0, slot, 2, true, 4, it - 1, pslot);
    step(f0, f1, slot, 3, true, -1, 0, 0);
    PTV(16 + 2 * it);
    asm volatile("s_waitcnt vmcnt(6) lgkmcnt(0)\n\ts_barrier" ::: "memory");
    PTV(17 + 2 * it);
    if (hk) hook(it);
    SBAR();
    step(f1, f0, nslot, 0, true, 0, it, slot);
  };
  iter(0, 0, 0, 1, true); iter(1, 1, 0, 2, true); iter(2, 2, 1, 0, true); iter(3, 0, 2, 1, true);
  iter(4, 1, 0, 2, true); iter(5, 2, 1, 0, true); iter(6, 0, 2, 1, true);
  int slot = 1, pslot = 0;
  for (int it = 7; it < 13; ++it) {
    const int nslot = slot == 2 ? 0 : slot + 1;
    iter(it, slot, pslot, nslot, false);
    pslot = slot;
    slot = nslot;
  }
  peek();
  step(f0, f1, 1, 1, true, 2, 12, 0);
  step(f1, f0, 1, 2, true, 4, 12, 0);
  step(f0, f1, 1, 3, true, -1, 0, 0);
  check();
  SBAR();
  step(f1, f0, 2, 0, true, 0, 13, 0);
  step(f0, f1, 2, 1, true, 2, 13, 0);
  step(f1, f0, 2, 2, true, -1, 0, 0);
  step(f0, f1, 2, 3, true, -1, 0, 0);
  asm volatile("s_waitcnt vmcnt(4) lgkmcnt(0)\n\ts_barrier" ::: "memory");
  SBAR();
  step(f1, f0, 0, 0, true, 0, 14, 0);
  step(f0, f1, 0, 1, true, 2, 14, 0);
  step(f1, f0, 0, 2, true, -1, 0, 0);
  step(f0, f1, 0, 3, true, -1, 0, 0);
  asm volatile("s_waitcnt lgkmcnt(0)\n\ts_barrier" ::: "memory");
  SBAR();
  step(f1, f0, 0, 0, false, -1, 0, 0);
}

DI void pre_kv(const char* __restrict__ xg, const char* __restrict__ wsel, char* __restrict__ ws, int rt, int t, char* lds, PDma& dma,
               u32x2& tag, f32x16& acc, lds_cptr& xlast, lds_cptr& wlast PT_ARG) {
  const int tid = threadIdx.x, lane = tid & 63;
  const int w = dma.w;
  const int r = lane & 31, h = lane >> 5;
  const unsigned lds0 = dma.lds0;
  u32x2 nonce;
  nonce_load(nonce, ws);
  const int xrow = 4 * w + (lane >> 4);
  const unsigned xo = xrow * 2048 + (((lane & 15) ^ (xrow & 15)) << 4), wo = w * 1024 + lane * 16;
  const char* xs = xg + (size_t)rt * 32 * 2048;
#define KV_OFF(u) ((u) == 0 || (u) == 7 ? KVC : P_SLOT + ((u) - 1) * 16384)
  auto sub = [&](int u) {
    const unsigned lb = lds0 + KV_OFF(u) + w * 1024;
    glds16<0>(xo, xs + u * 256, lb);
    glds16<0>(wo, wsel + u * 8192, lb + 8192);
  };
  sub(0); sub(1); sub(2); sub(3); sub(4); sub(5); sub(6);
  PT(0);
  const int g16 = lane >> 4, q4 = (lane & 15) >> 2, p4 = lane & 3;
  lds_cptr xa = (lds_cptr)lds + P_SLOT + r * 256 + (((2 * w + h) ^ (r & 15)) << 4);
  lds_cptr wa = (lds_cptr)lds + P_SLOT + 8192 + (16 * w + 8 * h + q4) * 64 + 32 * (g16 & 1) + 8 * p4;
  lds_cptr xc = xa + (KVC - P_SLOT), wc = wa + (KVC - P_SLOT);
  PIN(xa); PIN(wa); PIN(xc); PIN(wc);
  acc = (f32x16){};
  bf16x8 xf[4], wf[4];
#define KV_RD(i, u) do { \
    lds_cptr px_ = (u) == 0 ? xc : xa + ((u) - 1) * 16384; \
    lds_cptr pw_ = (u) == 0 ? wc : wa + ((u) - 1) * 16384; \
    xf[i] = lds_rd128(px_); wf[i] = cat8(lds_tr(pw_), lds_tr(pw_ + 256)); \
  } while (0)
#define KV_MM(i) do { if (t) acc = MFMA32(xf[i], wf[i], acc); else acc = MFMA32(wf[i], xf[i], acc); } while (0)
  asm volatile("s_waitcnt vmcnt(6)\n\ts_barrier" : "+v"(nonce)::"memory");
  PT(1);
  KV_RD(0, 0); KV_RD(1, 1); KV_RD(2, 2); KV_RD(3, 3);
  SBAR();
  dma.stage(0, 0);
  SBAR();
  KV_MM(0); KV_MM(1); KV_MM(2); KV_MM(3);
  asm volatile("s_waitcnt vmcnt(6) lgkmcnt(0)\n\ts_barrier" ::: "memory");
  PT(2);
  KV_RD(0, 4); KV_RD(1, 5); KV_RD(2, 6);
  SBAR();
  sub(7); dma.stage(1, 1);
  SBAR();
  KV_MM(0); KV_MM(1); KV_MM(2);
  asm volatile("s_waitcnt vmcnt(8) lgkmcnt(0)\n\ts_barrier" ::: "memory");
  PT(3);
  dma.piece(0, 2, 2); dma.piece(1, 2, 2);
#undef KV_RD
#undef KV_MM
#undef KV_OFF
  tag = nonce_tag(nonce);
  xlast = xc; wlast = wc;
  PT(9);
}

DI void pre_gate(const char* __restrict__ xg, const char* __restrict__ gw, char* __restrict__ ws, int row0, PDma& dma, u32x2& tag PT_ARG) {
  const int tid = threadIdx.x, lane = tid & 63;
  const int w = dma.w;
  u32x4 gx[2][2], gv[2];
  u32x2 nonce;
  const char* px = xg + (size_t)(row0 + 2 * w) * 2048;
  asm volatile("global_load_dwordx2 %6, %10, %11 sc1\n\t"
               "global_load_dwordx4 %0, %7, %8\n\tglobal_load_dwordx4 %1, %7, %8 offset:16\n\t"
               "global_load_dwordx4 %2, %7, %8 offset:2048\n\tglobal_load_dwordx4 %3, %7, %8 offset:2064\n\t"
               "global_load_dwordx4 %4, %7, %9\n\tglobal_load_dwordx4 %5, %7, %9 offset:16"
               : "=&v"(gx[0][0]), "=&v"(gx[0][1]), "=&v"(gx[1][0]), "=&v"(gx[1][1]), "=&v"(gv[0]), "=&v"(gv[1]), "=&v"(nonce)
               : "v"(lane * 32), "s"(px), "s"(gw), "v"(0u), "s"(ws + WS_NONCE) : "memory");
  dma.stage(0, 0);
  PT(0);
  asm volatile("s_barrier" ::: "memory");
  PT(1);
  dma.stage(1, 1); dma.piece(0, 2, 2); dma.piece(1, 2, 2);
  PT(2);
  asm volatile("s_waitcnt vmcnt(8)\n\ts_barrier"
               : "+v"(gx[0][0]), "+v"(gx[0][1]), "+v"(gx[1][0]), "+v"(gx[1][1]), "+v"(gv[0]), "+v"(gv[1]), "+v"(nonce)::"memory");
  PT(3);
  tag = nonce_tag(nonce);
#pragma unroll
  for (int i = 0; i < 2; ++i) {
    float z = 0.f, z2 = 0.f;
#pragma unroll
    for (int e = 0; e < 4; ++e) { z = dot2bf(gx[i][0][e], gv[0][e], z); z2 = dot2bf(gx[i][1][e], gv[1][e], z2); }
    z += z2;
    z = wave_sum63(z);
    const float g = 1.f / (1.f + __expf(-z));
    if (lane == 63) asm volatile("global_store_dword %0, %1, %2 sc1\n\ts_nop 0" :: "v"(0u), "v"(g), "s"(ws + WS_G + (row0 + 2 * w + i) * 4) : "memory");
  }
}

constexpr int SL = 32768;
constexpr int R_LDS = 5 * SL;
constexpr int XCH_OFF = 0;
constexpr int WO_OFF = 2 * SL;

DI int crow(int r, int h) { return (r & 3) + 8 * (r >> 2) + 4 * h; }
DI float swap_half(float x) {
  auto rr = __builtin_amdgcn_permlane32_swap(__float_as_uint(x), __float_as_uint(x), false, false);
  return __uint_as_float((threadIdx.x & 32) ? rr[0] : rr[1]);
}
DI void exp16(f32x16& s) {
#pragma unroll
  for (int i = 0; i < 16; ++i) s[i] = __builtin_amdgcn_exp2f(s[i]);
}
#define AE(S, i) S[i] = __builtin_amdgcn_exp2f(S[i])
#define AC(P, S, i) P[(i) & 3] = cvtpk(S[2 * (i)], S[2 * (i) + 1])
#define ABC(x) __builtin_bit_cast(bf16x8, x)
#define APHASE(QK, PV, SX, PXa, PXb, SY, CINY, PYa, PYb, QY0, QY1, OY, LY, KF0, KF1, VF0, VF1, R1, R2) do { \
  if (QK) { SY = MFMA32(KF0, QY0, CINY); PIN(SY); } SBAR(); \
  AE(SX, 0); AE(SX, 1); AE(SX, 2); AE(SX, 3); PIN(SX); SBAR(); \
  if (QK) { SY = MFMA32(KF1, QY1, SY); PIN(SY); } SBAR(); \
  R1; \
  AC(PXa, SX, 0); AC(PXa, SX, 1); AE(SX, 4); AE(SX, 5); AE(SX, 6); PIN(SX); PIN(PXa); SBAR(); \
  if (PV) { OY = MFMA32(VF0, ABC(PYa), OY); PIN(OY); } SBAR(); \
  AE(SX, 7); AC(PXa, SX, 2); AE(SX, 8); AE(SX, 9); AC(PXa, SX, 3); PIN(SX); PIN(PXa); SBAR(); \
  if (PV) { LY = MFMA16(ones, ABC(PYa), LY); PIN(LY); } SBAR(); \
  R2; \
  AE(SX, 10); AE(SX, 11); PIN(SX); SBAR(); \
  if (PV) { OY = MFMA32(VF1, ABC(PYb), OY); PIN(OY); } SBAR(); \
  AC(PXb, SX, 4); AC(PXb, SX, 5); AE(SX, 12); AE(SX, 13); AE(SX, 14); PIN(SX); PIN(PXb); SBAR(); \
  if (PV) { LY = MFMA16(ones, ABC(PYb), LY); PIN(LY); } SBAR(); \
  AE(SX, 15); AC(PXb, SX, 6); AC(PXb, SX, 7); PIN(PXb); SBAR(); \
} while (0)

template <bool MASKED>
DI void attn_exact(const char* __restrict__ kv, int t0, int t1, int qt, const bf16x8& q0, const bf16x8& q1, const bf16x8& ones,
                   f32x16& o, float& l) {
  const int lane = threadIdx.x & 63, r = lane & 31, h = lane >> 5;
  float m = -3.0e38f;
  for (int t = t0; t < t1; ++t) {
    const char* p = kv + (size_t)t * 4096 + lane * 16;
    bf16x8 k0 = gload16_now(p), k1 = gload16_now(p + 1024);
    f32x16 s = {};
    s = MFMA32(k0, q0, s); s = MFMA32(k1, q1, s);
#pragma unroll
    for (int i = 0; i < 16; ++i) {
      bool ok = true;
      if (MASKED) { const int c = crow(i, h); ok = (t == qt) ? (c <= r) : ((t == qt - 8) ? (c > r) : true); }
      m = fmaxf(m, ok ? s[i] : -3.0e38f);
    }
  }
  m = fmaxf(m, swap_half(m));
  o = (f32x16){};
  f32x4 la = {};
  for (int t = t0; t < t1; ++t) {
    const char* p = kv + (size_t)t * 4096 + lane * 16;
    bf16x8 k0 = gload16_now(p), k1 = gload16_now(p + 1024);
    bf16x8 v0 = gload16_now(p + 2048), v1 = gload16_now(p + 3072);
    f32x16 s = {};
    s = MFMA32(k0, q0, s); s = MFMA32(k1, q1, s);
#pragma unroll
    for (int i = 0; i < 16; ++i) {
      bool ok = true;
      if (MASKED) { const int c = crow(i, h); ok = (t == qt) ? (c <= r) : ((t == qt - 8) ? (c > r) : true); }
      s[i] = ok ? __builtin_amdgcn_exp2f(s[i] - m) : 0.f;
    }
    bf16x8 pa = pack8(s, 0), pb = pack8(s, 8);
    o = MFMA32(v0, pa, o); o = MFMA32(v1, pb, o);
    la = MFMA16(ones, pa, la); la = MFMA16(ones, pb, la);
  }
  l = la[0];
}
DI bool ref_unsafe(const f32x16& o, float l) {
  float z = 0.f;
#pragma unroll
  for (int i = 0; i < 16; ++i) z = __builtin_fmaf(o[i], 0.f, z);
  const bool ok = (l >= 5.421010862e-20f) && (l <= 1.8446744e19f) && (z == 0.f);
  return __any(!ok);
}

__global__ __launch_bounds__(512, 2) void k_fused(const char* __restrict__ xa, const char* __restrict__ wq,
                                                  const char* __restrict__ wk, const char* __restrict__ wv,
                                                  const char* __restrict__ woa, const char* __restrict__ wqkv,
                                                  const char* __restrict__ wob, const char* __restrict__ gw,
                                                  char* __restrict__ ws, char* __restrict__ out) {
  extern __shared__ __attribute__((aligned(16))) char lds[];
  STAMP_DECL STAMP(0); TRACE_DECL
  const int b = blockIdx.x;
  const int xcd = b & 7, idx = b >> 3;
  const int tid = threadIdx.x, lane = tid & 63;
  const int w = rfl(tid >> 6);
  const int r = lane & 31, h = lane >> 5;
  const unsigned lds0 = (unsigned)(uintptr_t)lds;
  const unsigned lo16 = lane * 16;
  const int pmb = (xcd & 1) * 8 + (idx & 7), pj = (xcd >> 1) * 4 + ((idx >> 3) & 1) * 2 + (idx >> 4);
  const int pr = w & 3, half = w >> 2;
  const char* kva = ws + WS_KVA;
  auto pieceA = [&](int k, int sl, int i) {
    const char* src = kva + (size_t)(half * 32 + 4 * k + pr) * 4096;
    const unsigned dst = lds0 + sl * SL + w * 4096;
    if (i == 0) glds16_sc1<0>(lo16, src, dst);
    else if (i == 1) glds16_sc1<1024>(lo16, src, dst);
    else if (i == 2) glds16_sc1<2048>(lo16, src, dst);
    else glds16_sc1<3072>(lo16, src, dst);
  };
  PDma dma;
  dma.init(xa, wq, wqkv, pmb, pj, lds0, w, lane);
  u32x2 tag;
  const bool evenj = (pj & 1) == 0;
  const int kvrt = pmb * 4 + (pj >> 2), kvt = (pj >> 1) & 1;
  PT(11);
  f32x16 kvacc = {};
  lds_cptr kvxl = (lds_cptr)lds, kvwl = (lds_cptr)lds;
  if (evenj) pre_kv(xa, kvt ? wv : wk, ws, kvrt, kvt, lds, dma, tag, kvacc, kvxl, kvwl PT_PASS);
  else pre_gate(xa, gw, ws, pmb * 128 + (pj >> 1) * 16, dma, tag PT_PASS);
  PT(12);
  STAMP(1);
  const int wn = w >> 1, pwm = w & 1;
  const int ct = 6 * pj + 2 * wn;
  const bool hasB = wn < 3;
  bf16x8 held[8];
  {
    f32x16 acc[2][2] = {};
    u32x2 kv0 = {~tag[0], 0u}, kv1 = kv0;
    auto tail = [&](int n) { pieceA(n >> 2, 2 + (n >> 2), n & 3); };
    auto peek = [&]() { flag_peek(kv0, lane * 8, ws + WS_FKV); flag_peek(kv1, lane * 8, ws + WS_FKV + 512); };
    auto check = [&]() {
      asm volatile("s_waitcnt vmcnt(4) lgkmcnt(0)" : "+v"(kv0), "+v"(kv1)::"memory");
      if (w == 0) { flag_wait(kv0, lane * 8, ws + WS_FKV, tag); flag_wait(kv1, lane * 8, ws + WS_FKV + 512, tag); }
      asm volatile("s_barrier" ::: "memory");
    };
    float* red = (float*)(lds + KVC) + lane * 4;
    auto put = [&](int s4) {
#pragma unroll
      for (int g = 0; g < 4; ++g) *(f32x4*)(red + (s4 * 4 + g) * 256) = (f32x4){kvacc[4 * g], kvacc[4 * g + 1], kvacc[4 * g + 2], kvacc[4 * g + 3]};
    };
    auto get = [&](int s4) {
#pragma unroll
      for (int g = 0; g < 4; ++g) {
        const f32x4 a = *(const f32x4*)(red + (s4 * 4 + g) * 256);
#pragma unroll
        for (int e = 0; e < 4; ++e) kvacc[4 * g + e] += a[e];
      }
    };
    auto hook = [&](int it) {
      if (evenj) {
        if (it == 0) {
          const bf16x8 xf = lds_rd128(kvxl), wf = cat8(lds_tr(kvwl), lds_tr(kvwl + 256));
          if (kvt) kvacc = MFMA32(xf, wf, kvacc); else kvacc = MFMA32(wf, xf, kvacc);
        }
        else if (it == 1) { if (w >= 4) put(w - 4); }
        else if (it == 2) { if (w < 4) get(w); }
        else if (it == 3) { if (w >= 1 && w < 4) put(w); }
        else if (it == 4) {
          if (w == 0) {
            get(1); get(2); get(3);
            char* dst = ws + WS_KVA + (size_t)kvrt * 4096 + kvt * 2048;
            gstore16_sc1<0>(lo16, dst, pack8(kvacc, 0));
            gstore16_sc1<1024>(lo16, dst, pack8(kvacc, 8));
          }
        } else if (it == 6) { if (tid == 0) flag_set(ws + WS_FKV + (kvrt * 2 + kvt) * 8, tag); }
      }
    };
    if (hasB && ct >= 64) proj_loop<true>(dma, lds, acc, tail, peek, check, hook PT_PASS);
    else proj_loop<false>(dma, lds, acc, tail, peek, check, hook PT_PASS);
    STAMP(2);
    const float sc = (!hasB || ct < 32) ? QSCALE : 1.f;
#pragma unroll
    for (int i = 0; i < 2; ++i)
#pragma unroll
      for (int j = 0; j < 2; ++j) {
        held[(i * 2 + j) * 2] = pack8s(acc[i][j], 0, sc);
        held[(i * 2 + j) * 2 + 1] = pack8s(acc[i][j], 8, sc);
      }
  }
  bf16x8 qb[2], q[2][2];
  typedef __attribute__((address_space(3))) bf16x8* lds_v8;
  if (hasB) {
    const unsigned hstep = ct < 32 ? 2048 : 4096;
    char* hbase = ws + (ct < 32 ? WS_QB : WS_KVB + (ct >= 64 ? 2048 : 0)) + (size_t)((ct & 31) * 64 + pmb * 4 + pwm * 2) * hstep;
#pragma unroll
    for (int n = 0; n < 8; ++n) {
      char* dst = hbase + (size_t)((n >> 2) + ((n >> 1) & 1) * 64) * hstep;
      if (n & 1) gstore16_sc1<1024>(lo16, dst, held[n]);
      else gstore16_sc1<0>(lo16, dst, held[n]);
    }
    asm volatile("s_waitcnt vmcnt(12)" ::: "memory");
  } else {
#pragma unroll
    for (int n = 0; n < 8; ++n)
      *(lds_v8)((lds_ptr)lds + QX_OFF + (((pwm * 2 + (n >> 2)) * 2 + ((n >> 1) & 1)) * 2 + (n & 1)) * 1024 + lo16) = held[n];
    asm volatile("s_waitcnt vmcnt(4)" ::: "memory");
  }
  asm volatile("s_waitcnt lgkmcnt(0)\n\ts_barrier" ::: "memory");
#pragma unroll
  for (int u = 0; u < 2; ++u)
#pragma unroll
    for (int s2 = 0; s2 < 2; ++s2) q[u][s2] = lds_rd128((lds_cptr)lds + QX_OFF + ((pr * 2 + u) * 2 + s2) * 1024 + lo16);
  STAMP(3);

  const int qt = pmb * 4 + pr, hA = 2 * pj;
  const int hB = (b & 7) + 8 * (b >> 6), rb = (b >> 3) & 7;
  const int qtB = rb * 8 + w;
  const int mb = (xcd & 3) * 4 + (idx & 3), nb = (xcd >> 2) * 8 + (idx >> 2);
  const char* kvb = ws + WS_KVB + (size_t)hB * 64 * 4096;
  bf16x8 ones;
  {
    const short v = ((((lane >> 4) & 1) == (((lane & 15) >> 2) & 1)) ? (short)0x3f80 : (short)0);
    ones = (bf16x8){v, v, v, v, v, v, v, v};
  }
  auto pieceB = [&](int n) {
    int gt = rb * 8 - 8 + 2 * w + (n >> 2);
    gt = gt < 0 ? 0 : gt;
    const char* src = kvb + (size_t)gt * 4096;
    const unsigned dst = lds0 + (2 * w + (n >> 2)) * 4096;
    if ((n & 3) == 0) glds16_sc1<0>(lo16, src, dst);
    else if ((n & 3) == 1) glds16_sc1<1024>(lo16, src, dst);
    else if ((n & 3) == 2) glds16_sc1<2048>(lo16, src, dst);
    else glds16_sc1<3072>(lo16, src, dst);
  };
  const char* pqb = ws + WS_QB + (size_t)(hB * 64 + qtB) * 2048;
  unsigned wo[2];
#pragma unroll
  for (int e = 0; e < 2; ++e) {
    const int row = 16 * w + 8 * e + (lane >> 3);
    const int c = (lane & 7) ^ (((row >> 1) & 1) << 2);
    wo[e] = row * 2048 + c * 16;
  }
  auto pieceW = [&](int st, int e) {
    const char* src = (st < 8 ? wob : woa) + (size_t)(st & 7) * 128 * 2048 + nb * 128;
    glds16<0>(wo[e], src, lds0 + WO_OFF + (st % 6) * 16384 + w * 2048 + e * 1024);
  };
#define NOP (void)0
  u32x2 seen;

  const f32x16 zero = {};
  f32x16 o0 = {}, o1 = {}, s0, s1;
  f32x4 l0 = {}, l1 = {};
  u32x4 p0a, p0b, p1a, p1b;
  bf16x8 ka0, ka1, kb0, kb1, va0, va1, vb0, vb1;
  lds_cptr kvbase = (lds_cptr)lds + half * 16384 + lane * 16;
#define APAIR(pp, SL0, SL1, FIRST, LAST, PA, PB, MID, D0, D1, D2, D3) do { \
    lds_cptr cur = kvbase + (SL0) * SL + ((pp) & 1) * 8192; PIN(cur);     \
    lds_cptr nxt = (SL0) == (SL1) && !((pp) & 1) ? cur + 8192 : kvbase + (SL1) * SL; \
    __builtin_amdgcn_s_setprio(PA); \
    if (FIRST) { ka0 = lds_rd128(cur); ka1 = lds_rd128(cur + 1024); s0 = MFMA32(ka0, q[0][0], zero); s0 = MFMA32(ka1, q[0][1], s0); } \
    SBAR(); \
    APHASE(1, !(FIRST), s0, p0a, p0b, s1, zero, p1a, p1b, q[1][0], q[1][1], o1, l1, ka0, ka1, vb0, vb1, \
           (kb0 = lds_rd128(cur + 4096), kb1 = lds_rd128(cur + 5120)), (va0 = lds_rd128(cur + 2048), va1 = lds_rd128(cur + 3072))); \
    D0; SBAR(); \
    APHASE(1, 1, s1, p1a, p1b, s0, zero, p0a, p0b, q[0][0], q[0][1], o0, l0, kb0, kb1, va0, va1, (void)0, (void)0); \
    D1; SBAR(); \
    ATRACE(2 * (pp)); \
    MID; \
    ATRACE(2 * (pp) + 1); \
    __builtin_amdgcn_s_setprio(PB); SBAR(); \
    APHASE(1, 1, s0, p0a, p0b, s1, zero, p1a, p1b, q[1][0], q[1][1], o1, l1, kb0, kb1, va0, va1, \
           if (!(LAST)) (ka0 = lds_rd128(nxt), ka1 = lds_rd128(nxt + 1024)), (vb0 = lds_rd128(cur + 4096 + 2048), vb1 = lds_rd128(cur + 4096 + 3072))); \
    D2; SBAR(); \
    APHASE(!(LAST), 1, s1, p1a, p1b, s0, zero, p0a, p0b, q[0][0], q[0][1], o0, l0, ka0, ka1, vb0, vb1, (void)0, (void)0); \
    D3; SBAR(); \
    if (LAST) { \
      o1 = MFMA32(vb0, ABC(p1a), o1); o1 = MFMA32(vb1, ABC(p1b), o1); \
      l1 = MFMA16(ones, ABC(p1a), l1); l1 = MFMA16(ones, ABC(p1b), l1); \
    } \
  } while (0)
#define ATRACE(i) do {} while (0)
#define DA(k, sl, i) pieceA(k, sl, i)
#define DA2(k, sl, i) (pieceA(k, sl, i), pieceA(k, sl, (i) + 1))
#define DB2(n) (pieceB(n), pieceB((n) + 1))
#define DW(st, e) pieceW(st, e)
#define BAR_V(n) asm volatile("s_waitcnt vmcnt(" #n ")\n\ts_barrier" ::: "memory")
  STAMP(4); STAMP(5);
  const int fbm = 2 * rb - 2 + (lane & 3);
  const unsigned fbo = ((fbm < 0 ? 2 * rb : fbm) * 16 + ((lane >> 2) % 3 * 32 + hB) / 6) * 8;
  seen = (u32x2){~tag[0], 0u};
  APAIR(0, 2, 2, 1, 0, 2, 1, NOP, DA(2, 4, 0), DA(2, 4, 1), DA(2, 4, 2), DA(2, 4, 3));
  APAIR(1, 2, 3, 0, 0, 0, 3, { BAR_V(4); if (tid == 0) flag_set(ws + WS_FPB + (pmb * 16 + pj) * 8, tag);   },
        NOP, NOP, NOP, NOP);
  APAIR(2, 3, 3, 0, 0, 2, 1, NOP, NOP, NOP, NOP, NOP);
  flag_peek(seen, fbo, ws + WS_FPB);
  APAIR(3, 3, 4, 0, 0, 0, 3, { asm volatile("s_waitcnt vmcnt(0)" : "+v"(seen)::"memory"); if (w == 0) flag_wait(seen, fbo, ws + WS_FPB, tag); asm volatile("s_barrier" ::: "memory"); },
        NOP, NOP, DB2(0), DB2(2));
  APAIR(4, 4, 4, 0, 0, 2, 2, NOP, DB2(4), DB2(6), (gload16_sc1<0>(qb[0], lo16, pqb), gload16_sc1<1024>(qb[1], lo16, pqb)), DA2(3, 2, 0));
  APAIR(5, 4, 4, 0, 1, 1, 0, NOP, DA2(3, 2, 2), NOP, NOP, NOP);
  asm volatile("s_waitcnt vmcnt(0) lgkmcnt(0)\n\ts_barrier" : "+v"(qb[0]), "+v"(qb[1])::"memory");
  STAMP(6);
  {
    f32x16 o = {};
    f32x4 la = {};
    lds_cptr bb = (lds_cptr)lds + w * 4096 + lane * 16;
    f32x16 mD;
#pragma unroll
    for (int i = 0; i < 16; ++i) mD[i] = crow(i, h) <= r ? 0.f : -1.0e30f;
    if (rb != 0) {
      f32x16 sa, sb;
#pragma unroll
      for (int i = 0; i < 16; ++i) sa[i] = -1.0e30f - mD[i];
      u32x4 pa0 = {}, pa1 = {}, pb0 = {}, pb1 = {};
      bf16x8 k0, k1, k2, k3, v0, v1, v2, v3;
      k0 = lds_rd128(bb); k1 = lds_rd128(bb + 1024);
      k2 = lds_rd128(bb + 4096); k3 = lds_rd128(bb + 4096 + 1024);
      sa = MFMA32(k0, qb[0], sa); sa = MFMA32(k1, qb[1], sa);
      SBAR();
#define BRDK(KA, KB, j) (KA = lds_rd128(bb + (j) * 4096), KB = lds_rd128(bb + (j) * 4096 + 1024))
#define BRDV(VA, VB, j) (VA = lds_rd128(bb + (j) * 4096 + 2048), VB = lds_rd128(bb + (j) * 4096 + 3072))
#define B_DMA(n) do { pieceA(4 + (n) / 4, 3 + (n) / 4, (n) & 3); SBAR(); } while (0)
      __builtin_amdgcn_s_setprio(3);
      APHASE(1, 0, sa, pa0, pa1, sb, zero, pb0, pb1, qb[0], qb[1], o, la, k2, k3, v2, v3, BRDK(k0, k1, 2), BRDV(v0, v1, 0));
      B_DMA(0); B_DMA(1);
      APHASE(1, 1, sb, pb0, pb1, sa, zero, pa0, pa1, qb[0], qb[1], o, la, k0, k1, v0, v1, BRDK(k2, k3, 3), BRDV(v2, v3, 1));
      B_DMA(2); B_DMA(3);
      __builtin_amdgcn_s_setprio(2);
      APHASE(1, 1, sa, pa0, pa1, sb, zero, pb0, pb1, qb[0], qb[1], o, la, k2, k3, v2, v3, BRDK(k0, k1, 4), BRDV(v0, v1, 2));
      B_DMA(4); B_DMA(5);
      APHASE(1, 1, sb, pb0, pb1, sa, zero, pa0, pa1, qb[0], qb[1], o, la, k0, k1, v0, v1, BRDK(k2, k3, 5), BRDV(v2, v3, 3));
      B_DMA(6); B_DMA(7);
      __builtin_amdgcn_s_setprio(1);
      APHASE(1, 1, sa, pa0, pa1, sb, zero, pb0, pb1, qb[0], qb[1], o, la, k2, k3, v2, v3, BRDK(k0, k1, 6), BRDV(v0, v1, 4));
      APHASE(1, 1, sb, pb0, pb1, sa, zero, pa0, pa1, qb[0], qb[1], o, la, k0, k1, v0, v1, BRDK(k2, k3, 7), BRDV(v2, v3, 5));
      __builtin_amdgcn_s_setprio(0);
      APHASE(1, 1, sa, pa0, pa1, sb, zero, pb0, pb1, qb[0], qb[1], o, la, k2, k3, v2, v3, BRDK(k0, k1, 8), BRDV(v0, v1, 6));
      APHASE(1, 1, sb, pb0, pb1, sa, mD, pa0, pa1, qb[0], qb[1], o, la, k0, k1, v0, v1, (void)0, BRDV(v2, v3, 7));
      APHASE(0, 1, sa, pa0, pa1, sb, zero, pb0, pb1, qb[0], qb[1], o, la, k2, k3, v2, v3, (void)0, BRDV(v0, v1, 8));
      o = MFMA32(v0, ABC(pa0), o); o = MFMA32(v1, ABC(pa1), o);
      la = MFMA16(ones, ABC(pa0), la); la = MFMA16(ones, ABC(pa1), la);
    } else {
#pragma unroll
      for (int n = 0; n < 8; ++n) B_DMA(n);
      auto tile = [&](int j, const f32x16& cin) {
        lds_cptr p = bb + j * 4096;
        bf16x8 k0 = lds_rd128(p), k1 = lds_rd128(p + 1024), v0 = lds_rd128(p + 2048), v1 = lds_rd128(p + 3072);
        f32x16 s = MFMA32(k0, qb[0], cin);
        s = MFMA32(k1, qb[1], s);
        exp16(s);
        bf16x8 pa = pack8(s, 0), pb = pack8(s, 8);
        o = MFMA32(v0, pa, o); o = MFMA32(v1, pb, o);
        la = MFMA16(ones, pa, la); la = MFMA16(ones, pb, la);
      };
      for (int j = 8 - w; j < 8; ++j) tile(j, zero);
      tile(8, mD);
    }
    float l = la[0];
    if (__builtin_expect(ref_unsafe(o, l), 0)) attn_exact<true>(kvb, qtB >= 8 ? qtB - 8 : 0, qtB + 1, qtB, qb[0], qb[1], ones, o, l);
    const float sc = __builtin_amdgcn_rcpf(l);
    char* dst = ws + WS_O + (size_t)(qtB * 64 + 32 + hB) * 2048;
    gstore16_sc1<0>(lo16, dst, pack8s(o, 0, sc));
    gstore16_sc1<1024>(lo16, dst, pack8s(o, 8, sc));
  }
  asm volatile("s_waitcnt vmcnt(2) lgkmcnt(0)\n\ts_barrier" ::: "memory");
  STAMP(7);
  const int wm = w & 3, kh = w >> 2;
  const char* obase = ws + WS_O + (size_t)(mb * 4 + wm) * 64 * 2048 + kh * 4096;
  bf16x8 of[5][4];
  auto loadO1 = [&](int st, int i, bf16x8& f) {
    const char* src = obase + (size_t)(st < 8 ? 32 + 4 * st : 4 * (st - 8)) * 2048;
    if (i == 0) gload16_sc1<0>(f, lo16, src);
    else if (i == 1) gload16_sc1<1024>(f, lo16, src);
    else if (i == 2) gload16_sc1<2048>(f, lo16, src);
    else gload16_sc1<3072>(f, lo16, src);
  };
#define LO(st, i) loadO1(st, i, of[st][i])
  auto loadO = [&](int st, bf16x8 (&f)[4]) {
    const char* src = obase + (size_t)(st < 8 ? 32 + 4 * st : 4 * (st - 8)) * 2048;
    gload16_sc1<0>(f[0], lo16, src); gload16_sc1<1024>(f[1], lo16, src);
    gload16_sc1<2048>(f[2], lo16, src); gload16_sc1<3072>(f[3], lo16, src);
  };
  const unsigned fob = ((mb >> 1) * 32 + (lane & 31)) * 8;
  seen = (u32x2){~tag[0], 0u};
  unsigned warm = 0;
  APAIR(6, 2, 2, 1, 0, 3, 3, NOP, DA(6, 0, 0), DA(6, 0, 1), DA(6, 0, 2), DA(6, 0, 3));
  APAIR(7, 2, 3, 0, 0, 3, 2, NOP, DA(7, 1, 0), DA(7, 1, 1), DA(7, 1, 2), DA(7, 1, 3));
  {
    const int R = 768 + (idx & 3) * 320 + w * 40 + (lane < 40 ? lane : 39);
    const char* pw = (R < 1024 ? wob + (size_t)R * 2048 : woa + (size_t)(R - 1024) * 2048) + nb * 128;
    asm volatile("global_load_dword %0, %1, off" : "+v"(warm) : "v"(pw) : "memory");
  }
  APAIR(8, 3, 3, 0, 0, 2, 2, NOP, NOP, NOP, NOP, NOP);
  APAIR(9, 3, 4, 0, 0, 1, 1, NOP, NOP, NOP, NOP, NOP);
  APAIR(10, 4, 4, 0, 0, 1, 0, NOP, NOP, NOP, NOP, NOP);
  APAIR(11, 4, 0, 0, 0, 0, 3, { asm volatile("s_waitcnt vmcnt(0)\n\ts_barrier" : "+v"(warm)::"memory");
                                if (tid == 0) flag_set(ws + WS_FOB + (rb * 32 + hB) * 8, tag);   }, NOP, NOP, NOP, NOP);
  APAIR(12, 0, 0, 0, 0, 3, 2, NOP, DW(0, 0), DW(0, 1), DW(1, 0), DW(1, 1));
  flag_peek(seen, fob, ws + WS_FOB);
  APAIR(13, 0, 1, 0, 0, 2, 1, NOP, DW(2, 0), DW(2, 1), DW(3, 0), DW(3, 1));
  APAIR(14, 1, 1, 0, 0, 0, 3, { asm volatile("s_waitcnt vmcnt(0)" : "+v"(seen)::"memory"); if (w == 0) flag_wait(seen, fob, ws + WS_FOB, tag); asm volatile("s_barrier" ::: "memory"); },
        NOP, NOP, (LO(0, 0), LO(0, 1), DW(4, 0)), (LO(0, 2), LO(0, 3), DW(4, 1)));
  APAIR(15, 1, 1, 0, 1, 2, 1, NOP, (LO(1, 0), LO(1, 1), DW(5, 0)), (LO(1, 2), LO(1, 3), DW(5, 1)), (LO(2, 0), LO(2, 1)), (LO(2, 2), LO(2, 3)));
  STAMP(8);

  loadO(3, of[3]);
  asm volatile("s_waitcnt lgkmcnt(0)\n\ts_barrier" ::: "memory");
  {
    float* xb = (float*)(lds + XCH_OFF) + w * (5 * 256);
    const f32x16& os = half ? o0 : o1;
    const float ls = half ? l0[0] : l1[0];
#pragma unroll
    for (int g = 0; g < 4; ++g) *(f32x4*)(xb + g * 256 + lane * 4) = (f32x4){os[4 * g], os[4 * g + 1], os[4 * g + 2], os[4 * g + 3]};
    xb[4 * 256 + lane] = ls;
  }
  asm volatile("s_waitcnt lgkmcnt(0)\n\ts_barrier" ::: "memory");
  {
    const float* xb = (const float*)(lds + XCH_OFF) + (w ^ 4) * (5 * 256);
    f32x16 o = half ? o1 : o0;
    float l = half ? l1[0] : l0[0];
#pragma unroll
    for (int g = 0; g < 4; ++g) {
      f32x4 v = *(const f32x4*)(xb + g * 256 + lane * 4);
#pragma unroll
      for (int e = 0; e < 4; ++e) o[4 * g + e] += v[e];
    }
    l += xb[4 * 256 + lane];
    if (__builtin_expect(ref_unsafe(o, l), 0)) {
      if (half) attn_exact<false>(kva, 0, 64, 0, q[1][0], q[1][1], ones, o, l);
      else attn_exact<false>(kva, 0, 64, 0, q[0][0], q[0][1], ones, o, l);
    }
    const float sc = __builtin_amdgcn_rcpf(l);
    char* dst = ws + WS_O + (size_t)(qt * 64 + hA + half) * 2048;
    gstore16_sc1<0>(lo16, dst, pack8s(o, 0, sc));
    gstore16_sc1<1024>(lo16, dst, pack8s(o, 8, sc));
  }
  STAMP(9);
  {
    const int g16 = lane >> 4, q4 = (lane & 15) >> 2, p4 = lane & 3;
    const int wc = 2 * (g16 & 1) + (p4 >> 1);
    const int wa0 = (kh * 64 + 4 * h + q4) * 128 + ((wc ^ (((q4 >> 1) & 1) << 2)) << 4) + 8 * (p4 & 1);
    lds_cptr wbase[2][2] = {{(lds_cptr)lds + WO_OFF + wa0, (lds_cptr)lds + WO_OFF + (wa0 ^ 64)},
                            {(lds_cptr)lds + WO_OFF + 49152 + wa0, (lds_cptr)lds + WO_OFF + 49152 + (wa0 ^ 64)}};
    PIN(wbase[0][0]); PIN(wbase[0][1]); PIN(wbase[1][0]); PIN(wbase[1][1]);
    f32x16 accB[2] = {}, accA[2] = {};
    bf16x8 wf[4][2];
    const unsigned foa = (mb * 16 + (lane & 15)) * 8;
    u32x2 seenA = {~tag[0], 0u};
    float g = 0.f;
    auto rdw = [&](bf16x8 (&f)[2], int st, int ks) {
#pragma unroll
      for (int j = 0; j < 2; ++j) {
        lds_cptr p = wbase[(st % 6) / 3][j] + ((st % 6) % 3) * 16384 + ks * 2048;
        f[j] = cat8(lds_tr(p), lds_tr(p + 1024));
      }
    };
    asm volatile("s_waitcnt vmcnt(17)" : "+v"(of[0][0]), "+v"(of[0][1]), "+v"(of[0][2]), "+v"(of[0][3])::"memory");
    rdw(wf[0], 0, 0); rdw(wf[1], 0, 1);
#define OTRACE(i) do {} while (0)
#define O_MM(ACC, f, a) do { ACC[0] = MFMA32(f[0], a, ACC[0]); SBAR(); ACC[1] = MFMA32(f[1], a, ACC[1]); SBAR(); } while (0)
#define O_STAGE(st, ACC, VM, PRE, POST) do { \
      rdw(wf[2], st, 2); SBAR(); O_MM(ACC, wf[0], of[(st) % 5][0]); \
      rdw(wf[3], st, 3); SBAR(); O_MM(ACC, wf[1], of[(st) % 5][1]); \
      if ((st) < 15) { \
        OTRACE(3 * (st)); \
        asm volatile("s_waitcnt vmcnt(" #VM ")" : "+v"(of[((st) + 1) % 5][0]), "+v"(of[((st) + 1) % 5][1]), \
                     "+v"(of[((st) + 1) % 5][2]), "+v"(of[((st) + 1) % 5][3]), "+v"(seenA)::"memory"); \
        PRE; \
        OTRACE(3 * (st) + 1); \
        asm volatile("s_barrier" ::: "memory"); \
        OTRACE(3 * (st) + 2); \
        POST; \
        SBAR(); \
        rdw(wf[0], (st) + 1, 0); \
      } \
      SBAR(); O_MM(ACC, wf[2], of[(st) % 5][2]); \
      if ((st) < 15) rdw(wf[1], (st) + 1, 1); \
      SBAR(); O_MM(ACC, wf[3], of[(st) % 5][3]); \
    } while (0)
#define OW(st) (pieceW(st, 0), pieceW(st, 1))
#define OL(st) loadO(st, of[(st) % 5])
    O_STAGE(0, accB, 11, NOP, OL(4));
    O_STAGE(1, accB, 4, NOP, { if (tid == 0) flag_set(ws + WS_FOA + (pmb * 16 + pj) * 8, tag);   OW(6); OL(5); });
    O_STAGE(2, accB, 10, NOP, (OW(7), OL(6)));
    O_STAGE(3, accB, 12, NOP, (flag_peek(seenA, foa, ws + WS_FOA), OW(8), OL(7)));
    O_STAGE(4, accB, 13, NOP, OW(9));
    O_STAGE(5, accB, 8, if (w == 0) flag_wait(seenA, foa, ws + WS_FOA, tag),
            (gload4_sc1(g, (wm * 32 + r) * 4, ws + WS_G + mb * 512), OW(10), OL(8)));
    O_STAGE(6, accB, 9, NOP, (OW(11), OL(9)));
    O_STAGE(7, accB, 6, NOP, (OW(12), OL(10)));
    O_STAGE(8, accA, 6, NOP, (OW(13), OL(11)));
    O_STAGE(9, accA, 6, NOP, (OW(14), OL(12)));
    O_STAGE(10, accA, 6, NOP, (OW(15), OL(13)));
    O_STAGE(11, accA, 6, NOP, OL(14));
    O_STAGE(12, accA, 4, NOP, OL(15));
    O_STAGE(13, accA, 4, NOP, NOP);
    O_STAGE(14, accA, 0, NOP, NOP);
    O_STAGE(15, accA, 0, NOP, NOP);
    asm volatile("" : "+v"(g));
    STAMP(10);
    float* xw = (float*)(lds + XCH_OFF) + w * 1024 + lane * 4;
    f32x16 acc[2];
#pragma unroll
    for (int j = 0; j < 2; ++j)
#pragma unroll
      for (int i = 0; i < 16; ++i) acc[j][i] = g * accA[j][i] + (1.f - g) * accB[j][i];
    const f32x16& as = kh ? acc[0] : acc[1];
#pragma unroll
    for (int g = 0; g < 4; ++g) *(f32x4*)(xw + g * 256) = (f32x4){as[4 * g], as[4 * g + 1], as[4 * g + 2], as[4 * g + 3]};
    asm volatile("s_waitcnt lgkmcnt(0)\n\ts_barrier" ::: "memory");
    f32x16 t = kh ? acc[1] : acc[0];
    const float* xr = (const float*)(lds + XCH_OFF) + (w ^ 4) * 1024 + lane * 4;
#pragma unroll
    for (int g = 0; g < 4; ++g) {
      f32x4 v = *(const f32x4*)(xr + g * 256);
#pragma unroll
      for (int e = 0; e < 4; ++e) t[4 * g + e] += v[e];
    }
    const int row = mb * 128 + wm * 32 + r;
    char* dst = out + (size_t)row * 2048 + (nb * 64 + kh * 32) * 2;
#pragma unroll
    for (int g = 0; g < 4; g += 2) {
      u32x2 a = {cvtpk(t[4 * g], t[4 * g + 1]), cvtpk(t[4 * g + 2], t[4 * g + 3])};
      u32x2 c = {cvtpk(t[4 * g + 4], t[4 * g + 5]), cvtpk(t[4 * g + 6], t[4 * g + 7])};
      auto x = __builtin_amdgcn_permlane32_swap(a[0], c[0], false, false);
      auto y = __builtin_amdgcn_permlane32_swap(a[1], c[1], false, false);
      *(u32x4*)(dst + g * 16 + h * 16) = (u32x4){x[0], y[0], x[1], y[1]};
    }
  }
  if (b == 0 && w == 0) {
    const unsigned long long want = ((unsigned long long)tag[1] << 32) | tag[0];
    gu64* f = (gu64*)(ws + WS_FOA) + lane;
    for (unsigned spins = 0; spins < (1u << 21); ++spins) {
      const unsigned long long a0 = __hip_atomic_load(f, __ATOMIC_RELAXED, __HIP_MEMORY_SCOPE_AGENT),
                               a1 = __hip_atomic_load(f + 64, __ATOMIC_RELAXED, __HIP_MEMORY_SCOPE_AGENT),
                               a2 = __hip_atomic_load(f + 128, __ATOMIC_RELAXED, __HIP_MEMORY_SCOPE_AGENT),
                               a3 = __hip_atomic_load(f + 192, __ATOMIC_RELAXED, __HIP_MEMORY_SCOPE_AGENT);
      if (__all(a0 == want && a1 == want && a2 == want && a3 == want)) break;
      __builtin_amdgcn_s_sleep(4);
    }
    if (lane == 0) flag_set(ws + WS_NONCE, tag);
  }
  STAMP(11);
  STAMP_OUT(0, ws); TRACE_OUT(ws);
}

extern "C" void kernel_launch(void* const* d_in, const int* in_sizes, int n_in, void* d_out, int out_size, void* d_ws,
                              size_t ws_size, hipStream_t stream) {
  const char* xa = (const char*)d_in[0];
  const char* wq = (const char*)d_in[1];
  const char* wk = (const char*)d_in[2];
  const char* wv = (const char*)d_in[3];
  const char* woa = (const char*)d_in[4];
  const char* wqkv = (const char*)d_in[6];
  const char* wob = (const char*)d_in[7];
  const char* gw = (const char*)d_in[8];
  (void)hipFuncSetAttribute((const void*)k_fused, hipFuncAttributeMaxDynamicSharedMemorySize, R_LDS);
  k_fused<<<256, 512, R_LDS, stream>>>(xa, wq, wk, wv, woa, wqkv, wob, gw, (char*)d_ws, (char*)d_out);
}
```

```cpp
#include <hip/hip_runtime.h>
#include <hip/hip_bf16.h>
#include <stdint.h>

typedef float f32x16 __attribute__((ext_vector_type(16)));
typedef float f32x4 __attribute__((ext_vector_type(4)));
typedef float f32x2 __attribute__((ext_vector_type(2)));
typedef short bf16x8 __attribute__((ext_vector_type(8)));
typedef short s16x4 __attribute__((ext_vector_type(4)));
typedef __bf16 bf16x2 __attribute__((ext_vector_type(2)));
typedef unsigned u32x4 __attribute__((ext_vector_type(4)));
typedef unsigned u32x2 __attribute__((ext_vector_type(2)));
typedef __attribute__((address_space(3))) const char* lds_cptr;
typedef __attribute__((address_space(3))) char* lds_ptr;

#define DI __device__ __forceinline__
#define SBAR() __builtin_amdgcn_sched_barrier(0)
#define PIN(x) asm volatile("" : "+v"(x))
#define MFMA32(a, b, c) __builtin_amdgcn_mfma_f32_32x32x16_bf16(a, b, c, 0, 0, 0)
#define MFMA16(a, b, c) __builtin_amdgcn_mfma_f32_16x16x32_bf16(a, b, c, 0, 0, 0)

constexpr int SEQ = 2048, DM = 1024;
constexpr float QSCALE = 0.17677669529663687f * 1.4426950408889634f;

constexpr size_t WS_KVA = 4u << 20;
constexpr size_t WS_G = WS_KVA + (256u << 10);
constexpr size_t WS_NONCE = WS_G + 8192;
constexpr size_t WS_FKV = WS_NONCE + 1024;
constexpr size_t WS_FPB = WS_FKV + 1024;
constexpr size_t WS_FOA = WS_FPB + 2048;
constexpr size_t WS_FOB = WS_FOA + 2048;
constexpr size_t WS_QB = 5u << 20;
constexpr size_t WS_KVB = 9u << 20;
constexpr size_t WS_O = 17u << 20;

#define TRACE(i) do {} while (0)
#define TRACE_DECL
#define TRACE_OUT(wsp) do {} while (0)
#define STAMP_ARG
#define STAMP_PASS
#define STAMP_DECL
#define STAMP(i) do {} while (0)
#define STAMP_OUT(k, wsp) do {} while (0)
#define PT(i) do {} while (0)
#define PTV(i) do {} while (0)
#define PT_ARG
#define PT_PASS
DI unsigned rfl(unsigned x) { return __builtin_amdgcn_readfirstlane(x); }
DI unsigned cvtpk(float lo, float hi) {
  f32x2 v = {lo, hi};
  bf16x2 b = __builtin_convertvector(v, bf16x2);
  return __builtin_bit_cast(unsigned, b);
}
DI bf16x8 pack8(const f32x16& a, int o) {
  u32x4 r = {cvtpk(a[o], a[o + 1]), cvtpk(a[o + 2], a[o + 3]), cvtpk(a[o + 4], a[o + 5]), cvtpk(a[o + 6], a[o + 7])};
  return __builtin_bit_cast(bf16x8, r);
}
DI bf16x8 pack8s(const f32x16& a, int o, float s) {
  u32x4 r = {cvtpk(a[o] * s, a[o + 1] * s), cvtpk(a[o + 2] * s, a[o + 3] * s), cvtpk(a[o + 4] * s, a[o + 5] * s),
             cvtpk(a[o + 6] * s, a[o + 7] * s)};
  return __builtin_bit_cast(bf16x8, r);
}
template <int OFF>
DI void glds16(unsigned voff, const void* sbase, unsigned lds_base) {
  asm volatile("s_mov_b32 m0, %2\n\ts_nop 0\n\tglobal_load_lds_dwordx4 %0, %1 offset:%3"
               :: "v"(voff), "s"(sbase), "s"(rfl(lds_base)), "i"(OFF) : "memory");
}
template <int OFF>
DI void glds16_sc1(unsigned voff, const void* sbase, unsigned lds_base) {
  asm volatile("s_mov_b32 m0, %2\n\ts_nop 0\n\tglobal_load_lds_dwordx4 %0, %1 offset:%3 sc1"
               :: "v"(voff), "s"(sbase), "s"(rfl(lds_base)), "i"(OFF) : "memory");
}
DI void glds16v(const char* vaddr, unsigned lds_base) {
  asm volatile("s_mov_b32 m0, %1\n\ts_nop 0\n\tglobal_load_lds_dwordx4 %0, off" :: "v"(vaddr), "s"(rfl(lds_base)) : "memory");
}
DI bf16x8 lds_rd128(lds_cptr p) { return *(const __attribute__((address_space(3))) bf16x8*)p; }
DI s16x4 lds_tr(lds_cptr p) {
  typedef short v4i16_t __attribute__((ext_vector_type(4)));
  return __builtin_bit_cast(s16x4, __builtin_amdgcn_ds_read_tr16_b64_v4i16((__attribute__((address_space(3))) v4i16_t*)p));
}
DI bf16x8 cat8(s16x4 lo, s16x4 hi) { return (bf16x8){lo[0], lo[1], lo[2], lo[3], hi[0], hi[1], hi[2], hi[3]}; }
DI float bf2f(unsigned short b) { return __uint_as_float((unsigned)b << 16); }

template <int OFF>
DI void gload16_sc1(bf16x8& d, unsigned voff, const void* sbase) {
  asm volatile("global_load_dwordx4 %0, %1, %2 offset:%3 sc1" : "=v"(d) : "v"(voff), "s"(sbase), "i"(OFF) : "memory");
}
template <int OFF>
DI void gstore16_sc1(unsigned voff, void* sbase, const bf16x8& d) {
  asm volatile("global_store_dwordx4 %0, %1, %2 offset:%3 sc1\n\ts_nop 1" :: "v"(voff), "v"(d), "s"(sbase), "i"(OFF) : "memory");
}
DI void gload4_sc1(float& d, unsigned voff, const void* sbase) {
  asm volatile("global_load_dword %0, %1, %2 sc1" : "=v"(d) : "v"(voff), "s"(sbase) : "memory");
}
DI bf16x8 gload16_now(const char* p) {
  bf16x8 d;
  asm volatile("global_load_dwordx4 %0, %1, off sc1\n\ts_waitcnt vmcnt(0)" : "=&v"(d) : "v"(p) : "memory");
  return d;
}
typedef __attribute__((address_space(1))) unsigned long long gu64;
DI void flag_set(char* p, u32x2 tag) {
  asm volatile("global_store_dwordx2 %0, %1, %2 sc1\n\ts_nop 1" :: "v"(0u), "v"(tag), "s"(p) : "memory");
}
DI void flag_peek(u32x2& d, unsigned voff, const void* sbase) {
  asm volatile("global_load_dwordx2 %0, %1, %2 sc1" : "+v"(d) : "v"(voff), "s"(sbase) : "memory");
}
DI void flag_wait(u32x2 seen, unsigned voff, const char* sbase, u32x2 tag) {
  if (__builtin_expect(__all(seen[0] == tag[0] && seen[1] == tag[1]), 1)) return;
  const unsigned long long want = ((unsigned long long)tag[1] << 32) | tag[0];
  for (unsigned spins = 0; spins < (1u << 21); ++spins) {
    if (__all(__hip_atomic_load((gu64*)(sbase + voff), __ATOMIC_RELAXED, __HIP_MEMORY_SCOPE_AGENT) == want)) return;
    __builtin_amdgcn_s_sleep(4);
  }
}

DI float dot2bf(unsigned a, unsigned b, float c) {
  return __builtin_amdgcn_fdot2_f32_bf16(__builtin_bit_cast(bf16x2, a), __builtin_bit_cast(bf16x2, b), c, false);
}
DI float wave_sum63(float z) {
#define DPP_ADD(ctrl, rmask) z += __uint_as_float(__builtin_amdgcn_update_dpp(0u, __float_as_uint(z), ctrl, rmask, 0xf, true))
  DPP_ADD(0x111, 0xf); DPP_ADD(0x112, 0xf); DPP_ADD(0x114, 0xf); DPP_ADD(0x118, 0xf);
  DPP_ADD(0x142, 0xa); DPP_ADD(0x143, 0xc);
#undef DPP_ADD
  return z;
}

DI void nonce_load(u32x2& nonce, const char* ws) {
  asm volatile("global_load_dwordx2 %0, %1, %2 sc1" : "=v"(nonce) : "v"(0u), "s"(ws + WS_NONCE) : "memory");
}
DI u32x2 nonce_tag(u32x2 nonce) {
  u32x2 tag;
  tag[0] = nonce[0] + 1u;
  tag[1] = nonce[1] + (tag[0] == 0u ? 1u : 0u);
  return tag;
}

constexpr int P_SLOT = 49152;
constexpr int P_XOFF = 0, P_WOFF = 16384;
constexpr int QX_OFF = 0;
constexpr int KVC = 147456;
struct PDma {
  const char* xsrc; const char* wp[4]; unsigned xo[2], wstep[2], lds0; int w;
  DI void init(const char* xa, const char* wq, const char* wqkv, int mb, int j, unsigned lds0_, int w_, int lane) {
    lds0 = lds0_; w = w_;
    xsrc = xa + (size_t)mb * 128 * 2048;
#pragma unroll
    for (int e = 0; e < 2; ++e) {
      const int row = 16 * w + 8 * e + (lane >> 3);
      xo[e] = row * 2048 + (((lane & 7) ^ (4 * e + (lane >> 4))) << 4);
    }
#pragma unroll
    for (int e = 0; e < 4; ++e) {
      const int krow = 8 * w + 2 * e + (lane >> 5);
      const int c = (lane & 31) ^ ((krow & 3) << 2);
      wp[e] = c < 24 ? wqkv + (size_t)krow * 6144 + j * 384 + c * 16 : wq + (size_t)krow * 2048 + j * 128 + (c - 24) * 16;
      if (e < 2) wstep[e] = c < 24 ? 64 * 6144 : 64 * 2048;
    }
  }
  DI void piece(int pc, int st, int slot) {
    const unsigned lb = lds0 + slot * P_SLOT;
    if (pc < 2) glds16<0>(xo[pc], xsrc + st * 128, lb + P_XOFF + w * 2048 + pc * 1024);
    else { glds16v(wp[pc - 2], lb + P_WOFF + w * 4096 + (pc - 2) * 1024); wp[pc - 2] += wstep[pc & 1]; }
  }
  DI void stage(int st, int slot) {
#pragma unroll
    for (int pc = 0; pc < 6; ++pc) piece(pc, st, slot);
  }
};
struct PFrag { bf16x8 x[2]; bf16x8 w[2]; };

template <bool VT, class Tail, class Peek, class Check, class Hook>
DI void proj_loop(PDma& dma, char* lds, f32x16 (&acc)[2][2], Tail tail, Peek peek, Check check, Hook hook PT_ARG) {
  const int tid = threadIdx.x, lane = tid & 63;
  const int w = dma.w;
  const int wm = w & 1, wn = w >> 1;
  const int r = lane & 31, h = lane >> 5;
  const int t3 = (r >> 1) & 7;
  const int xa0 = P_XOFF + (wm * 64 + r) * 128 + ((h ^ t3) << 4);
  const int g16 = lane >> 4, q4 = (lane & 15) >> 2, p4 = lane & 3;
  const int wc = wn * 8 + 2 * (g16 & 1) + (p4 >> 1);
  const int wa0 = P_WOFF + (8 * h + q4) * 512 + ((wc ^ (q4 << 2)) << 4) + 8 * (p4 & 1);
  auto rdx = [&](PFrag& f, int i, int slot, int s) { f.x[i] = lds_rd128((lds_cptr)lds + slot * P_SLOT + (xa0 ^ (s << 5)) + i * 4096); };
  auto rdw = [&](PFrag& f, int j, int slot, int s) {
    lds_cptr p = (lds_cptr)lds + slot * P_SLOT + (wa0 ^ (j << 6)) + s * 8192;
    f.w[j] = cat8(lds_tr(p), lds_tr(p + 2048));
  };
  auto mm = [&](const PFrag& f, int i, int j) {
    if constexpr (VT) acc[i][j] = MFMA32(f.x[i], f.w[j], acc[i][j]);
    else acc[i][j] = MFMA32(f.w[j], f.x[i], acc[i][j]);
  };
  auto win = [&](int j, int pc, int slot) {
    if (j == -1) dma.piece(pc, 2, 2);
    else if (j <= 12) dma.piece(pc, j + 3, slot);
    else if (j <= 14 && pc < 4) tail((j - 13) * 4 + pc);
  };
  auto step = [&](const PFrag& c, PFrag& n, int rslot, int rs, bool rd, int pa, int wj, int dslot) {
    mm(c, 0, 0); SBAR();
    if (rd) { rdw(n, 0, rslot, rs); rdx(n, 0, rslot, rs); }
    SBAR();
    mm(c, 0, 1); SBAR();
    if (rd) { rdw(n, 1, rslot, rs); rdx(n, 1, rslot, rs); }
    SBAR();
    mm(c, 1, 0); SBAR();
    if (pa >= 0) win(wj, pa, dslot);
    SBAR();
    mm(c, 1, 1); SBAR();
    if (pa >= 0) win(wj, pa + 1, dslot);
    SBAR();
  };
  PFrag f0, f1;
  rdw(f0, 0, 0, 0); rdx(f0, 0, 0, 0); rdw(f0, 1, 0, 0); rdx(f0, 1, 0, 0);
  auto iter = [&](int it, int slot, int pslot, int nslot, bool hk) {
    step(f0, f1, slot, 1, true, 2, it - 1, pslot);
    step(f1, f0, slot, 2, true, 4, it - 1, pslot);
    step(f0, f1, slot, 3, true, -1, 0, 0);
    PTV(16 + 2 * it);
    asm volatile("s_waitcnt vmcnt(6) lgkmcnt(0)\n\ts_barrier" ::: "memory");
    PTV(17 + 2 * it);
    if (hk) hook(it);
    SBAR();
    step(f1, f0, nslot, 0, true, 0, it, slot);
  };
  iter(0, 0, 0, 1, true); iter(1, 1, 0, 2, true); iter(2, 2, 1, 0, true); iter(3, 0, 2, 1, true);
  iter(4, 1, 0, 2, true); iter(5, 2, 1, 0, true); iter(6, 0, 2, 1, true);
  int slot = 1, pslot = 0;
  for (int it = 7; it < 13; ++it) {
    const int nslot = slot == 2 ? 0 : slot + 1;
    iter(it, slot, pslot, nslot, false);
    pslot = slot;
    slot = nslot;
  }
  peek();
  step(f0, f1, 1, 1, true, 2, 12, 0);
  step(f1, f0, 1, 2, true, 4, 12, 0);
  step(f0, f1, 1, 3, true, -1, 0, 0);
  check();
  SBAR();
  step(f1, f0, 2, 0, true, 0, 13, 0);
  step(f0, f1, 2, 1, true, 2, 13, 0);
  step(f1, f0, 2, 2, true, -1, 0, 0);
  step(f0, f1, 2, 3, true, -1, 0, 0);
  asm volatile("s_waitcnt vmcnt(4) lgkmcnt(0)\n\ts_barrier" ::: "memory");
  SBAR();
  step(f1, f0, 0, 0, true, 0, 14, 0);
  step(f0, f1, 0, 1, true, 2, 14, 0);
  step(f1, f0, 0, 2, true, -1, 0, 0);
  step(f0, f1, 0, 3, true, -1, 0, 0);
  asm volatile("s_waitcnt lgkmcnt(0)\n\ts_barrier" ::: "memory");
  SBAR();
  step(f1, f0, 0, 0, false, -1, 0, 0);
}

DI void pre_kv(const char* __restrict__ xg, const char* __restrict__ wsel, char* __restrict__ ws, int rt, int t, char* lds, PDma& dma,
               u32x2& tag, f32x16& acc, lds_cptr& xlast, lds_cptr& wlast PT_ARG) {
  const int tid = threadIdx.x, lane = tid & 63;
  const int w = dma.w;
  const int r = lane & 31, h = lane >> 5;
  const unsigned lds0 = dma.lds0;
  u32x2 nonce;
  nonce_load(nonce, ws);
  const int xrow = 4 * w + (lane >> 4);
  const unsigned xo = xrow * 2048 + (((lane & 15) ^ (xrow & 15)) << 4), wo = w * 1024 + lane * 16;
  const char* xs = xg + (size_t)rt * 32 * 2048;
#define KV_OFF(u) ((u) == 0 || (u) == 7 ? KVC : P_SLOT + ((u) - 1) * 16384)
  auto sub = [&](int u) {
    const unsigned lb = lds0 + KV_OFF(u) + w * 1024;
    glds16<0>(xo, xs + u * 256, lb);
    glds16<0>(wo, wsel + u * 8192, lb + 8192);
  };
  sub(0); sub(1); sub(2); sub(3); sub(4); sub(5); sub(6);
  PT(0);
  const int g16 = lane >> 4, q4 = (lane & 15) >> 2, p4 = lane & 3;
  lds_cptr xa = (lds_cptr)lds + P_SLOT + r * 256 + (((2 * w + h) ^ (r & 15)) << 4);
  lds_cptr wa = (lds_cptr)lds + P_SLOT + 8192 + (16 * w + 8 * h + q4) * 64 + 32 * (g16 & 1) + 8 * p4;
  lds_cptr xc = xa + (KVC - P_SLOT), wc = wa + (KVC - P_SLOT);
  PIN(xa); PIN(wa); PIN(xc); PIN(wc);
  acc = (f32x16){};
  bf16x8 xf[4], wf[4];
#define KV_RD(i, u) do { \
    lds_cptr px_ = (u) == 0 ? xc : xa + ((u) - 1) * 16384; \
    lds_cptr pw_ = (u) == 0 ? wc : wa + ((u) - 1) * 16384; \
    xf[i] = lds_rd128(px_); wf[i] = cat8(lds_tr(pw_), lds_tr(pw_ + 256)); \
  } while (0)
#define KV_MM(i) do { if (t) acc = MFMA32(xf[i], wf[i], acc); else acc = MFMA32(wf[i], xf[i], acc); } while (0)
  asm volatile("s_waitcnt vmcnt(6)\n\ts_barrier" : "+v"(nonce)::"memory");
  PT(1);
  KV_RD(0, 0); KV_RD(1, 1); KV_RD(2, 2); KV_RD(3, 3);
  SBAR();
  dma.stage(0, 0);
  SBAR();
  KV_MM(0); KV_MM(1); KV_MM(2); KV_MM(3);
  asm volatile("s_waitcnt vmcnt(6) lgkmcnt(0)\n\ts_barrier" ::: "memory");
  PT(2);
  KV_RD(0, 4); KV_RD(1, 5); KV_RD(2, 6);
  SBAR();
  sub(7); dma.stage(1, 1);
  SBAR();
  KV_MM(0); KV_MM(1); KV_MM(2);
  asm volatile("s_waitcnt vmcnt(8) lgkmcnt(0)\n\ts_barrier" ::: "memory");
  PT(3);
  dma.piece(0, 2, 2); dma.piece(1, 2, 2);
#undef KV_RD
#undef KV_MM
#undef KV_OFF
  tag = nonce_tag(nonce);
  xlast = xc; wlast = wc;
  PT(9);
}

DI void pre_gate(const char* __restrict__ xg, const char* __restrict__ gw, char* __restrict__ ws, int row0, PDma& dma, u32x2& tag PT_ARG) {
  const int tid = threadIdx.x, lane = tid & 63;
  const int w = dma.w;
  u32x4 gx[2][2], gv[2];
  u32x2 nonce;
  const char* px = xg + (size_t)(row0 + 2 * w) * 2048;
  asm volatile("global_load_dwordx2 %6, %10, %11 sc1\n\t"
               "global_load_dwordx4 %0, %7, %8\n\tglobal_load_dwordx4 %1, %7, %8 offset:16\n\t"
               "global_load_dwordx4 %2, %7, %8 offset:2048\n\tglobal_load_dwordx4 %3, %7, %8 offset:2064\n\t"
               "global_load_dwordx4 %4, %7, %9\n\tglobal_load_dwordx4 %5, %7, %9 offset:16"
               : "=&v"(gx[0][0]), "=&v"(gx[0][1]), "=&v"(gx[1][0]), "=&v"(gx[1][1]), "=&v"(gv[0]), "=&v"(gv[1]), "=&v"(nonce)
               : "v"(lane * 32), "s"(px), "s"(gw), "v"(0u), "s"(ws + WS_NONCE) : "memory");
  dma.stage(0, 0);
  PT(0);
  asm volatile("s_barrier" ::: "memory");
  PT(1);
  dma.stage(1, 1); dma.piece(0, 2, 2); dma.piece(1, 2, 2);
  PT(2);
  asm volatile("s_waitcnt vmcnt(8)\n\ts_barrier"
               : "+v"(gx[0][0]), "+v"(gx[0][1]), "+v"(gx[1][0]), "+v"(gx[1][1]), "+v"(gv[0]), "+v"(gv[1]), "+v"(nonce)::"memory");
  PT(3);
  tag = nonce_tag(nonce);
#pragma unroll
  for (int i = 0; i < 2; ++i) {
    float z = 0.f, z2 = 0.f;
#pragma unroll
    for (int e = 0; e < 4; ++e) { z = dot2bf(gx[i][0][e], gv[0][e], z); z2 = dot2bf(gx[i][1][e], gv[1][e], z2); }
    z += z2;
    z = wave_sum63(z);
    const float g = 1.f / (1.f + __expf(-z));
    if (lane == 63) asm volatile("global_store_dword %0, %1, %2 sc1\n\ts_nop 0" :: "v"(0u), "v"(g), "s"(ws + WS_G + (row0 + 2 * w + i) * 4) : "memory");
  }
}

constexpr int SL = 32768;
constexpr int R_LDS = 5 * SL;
constexpr int XCH_OFF = 0;
constexpr int WO_OFF = 2 * SL;

DI int crow(int r, int h) { return (r & 3) + 8 * (r >> 2) + 4 * h; }
DI float swap_half(float x) {
  auto rr = __builtin_amdgcn_permlane32_swap(__float_as_uint(x), __float_as_uint(x), false, false);
  return __uint_as_float((threadIdx.x & 32) ? rr[0] : rr[1]);
}
DI void exp16(f32x16& s) {
#pragma unroll
  for (int i = 0; i < 16; ++i) s[i] = __builtin_amdgcn_exp2f(s[i]);
}
#define AE(S, i) S[i] = __builtin_amdgcn_exp2f(S[i])
#define AC(P, S, i) P[(i) & 3] = cvtpk(S[2 * (i)], S[2 * (i) + 1])
#define ABC(x) __builtin_bit_cast(bf16x8, x)
#define APHASE(QK, PV, SX, PXa, PXb, SY, CINY, PYa, PYb, QY0, QY1, OY, LY, KF0, KF1, VF0, VF1, R1, R2) do { \
  if (QK) { SY = MFMA32(KF0, QY0, CINY); PIN(SY); } SBAR(); \
  AE(SX, 0); AE(SX, 1); AE(SX, 2); AE(SX, 3); PIN(SX); SBAR(); \
  if (QK) { SY = MFMA32(KF1, QY1, SY); PIN(SY); } SBAR(); \
  R1; \
  AC(PXa, SX, 0); AC(PXa, SX, 1); AE(SX, 4); AE(SX, 5); AE(SX, 6); PIN(SX); PIN(PXa); SBAR(); \
  if (PV) { OY = MFMA32(VF0, ABC(PYa), OY); PIN(OY); } SBAR(); \
  AE(SX, 7); AC(PXa, SX, 2); AE(SX, 8); AE(SX, 9); AC(PXa, SX, 3); PIN(SX); PIN(PXa); SBAR(); \
  if (PV) { LY = MFMA16(ones, ABC(PYa), LY); PIN(LY); } SBAR(); \
  R2; \
  AE(SX, 10); AE(SX, 11); PIN(SX); SBAR(); \
  if (PV) { OY = MFMA32(VF1, ABC(PYb), OY); PIN(OY); } SBAR(); \
  AC(PXb, SX, 4); AC(PXb, SX, 5); AE(SX, 12); AE(SX, 13); AE(SX, 14); PIN(SX); PIN(PXb); SBAR(); \
  if (PV) { LY = MFMA16(ones, ABC(PYb), LY); PIN(LY); } SBAR(); \
  AE(SX, 15); AC(PXb, SX, 6); AC(PXb, SX, 7); PIN(PXb); SBAR(); \
} while (0)

template <bool MASKED>
DI void attn_exact(const char* __restrict__ kv, int t0, int t1, int qt, const bf16x8& q0, const bf16x8& q1, const bf16x8& ones,
                   f32x16& o, float& l) {
  const int lane = threadIdx.x & 63, r = lane & 31, h = lane >> 5;
  float m = -3.0e38f;
  for (int t = t0; t < t1; ++t) {
    const char* p = kv + (size_t)t * 4096 + lane * 16;
    bf16x8 k0 = gload16_now(p), k1 = gload16_now(p + 1024);
    f32x16 s = {};
    s = MFMA32(k0, q0, s); s = MFMA32(k1, q1, s);
#pragma unroll
    for (int i = 0; i < 16; ++i) {
      bool ok = true;
      if (MASKED) { const int c = crow(i, h); ok = (t == qt) ? (c <= r) : ((t == qt - 8) ? (c > r) : true); }
      m = fmaxf(m, ok ? s[i] : -3.0e38f);
    }
  }
  m = fmaxf(m, swap_half(m));
  o = (f32x16){};
  f32x4 la = {};
  for (int t = t0; t < t1; ++t) {
    const char* p = kv + (size_t)t * 4096 + lane * 16;
    bf16x8 k0 = gload16_now(p), k1 = gload16_now(p + 1024);
    bf16x8 v0 = gload16_now(p + 2048), v1 = gload16_now(p + 3072);
    f32x16 s = {};
    s = MFMA32(k0, q0, s); s = MFMA32(k1, q1, s);
#pragma unroll
    for (int i = 0; i < 16; ++i) {
      bool ok = true;
      if (MASKED) { const int c = crow(i, h); ok = (t == qt) ? (c <= r) : ((t == qt - 8) ? (c > r) : true); }
      s[i] = ok ? __builtin_amdgcn_exp2f(s[i] - m) : 0.f;
    }
    bf16x8 pa = pack8(s, 0), pb = pack8(s, 8);
    o = MFMA32(v0, pa, o); o = MFMA32(v1, pb, o);
    la = MFMA16(ones, pa, la); la = MFMA16(ones, pb, la);
  }
  l = la[0];
}
DI bool ref_unsafe(const f32x16& o, float l) {
  float z = 0.f;
#pragma unroll
  for (int i = 0; i < 16; ++i) z = __builtin_fmaf(o[i], 0.f, z);
  const bool ok = (l >= 5.421010862e-20f) && (l <= 1.8446744e19f) && (z == 0.f);
  return __any(!ok);
}

__global__ __launch_bounds__(512, 2) void k_fused(const char* __restrict__ xa, const char* __restrict__ wq,
                                                  const char* __restrict__ wk, const char* __restrict__ wv,
                                                  const char* __restrict__ woa, const char* __restrict__ wqkv,
                                                  const char* __restrict__ wob, const char* __restrict__ gw,
                                                  char* __restrict__ ws, char* __restrict__ out) {
  extern __shared__ __attribute__((aligned(16))) char lds[];
  STAMP_DECL STAMP(0); TRACE_DECL
  const int b = blockIdx.x;
  const int xcd = b & 7, idx = b >> 3;
  const int tid = threadIdx.x, lane = tid & 63;
  const int w = rfl(tid >> 6);
  const int r = lane & 31, h = lane >> 5;
  const unsigned lds0 = (unsigned)(uintptr_t)lds;
  const unsigned lo16 = lane * 16;
  const int pmb = (xcd & 1) * 8 + (idx & 7), pj = (xcd >> 1) * 4 + ((idx >> 3) & 1) * 2 + (idx >> 4);
  const int pr = w & 3, half = w >> 2;
  const char* kva = ws + WS_KVA;
  auto pieceA = [&](int k, int sl, int i) {
    const char* src = kva + (size_t)(half * 32 + 4 * k + pr) * 4096;
    const unsigned dst = lds0 + sl * SL + w * 4096;
    if (i == 0) glds16_sc1<0>(lo16, src, dst);
    else if (i == 1) glds16_sc1<1024>(lo16, src, dst);
    else if (i == 2) glds16_sc1<2048>(lo16, src, dst);
    else glds16_sc1<3072>(lo16, src, dst);
  };
  PDma dma;
  dma.init(xa, wq, wqkv, pmb, pj, lds0, w, lane);
  u32x2 tag;
  const bool evenj = (pj & 1) == 0;
  const int kvrt = pmb * 4 + (pj >> 2), kvt = (pj >> 1) & 1;
  PT(11);
  f32x16 kvacc = {};
  lds_cptr kvxl = (lds_cptr)lds, kvwl = (lds_cptr)lds;
  if (evenj) pre_kv(xa, kvt ? wv : wk, ws, kvrt, kvt, lds, dma, tag, kvacc, kvxl, kvwl PT_PASS);
  else pre_gate(xa, gw, ws, pmb * 128 + (pj >> 1) * 16, dma, tag PT_PASS);
  PT(12);
  STAMP(1);
  const int wn = w >> 1, pwm = w & 1;
  const int ct = 6 * pj + 2 * wn;
  const bool hasB = wn < 3;
  bf16x8 held[8];
  {
    f32x16 acc[2][2] = {};
    u32x2 kv0 = {~tag[0], 0u}, kv1 = kv0;
    auto tail = [&](int n) { pieceA(n >> 2, 2 + (n >> 2), n & 3); };
    auto peek = [&]() { flag_peek(kv0, lane * 8, ws + WS_FKV); flag_peek(kv1, lane * 8, ws + WS_FKV + 512); };
    auto check = [&]() {
      asm volatile("s_waitcnt vmcnt(4) lgkmcnt(0)" : "+v"(kv0), "+v"(kv1)::"memory");
      if (w == 0) { flag_wait(kv0, lane * 8, ws + WS_FKV, tag); flag_wait(kv1, lane * 8, ws + WS_FKV + 512, tag); }
      asm volatile("s_barrier" ::: "memory");
    };
    float* red = (float*)(lds + KVC) + lane * 4;
    auto put = [&](int s4) {
#pragma unroll
      for (int g = 0; g < 4; ++g) *(f32x4*)(red + (s4 * 4 + g) * 256) = (f32x4){kvacc[4 * g], kvacc[4 * g + 1], kvacc[4 * g + 2], kvacc[4 * g + 3]};
    };
    auto get = [&](int s4) {
#pragma unroll
      for (int g = 0; g < 4; ++g) {
        const f32x4 a = *(const f32x4*)(red + (s4 * 4 + g) * 256);
#pragma unroll
        for (int e = 0; e < 4; ++e) kvacc[4 * g + e] += a[e];
      }
    };
    auto hook = [&](int it) {
      if (evenj) {
        if (it == 0) {
          const bf16x8 xf = lds_rd128(kvxl), wf = cat8(lds_tr(kvwl), lds_tr(kvwl + 256));
          if (kvt) kvacc = MFMA32(xf, wf, kvacc); else kvacc = MFMA32(wf, xf, kvacc);
        }
        else if (it == 1) { if (w >= 4) put(w - 4); }
        else if (it == 2) { if (w < 4) get(w); }
        else if (it == 3) { if (w >= 1 && w < 4) put(w); }
        else if (it == 4) {
          if (w == 0) {
            get(1); get(2); get(3);
            char* dst = ws + WS_KVA + (size_t)kvrt * 4096 + kvt * 2048;
            gstore16_sc1<0>(lo16, dst, pack8(kvacc, 0));
            gstore16_sc1<1024>(lo16, dst, pack8(kvacc, 8));
          }
        } else if (it == 6) { if (tid == 0) flag_set(ws + WS_FKV + (kvrt * 2 + kvt) * 8, tag); }
      }
    };
    if (hasB && ct >= 64) proj_loop<true>(dma, lds, acc, tail, peek, check, hook PT_PASS);
    else proj_loop<false>(dma, lds, acc, tail, peek, check, hook PT_PASS);
    STAMP(2);
    if (!hasB || ct < 32) {
#pragma unroll
      for (int i = 0; i < 2; ++i)
#pragma unroll
        for (int j = 0; j < 2; ++j)
#pragma unroll
          for (int e = 0; e < 16; ++e) acc[i][j][e] *= QSCALE;
    }
#pragma unroll
    for (int i = 0; i < 2; ++i)
#pragma unroll
      for (int j = 0; j < 2; ++j) {
        held[(i * 2 + j) * 2] = pack8(acc[i][j], 0);
        held[(i * 2 + j) * 2 + 1] = pack8(acc[i][j], 8);
      }
  }
  bf16x8 qb[2], q[2][2];
  typedef __attribute__((address_space(3))) bf16x8* lds_v8;
  const unsigned hstep = ct < 32 ? 2048 : 4096;
  char* hbase = ws + (ct < 32 ? WS_QB : WS_KVB + (ct >= 64 ? 2048 : 0)) + (size_t)((ct & 31) * 64 + pmb * 4 + pwm * 2) * hstep;
  auto storeH = [&](int n) {
    if (hasB) {
      char* dst = hbase + (size_t)((n >> 2) + ((n >> 1) & 1) * 64) * hstep;
      if (n & 1) gstore16_sc1<1024>(lo16, dst, held[n]);
      else gstore16_sc1<0>(lo16, dst, held[n]);
    }
  };
  if (!hasB) {
#pragma unroll
    for (int n = 0; n < 8; ++n)
      *(lds_v8)((lds_ptr)lds + QX_OFF + (((pwm * 2 + (n >> 2)) * 2 + ((n >> 1) & 1)) * 2 + (n & 1)) * 1024 + lo16) = held[n];
  }
  asm volatile("s_waitcnt vmcnt(4) lgkmcnt(0)\n\ts_barrier" ::: "memory");
#pragma unroll
  for (int u = 0; u < 2; ++u)
#pragma unroll
    for (int s2 = 0; s2 < 2; ++s2) q[u][s2] = lds_rd128((lds_cptr)lds + QX_OFF + ((pr * 2 + u) * 2 + s2) * 1024 + lo16);
  STAMP(3);

  const int qt = pmb * 4 + pr, hA = 2 * pj;
  const int hB = (b & 7) + 8 * (b >> 6), rb = (b >> 3) & 7;
  const int qtB = rb * 8 + w;
  const int mb = (xcd & 3) * 4 + (idx & 3), nb = (xcd >> 2) * 8 + (idx >> 2);
  const char* kvb = ws + WS_KVB + (size_t)hB * 64 * 4096;
  bf16x8 ones;
  {
    const short v = ((lane & 3) == 0 && (((lane >> 4) & 1) == (((lane & 15) >> 2) & 1))) ? (short)0x3f80 : (short)0;
    ones = (bf16x8){v, v, v, v, v, v, v, v};
  }
  auto pieceB = [&](int n) {
    int gt = rb * 8 - 8 + 2 * w + (n >> 2);
    gt = gt < 0 ? 0 : gt;
    const char* src = kvb + (size_t)gt * 4096;
    const unsigned dst = lds0 + (2 * w + (n >> 2)) * 4096;
    if ((n & 3) == 0) glds16_sc1<0>(lo16, src, dst);
    else if ((n & 3) == 1) glds16_sc1<1024>(lo16, src, dst);
    else if ((n & 3) == 2) glds16_sc1<2048>(lo16, src, dst);
    else glds16_sc1<3072>(lo16, src, dst);
  };
  const char* pqb = ws + WS_QB + (size_t)(hB * 64 + qtB) * 2048;
  unsigned wo[2];
#pragma unroll
  for (int e = 0; e < 2; ++e) {
    const int row = 16 * w + 8 * e + (lane >> 3);
    const int c = (lane & 7) ^ (((row >> 1) & 1) << 2);
    wo[e] = row * 2048 + c * 16;
  }
  auto pieceW = [&](int st, int e) {
    const char* src = (st < 8 ? wob : woa) + (size_t)(st & 7) * 128 * 2048 + nb * 128;
    glds16<0>(wo[e], src, lds0 + WO_OFF + (st % 6) * 16384 + w * 2048 + e * 1024);
  };
#define NOP (void)0
  u32x2 seen;

  const f32x16 zero = {};
  f32x16 o0 = {}, o1 = {}, s0, s1;
  f32x4 l0 = {}, l1 = {};
  u32x4 p0a, p0b, p1a, p1b;
  bf16x8 ka0, ka1, kb0, kb1, va0, va1, vb0, vb1;
  lds_cptr kvbase = (lds_cptr)lds + half * 16384 + lane * 16;
#define APAIR(pp, SL0, SL1, FIRST, LAST, PA, PB, MID, D0, D1, D2, D3) do { \
    lds_cptr cur = kvbase + (SL0) * SL + ((pp) & 1) * 8192; PIN(cur);     \
    lds_cptr nxt = (SL0) == (SL1) && !((pp) & 1) ? cur + 8192 : kvbase + (SL1) * SL; \
    __builtin_amdgcn_s_setprio(PA); \
    if (FIRST) { ka0 = lds_rd128(cur); ka1 = lds_rd128(cur + 1024); s0 = MFMA32(ka0, q[0][0], zero); s0 = MFMA32(ka1, q[0][1], s0); } \
    SBAR(); \
    APHASE(1, !(FIRST), s0, p0a, p0b, s1, zero, p1a, p1b, q[1][0], q[1][1], o1, l1, ka0, ka1, vb0, vb1, \
           (kb0 = lds_rd128(cur + 4096), kb1 = lds_rd128(cur + 5120)), (va0 = lds_rd128(cur + 2048), va1 = lds_rd128(cur + 3072))); \
    D0; SBAR(); \
    APHASE(1, 1, s1, p1a, p1b, s0, zero, p0a, p0b, q[0][0], q[0][1], o0, l0, kb0, kb1, va0, va1, (void)0, (void)0); \
    D1; SBAR(); \
    ATRACE(2 * (pp)); \
    MID; \
    ATRACE(2 * (pp) + 1); \
    __builtin_amdgcn_s_setprio(PB); SBAR(); \
    APHASE(1, 1, s0, p0a, p0b, s1, zero, p1a, p1b, q[1][0], q[1][1], o1, l1, kb0, kb1, va0, va1, \
           if (!(LAST)) (ka0 = lds_rd128(nxt), ka1 = lds_rd128(nxt + 1024)), (vb0 = lds_rd128(cur + 4096 + 2048), vb1 = lds_rd128(cur + 4096 + 3072))); \
    D2; SBAR(); \
    APHASE(!(LAST), 1, s1, p1a, p1b, s0, zero, p0a, p0b, q[0][0], q[0][1], o0, l0, ka0, ka1, vb0, vb1, (void)0, (void)0); \
    D3; SBAR(); \
    if (LAST) { \
      o1 = MFMA32(vb0, ABC(p1a), o1); o1 = MFMA32(vb1, ABC(p1b), o1); \
      l1 = MFMA16(ones, ABC(p1a), l1); l1 = MFMA16(ones, ABC(p1b), l1); \
    } \
  } while (0)
#define ATRACE(i) do {} while (0)
#define DA(k, sl, i) pieceA(k, sl, i)
#define DA2(k, sl, i) (pieceA(k, sl, i), pieceA(k, sl, (i) + 1))
#define DB2(n) (pieceB(n), pieceB((n) + 1))
#define DW(st, e) pieceW(st, e)
#define BAR_V(n) asm volatile("s_waitcnt vmcnt(" #n ")\n\ts_barrier" ::: "memory")
  STAMP(4); STAMP(5);
  const int fbm = 2 * rb - 2 + (lane & 3);
  const unsigned fbo = ((fbm < 0 ? 2 * rb : fbm) * 16 + ((lane >> 2) % 3 * 32 + hB) / 6) * 8;
  seen = (u32x2){~tag[0], 0u};
#define DH4(n) (storeH(n), storeH((n) + 1), storeH((n) + 2), storeH((n) + 3))
  APAIR(0, 2, 2, 1, 0, 2, 1, NOP, DA2(2, 4, 0), DA2(2, 4, 2), DH4(0), DH4(4));
  APAIR(1, 2, 3, 0, 0, 0, 3, { if (hasB) BAR_V(12); else BAR_V(4); }, NOP, NOP, NOP, NOP);
  APAIR(2, 3, 3, 0, 0, 2, 1, NOP, NOP, NOP, NOP, NOP);
  APAIR(3, 3, 4, 0, 0, 0, 3, { BAR_V(0); if (tid == 0) flag_set(ws + WS_FPB + (pmb * 16 + pj) * 8, tag);   },
        NOP, NOP, DA2(3, 2, 0), DA2(3, 2, 2));
  APAIR(4, 4, 4, 0, 0, 2, 1, NOP, NOP, NOP, NOP, NOP);
  flag_peek(seen, fbo, ws + WS_FPB);
  APAIR(5, 4, 2, 0, 0, 0, 3, { asm volatile("s_waitcnt vmcnt(0)" : "+v"(seen)::"memory"); if (w == 0) flag_wait(seen, fbo, ws + WS_FPB, tag); asm volatile("s_barrier" ::: "memory"); },
        NOP, NOP, DB2(0), DB2(2));
  APAIR(6, 2, 2, 0, 0, 2, 1, NOP, DB2(4), DB2(6), (gload16_sc1<0>(qb[0], lo16, pqb), gload16_sc1<1024>(qb[1], lo16, pqb)), DA2(4, 3, 0));
  APAIR(7, 2, 2, 0, 1, 1, 0, NOP, DA2(4, 3, 2), NOP, NOP, NOP);
  asm volatile("s_waitcnt vmcnt(0) lgkmcnt(0)\n\ts_barrier" : "+v"(qb[0]), "+v"(qb[1])::"memory");
  STAMP(6);
  {
    f32x16 o = {};
    f32x4 la = {};
    lds_cptr bb = (lds_cptr)lds + w * 4096 + lane * 16;
    f32x16 mD;
#pragma unroll
    for (int i = 0; i < 16; ++i) mD[i] = crow(i, h) <= r ? 0.f : -1.0e30f;
    if (rb != 0) {
      f32x16 sa, sb;
#pragma unroll
      for (int i = 0; i < 16; ++i) sa[i] = -1.0e30f - mD[i];
      u32x4 pa0 = {}, pa1 = {}, pb0 = {}, pb1 = {};
      bf16x8 k0, k1, k2, k3, v0, v1, v2, v3;
      k0 = lds_rd128(bb); k1 = lds_rd128(bb + 1024);
      k2 = lds_rd128(bb + 4096); k3 = lds_rd128(bb + 4096 + 1024);
      sa = MFMA32(k0, qb[0], sa); sa = MFMA32(k1, qb[1], sa);
      SBAR();
#define BRDK(KA, KB, j) (KA = lds_rd128(bb + (j) * 4096), KB = lds_rd128(bb + (j) * 4096 + 1024))
#define BRDV(VA, VB, j) (VA = lds_rd128(bb + (j) * 4096 + 2048), VB = lds_rd128(bb + (j) * 4096 + 3072))
#define B_DMA(n) do { pieceA(5, 4, n); SBAR(); } while (0)
      __builtin_amdgcn_s_setprio(3);
      APHASE(1, 0, sa, pa0, pa1, sb, zero, pb0, pb1, qb[0], qb[1], o, la, k2, k3, v2, v3, BRDK(k0, k1, 2), BRDV(v0, v1, 0));
      B_DMA(0); B_DMA(1);
      APHASE(1, 1, sb, pb0, pb1, sa, zero, pa0, pa1, qb[0], qb[1], o, la, k0, k1, v0, v1, BRDK(k2, k3, 3), BRDV(v2, v3, 1));
      B_DMA(2); B_DMA(3);
      __builtin_amdgcn_s_setprio(2);
      APHASE(1, 1, sa, pa0, pa1, sb, zero, pb0, pb1, qb[0], qb[1], o, la, k2, k3, v2, v3, BRDK(k0, k1, 4), BRDV(v0, v1, 2));
      APHASE(1, 1, sb, pb0, pb1, sa, zero, pa0, pa1, qb[0], qb[1], o, la, k0, k1, v0, v1, BRDK(k2, k3, 5), BRDV(v2, v3, 3));
      __builtin_amdgcn_s_setprio(1);
      APHASE(1, 1, sa, pa0, pa1, sb, zero, pb0, pb1, qb[0], qb[1], o, la, k2, k3, v2, v3, BRDK(k0, k1, 6), BRDV(v0, v1, 4));
      APHASE(1, 1, sb, pb0, pb1, sa, zero, pa0, pa1, qb[0], qb[1], o, la, k0, k1, v0, v1, BRDK(k2, k3, 7), BRDV(v2, v3, 5));
      __builtin_amdgcn_s_setprio(0);
      APHASE(1, 1, sa, pa0, pa1, sb, zero, pb0, pb1, qb[0], qb[1], o, la, k2, k3, v2, v3, BRDK(k0, k1, 8), BRDV(v0, v1, 6));
      APHASE(1, 1, sb, pb0, pb1, sa, mD, pa0, pa1, qb[0], qb[1], o, la, k0, k1, v0, v1, (void)0, BRDV(v2, v3, 7));
      APHASE(0, 1, sa, pa0, pa1, sb, zero, pb0, pb1, qb[0], qb[1], o, la, k2, k3, v2, v3, (void)0, BRDV(v0, v1, 8));
      o = MFMA32(v0, ABC(pa0), o); o = MFMA32(v1, ABC(pa1), o);
      la = MFMA16(ones, ABC(pa0), la); la = MFMA16(ones, ABC(pa1), la);
    } else {
#pragma unroll
      for (int n = 0; n < 4; ++n) B_DMA(n);
      auto tile = [&](int j, const f32x16& cin) {
        lds_cptr p = bb + j * 4096;
        bf16x8 k0 = lds_rd128(p), k1 = lds_rd128(p + 1024), v0 = lds_rd128(p + 2048), v1 = lds_rd128(p + 3072);
        f32x16 s = MFMA32(k0, qb[0], cin);
        s = MFMA32(k1, qb[1], s);
        exp16(s);
        bf16x8 pa = pack8(s, 0), pb = pack8(s, 8);
        o = MFMA32(v0, pa, o); o = MFMA32(v1, pb, o);
        la = MFMA16(ones, pa, la); la = MFMA16(ones, pb, la);
      };
      for (int j = 8 - w; j < 8; ++j) tile(j, zero);
      tile(8, mD);
    }
    float l = la[0];
    if (__builtin_expect(ref_unsafe(o, l), 0)) attn_exact<true>(kvb, qtB >= 8 ? qtB - 8 : 0, qtB + 1, qtB, qb[0], qb[1], ones, o, l);
    const float sc = __builtin_amdgcn_rcpf(l);
    char* dst = ws + WS_O + (size_t)(qtB * 64 + 32 + hB) * 2048;
    gstore16_sc1<0>(lo16, dst, pack8s(o, 0, sc));
    gstore16_sc1<1024>(lo16, dst, pack8s(o, 8, sc));
  }
  asm volatile("s_waitcnt vmcnt(2) lgkmcnt(0)\n\ts_barrier" ::: "memory");
  STAMP(7);
  const int wm = w & 3, kh = w >> 2;
  const char* obase = ws + WS_O + (size_t)(mb * 4 + wm) * 64 * 2048 + kh * 4096;
  bf16x8 of[5][4];
  auto loadO1 = [&](int st, int i, bf16x8& f) {
    const char* src = obase + (size_t)(st < 8 ? 32 + 4 * st : 4 * (st - 8)) * 2048;
    if (i == 0) gload16_sc1<0>(f, lo16, src);
    else if (i == 1) gload16_sc1<1024>(f, lo16, src);
    else if (i == 2) gload16_sc1<2048>(f, lo16, src);
    else gload16_sc1<3072>(f, lo16, src);
  };
#define LO(st, i) loadO1(st, i, of[st][i])
  auto loadO = [&](int st, bf16x8 (&f)[4]) {
    const char* src = obase + (size_t)(st < 8 ? 32 + 4 * st : 4 * (st - 8)) * 2048;
    gload16_sc1<0>(f[0], lo16, src); gload16_sc1<1024>(f[1], lo16, src);
    gload16_sc1<2048>(f[2], lo16, src); gload16_sc1<3072>(f[3], lo16, src);
  };
  const unsigned fob = ((mb >> 1) * 32 + (lane & 31)) * 8;
  seen = (u32x2){~tag[0], 0u};
  unsigned warm = 0;
  APAIR(8, 3, 3, 1, 0, 3, 3, NOP, DA(6, 0, 0), DA(6, 0, 1), DA(6, 0, 2), DA(6, 0, 3));
  APAIR(9, 3, 4, 0, 0, 2, 2, NOP, DA(7, 1, 0), DA(7, 1, 1), DA(7, 1, 2), DA(7, 1, 3));
  {
    const int R = 768 + (idx & 3) * 320 + w * 40 + (lane < 40 ? lane : 39);
    const char* pw = (R < 1024 ? wob + (size_t)R * 2048 : woa + (size_t)(R - 1024) * 2048) + nb * 128;
    asm volatile("global_load_dword %0, %1, off" : "+v"(warm) : "v"(pw) : "memory");
  }
  APAIR(10, 4, 4, 0, 0, 1, 0, NOP, NOP, NOP, NOP, NOP);
  APAIR(11, 4, 0, 0, 0, 0, 3, { asm volatile("s_waitcnt vmcnt(0)\n\ts_barrier" : "+v"(warm)::"memory");
                                if (tid == 0) flag_set(ws + WS_FOB + (rb * 32 + hB) * 8, tag);   }, NOP, NOP, NOP, NOP);
  APAIR(12, 0, 0, 0, 0, 3, 2, NOP, DW(0, 0), DW(0, 1), DW(1, 0), DW(1, 1));
  flag_peek(seen, fob, ws + WS_FOB);
  APAIR(13, 0, 1, 0, 0, 2, 1, NOP, DW(2, 0), DW(2, 1), DW(3, 0), DW(3, 1));
  APAIR(14, 1, 1, 0, 0, 0, 3, { asm volatile("s_waitcnt vmcnt(0)" : "+v"(seen)::"memory"); if (w == 0) flag_wait(seen, fob, ws + WS_FOB, tag); asm volatile("s_barrier" ::: "memory"); },
        NOP, NOP, (LO(0, 0), LO(0, 1), DW(4, 0)), (LO(0, 2), LO(0, 3), DW(4, 1)));
  APAIR(15, 1, 1, 0, 1, 2, 1, NOP, (LO(1, 0), LO(1, 1), DW(5, 0)), (LO(1, 2), LO(1, 3), DW(5, 1)), (LO(2, 0), LO(2, 1)), (LO(2, 2), LO(2, 3)));
  STAMP(8);

  loadO(3, of[3]);
  asm volatile("s_waitcnt lgkmcnt(0)\n\ts_barrier" ::: "memory");
  {
    float* xb = (float*)(lds + XCH_OFF) + w * (5 * 256);
    const f32x16& os = half ? o0 : o1;
    const float ls = half ? l0[0] : l1[0];
#pragma unroll
    for (int g = 0; g < 4; ++g) *(f32x4*)(xb + g * 256 + lane * 4) = (f32x4){os[4 * g], os[4 * g + 1], os[4 * g + 2], os[4 * g + 3]};
    xb[4 * 256 + lane] = ls;
  }
  f32x16 oA = half ? o1 : o0;
  float lA = half ? l1[0] : l0[0];
  auto finishA = [&]() {
    const float* xb = (const float*)(lds + XCH_OFF) + (w ^ 4) * (5 * 256);
#pragma unroll
    for (int g = 0; g < 4; ++g) {
      f32x4 v = *(const f32x4*)(xb + g * 256 + lane * 4);
#pragma unroll
      for (int e = 0; e < 4; ++e) oA[4 * g + e] += v[e];
    }
    lA += xb[4 * 256 + lane];
    if (__builtin_expect(ref_unsafe(oA, lA), 0)) {
      if (half) attn_exact<false>(kva, 0, 64, 0, q[1][0], q[1][1], ones, oA, lA);
      else attn_exact<false>(kva, 0, 64, 0, q[0][0], q[0][1], ones, oA, lA);
    }
    const float sc = __builtin_amdgcn_rcpf(lA);
    char* dst = ws + WS_O + (size_t)(qt * 64 + hA + half) * 2048;
    gstore16_sc1<0>(lo16, dst, pack8s(oA, 0, sc));
    gstore16_sc1<1024>(lo16, dst, pack8s(oA, 8, sc));
  };
  STAMP(9);
  {
    const int g16 = lane >> 4, q4 = (lane & 15) >> 2, p4 = lane & 3;
    const int wc = 2 * (g16 & 1) + (p4 >> 1);
    const int wa0 = (kh * 64 + 4 * h + q4) * 128 + ((wc ^ (((q4 >> 1) & 1) << 2)) << 4) + 8 * (p4 & 1);
    lds_cptr wbase[2][2] = {{(lds_cptr)lds + WO_OFF + wa0, (lds_cptr)lds + WO_OFF + (wa0 ^ 64)},
                            {(lds_cptr)lds + WO_OFF + 49152 + wa0, (lds_cptr)lds + WO_OFF + 49152 + (wa0 ^ 64)}};
    PIN(wbase[0][0]); PIN(wbase[0][1]); PIN(wbase[1][0]); PIN(wbase[1][1]);
    f32x16 accB[2] = {}, accA[2] = {};
    bf16x8 wf[4][2];
    const unsigned foa = (mb * 16 + (lane & 15)) * 8;
    u32x2 seenA = {~tag[0], 0u};
    float g = 0.f;
    auto rdw = [&](bf16x8 (&f)[2], int st, int ks) {
#pragma unroll
      for (int j = 0; j < 2; ++j) {
        lds_cptr p = wbase[(st % 6) / 3][j] + ((st % 6) % 3) * 16384 + ks * 2048;
        f[j] = cat8(lds_tr(p), lds_tr(p + 1024));
      }
    };
    asm volatile("s_waitcnt vmcnt(15)" : "+v"(of[0][0]), "+v"(of[0][1]), "+v"(of[0][2]), "+v"(of[0][3])::"memory");
    rdw(wf[0], 0, 0); rdw(wf[1], 0, 1);
#define OTRACE(i) do {} while (0)
#define O_MM(ACC, f, a) do { ACC[0] = MFMA32(f[0], a, ACC[0]); SBAR(); ACC[1] = MFMA32(f[1], a, ACC[1]); SBAR(); } while (0)
#define O_STAGE(st, ACC, VM, PRE, POST) do { \
      rdw(wf[2], st, 2); SBAR(); O_MM(ACC, wf[0], of[(st) % 5][0]); \
      rdw(wf[3], st, 3); SBAR(); O_MM(ACC, wf[1], of[(st) % 5][1]); \
      if ((st) < 15) { \
        OTRACE(3 * (st)); \
        if ((st) == 0) asm volatile("s_waitcnt lgkmcnt(0)" ::: "memory");     \
        asm volatile("s_waitcnt vmcnt(" #VM ")" : "+v"(of[((st) + 1) % 5][0]), "+v"(of[((st) + 1) % 5][1]), \
                     "+v"(of[((st) + 1) % 5][2]), "+v"(of[((st) + 1) % 5][3]), "+v"(seenA)::"memory"); \
        PRE; \
        OTRACE(3 * (st) + 1); \
        asm volatile("s_barrier" ::: "memory"); \
        OTRACE(3 * (st) + 2); \
        POST; \
        SBAR(); \
        rdw(wf[0], (st) + 1, 0); \
      } \
      SBAR(); O_MM(ACC, wf[2], of[(st) % 5][2]); \
      if ((st) < 15) rdw(wf[1], (st) + 1, 1); \
      SBAR(); O_MM(ACC, wf[3], of[(st) % 5][3]); \
    } while (0)
#define OW(st) (pieceW(st, 0), pieceW(st, 1))
#define OL(st) loadO(st, of[(st) % 5])
    O_STAGE(0, accB, 9, NOP, { OL(4); SBAR(); finishA(); });
    O_STAGE(1, accB, 10, NOP, (OW(6), OL(5)));
    O_STAGE(2, accB, 6, NOP, { if (tid == 0) flag_set(ws + WS_FOA + (pmb * 16 + pj) * 8, tag);   OW(7); OL(6); });
    O_STAGE(3, accB, 12, NOP, (OW(8), OL(7)));
    O_STAGE(4, accB, 12, NOP, (flag_peek(seenA, foa, ws + WS_FOA), OW(9)));
    O_STAGE(5, accB, 2, if (w == 0) flag_wait(seenA, foa, ws + WS_FOA, tag),
            (gload4_sc1(g, (wm * 32 + r) * 4, ws + WS_G + mb * 512), OW(10), OL(8)));
    O_STAGE(6, accB, 9, NOP, (OW(11), OL(9)));
    O_STAGE(7, accB, 6, NOP, (OW(12), OL(10)));
    O_STAGE(8, accA, 6, NOP, (OW(13), OL(11)));
    O_STAGE(9, accA, 6, NOP, (OW(14), OL(12)));
    O_STAGE(10, accA, 6, NOP, (OW(15), OL(13)));
    O_STAGE(11, accA, 6, NOP, OL(14));
    O_STAGE(12, accA, 4, NOP, OL(15));
    O_STAGE(13, accA, 4, NOP, NOP);
    O_STAGE(14, accA, 0, NOP, NOP);
    O_STAGE(15, accA, 0, NOP, NOP);
    asm volatile("" : "+v"(g));
    STAMP(10);
    float* xw = (float*)(lds + XCH_OFF) + w * 1024 + lane * 4;
    f32x16 acc[2];
#pragma unroll
    for (int j = 0; j < 2; ++j)
#pragma unroll
      for (int i = 0; i < 16; ++i) acc[j][i] = g * accA[j][i] + (1.f - g) * accB[j][i];
    const f32x16& as = kh ? acc[0] : acc[1];
#pragma unroll
    for (int g = 0; g < 4; ++g) *(f32x4*)(xw + g * 256) = (f32x4){as[4 * g], as[4 * g + 1], as[4 * g + 2], as[4 * g + 3]};
    asm volatile("s_waitcnt lgkmcnt(0)\n\ts_barrier" ::: "memory");
    f32x16 t = kh ? acc[1] : acc[0];
    const float* xr = (const float*)(lds + XCH_OFF) + (w ^ 4) * 1024 + lane * 4;
#pragma unroll
    for (int g = 0; g < 4; ++g) {
      f32x4 v = *(const f32x4*)(xr + g * 256);
#pragma unroll
      for (int e = 0; e < 4; ++e) t[4 * g + e] += v[e];
    }
    const int row = mb * 128 + wm * 32 + r;
    char* dst = out + (size_t)row * 2048 + (nb * 64 + kh * 32) * 2;
#pragma unroll
    for (int g = 0; g < 4; g += 2) {
      u32x2 a = {cvtpk(t[4 * g], t[4 * g + 1]), cvtpk(t[4 * g + 2], t[4 * g + 3])};
      u32x2 c = {cvtpk(t[4 * g + 4], t[4 * g + 5]), cvtpk(t[4 * g + 6], t[4 * g + 7])};
      auto x = __builtin_amdgcn_permlane32_swap(a[0], c[0], false, false);
      auto y = __builtin_amdgcn_permlane32_swap(a[1], c[1], false, false);
      *(u32x4*)(dst + g * 16 + h * 16) = (u32x4){x[0], y[0], x[1], y[1]};
    }
  }
  if (b == 0 && w == 0) {
    const unsigned long long want = ((unsigned long long)tag[1] << 32) | tag[0];
    gu64* f = (gu64*)(ws + WS_FOA) + lane;
    for (unsigned spins = 0; spins < (1u << 21); ++spins) {
      const unsigned long long a0 = __hip_atomic_load(f, __ATOMIC_RELAXED, __HIP_MEMORY_SCOPE_AGENT),
                               a1 = __hip_atomic_load(f + 64, __ATOMIC_RELAXED, __HIP_MEMORY_SCOPE_AGENT),
                               a2 = __hip_atomic_load(f + 128, __ATOMIC_RELAXED, __HIP_MEMORY_SCOPE_AGENT),
                               a3 = __hip_atomic_load(f + 192, __ATOMIC_RELAXED, __HIP_MEMORY_SCOPE_AGENT);
      if (__all(a0 == want && a1 == want && a2 == want && a3 == want)) break;
      __builtin_amdgcn_s_sleep(4);
    }
    if (lane == 0) flag_set(ws + WS_NONCE, tag);
  }
  STAMP(11);
  STAMP_OUT(0, ws); TRACE_OUT(ws);
}

extern "C" void kernel_launch(void* const* d_in, const int* in_sizes, int n_in, void* d_out, int out_size, void* d_ws,
                              size_t ws_size, hipStream_t stream) {
  const char* xa = (const char*)d_in[0];
  const char* wq = (const char*)d_in[1];
  const char* wk = (const char*)d_in[2];
  const char* wv = (const char*)d_in[3];
  const char* woa = (const char*)d_in[4];
  const char* wqkv = (const char*)d_in[6];
  const char* wob = (const char*)d_in[7];
  const char* gw = (const char*)d_in[8];
  (void)hipFuncSetAttribute((const void*)k_fused, hipFuncAttributeMaxDynamicSharedMemorySize, R_LDS);
  k_fused<<<256, 512, R_LDS, stream>>>(xa, wq, wk, wv, woa, wqkv, wob, gw, (char*)d_ws, (char*)d_out);
}
```

```cpp
#include <hip/hip_runtime.h>
#include <hip/hip_bf16.h>
#include <stdint.h>

typedef float f32x16 __attribute__((ext_vector_type(16)));
typedef float f32x4 __attribute__((ext_vector_type(4)));
typedef float f32x2 __attribute__((ext_vector_type(2)));
typedef short bf16x8 __attribute__((ext_vector_type(8)));
typedef short s16x4 __attribute__((ext_vector_type(4)));
typedef __bf16 bf16x2 __attribute__((ext_vector_type(2)));
typedef unsigned u32x4 __attribute__((ext_vector_type(4)));
typedef unsigned u32x2 __attribute__((ext_vector_type(2)));
typedef __attribute__((address_space(3))) const char* lds_cptr;
typedef __attribute__((address_space(3))) char* lds_ptr;

#define DI __device__ __forceinline__
#define SBAR() __builtin_amdgcn_sched_barrier(0)
#define PIN(x) asm volatile("" : "+v"(x))
#define MFMA32(a, b, c) __builtin_amdgcn_mfma_f32_32x32x16_bf16(a, b, c, 0, 0, 0)
#define MFMA16(a, b, c) __builtin_amdgcn_mfma_f32_16x16x32_bf16(a, b, c, 0, 0, 0)

constexpr int SEQ = 2048, DM = 1024;
constexpr float QSCALE = 0.17677669529663687f * 1.4426950408889634f;

constexpr size_t WS_KVA = 4u << 20;
constexpr size_t WS_G = WS_KVA + (256u << 10);
constexpr size_t WS_NONCE = WS_G + 8192;
constexpr size_t WS_FKV = WS_NONCE + 1024;
constexpr size_t WS_FPB = WS_FKV + 1024;
constexpr size_t WS_FOA = WS_FPB + 2048;
constexpr size_t WS_FOB = WS_FOA + 2048;
constexpr size_t WS_QB = 5u << 20;
constexpr size_t WS_KVB = 9u << 20;
constexpr size_t WS_O = 17u << 20;

DI unsigned rfl(unsigned x) { return __builtin_amdgcn_readfirstlane(x); }
DI unsigned cvtpk(float lo, float hi) {
  f32x2 v = {lo, hi};
  bf16x2 b = __builtin_convertvector(v, bf16x2);
  return __builtin_bit_cast(unsigned, b);
}
DI bf16x8 pack8(const f32x16& a, int o) {
  u32x4 r = {cvtpk(a[o], a[o + 1]), cvtpk(a[o + 2], a[o + 3]), cvtpk(a[o + 4], a[o + 5]), cvtpk(a[o + 6], a[o + 7])};
  return __builtin_bit_cast(bf16x8, r);
}
DI bf16x8 pack8s(const f32x16& a, int o, float s) {
  u32x4 r = {cvtpk(a[o] * s, a[o + 1] * s), cvtpk(a[o + 2] * s, a[o + 3] * s), cvtpk(a[o + 4] * s, a[o + 5] * s),
             cvtpk(a[o + 6] * s, a[o + 7] * s)};
  return __builtin_bit_cast(bf16x8, r);
}
template <int OFF>
DI void glds16(unsigned voff, const void* sbase, unsigned lds_base) {
  asm volatile("s_mov_b32 m0, %2\n\ts_nop 0\n\tglobal_load_lds_dwordx4 %0, %1 offset:%3"
               :: "v"(voff), "s"(sbase), "s"(rfl(lds_base)), "i"(OFF) : "memory");
}
template <int OFF>
DI void glds16_sc1(unsigned voff, const void* sbase, unsigned lds_base) {
  asm volatile("s_mov_b32 m0, %2\n\ts_nop 0\n\tglobal_load_lds_dwordx4 %0, %1 offset:%3 sc1"
               :: "v"(voff), "s"(sbase), "s"(rfl(lds_base)), "i"(OFF) : "memory");
}
DI void glds16v(const char* vaddr, unsigned lds_base) {
  asm volatile("s_mov_b32 m0, %1\n\ts_nop 0\n\tglobal_load_lds_dwordx4 %0, off" :: "v"(vaddr), "s"(rfl(lds_base)) : "memory");
}
DI bf16x8 lds_rd128(lds_cptr p) { return *(const __attribute__((address_space(3))) bf16x8*)p; }
DI s16x4 lds_tr(lds_cptr p) {
  typedef short v4i16_t __attribute__((ext_vector_type(4)));
  return __builtin_bit_cast(s16x4, __builtin_amdgcn_ds_read_tr16_b64_v4i16((__attribute__((address_space(3))) v4i16_t*)p));
}
DI bf16x8 cat8(s16x4 lo, s16x4 hi) { return (bf16x8){lo[0], lo[1], lo[2], lo[3], hi[0], hi[1], hi[2], hi[3]}; }
DI float bf2f(unsigned short b) { return __uint_as_float((unsigned)b << 16); }

template <int OFF>
DI void gload16_sc1(bf16x8& d, unsigned voff, const void* sbase) {
  asm volatile("global_load_dwordx4 %0, %1, %2 offset:%3 sc1" : "=v"(d) : "v"(voff), "s"(sbase), "i"(OFF) : "memory");
}
template <int OFF>
DI void gstore16_sc1(unsigned voff, void* sbase, const bf16x8& d) {
  asm volatile("global_store_dwordx4 %0, %1, %2 offset:%3 sc1\n\ts_nop 1" :: "v"(voff), "v"(d), "s"(sbase), "i"(OFF) : "memory");
}
DI void gload4_sc1(float& d, unsigned voff, const void* sbase) {
  asm volatile("global_load_dword %0, %1, %2 sc1" : "=v"(d) : "v"(voff), "s"(sbase) : "memory");
}
DI bf16x8 gload16_now(const char* p) {
  bf16x8 d;
  asm volatile("global_load_dwordx4 %0, %1, off sc1\n\ts_waitcnt vmcnt(0)" : "=&v"(d) : "v"(p) : "memory");
  return d;
}
typedef __attribute__((address_space(1))) unsigned long long gu64;
DI void flag_set(char* p, u32x2 tag) {
  asm volatile("global_store_dwordx2 %0, %1, %2 sc1\n\ts_nop 1" :: "v"(0u), "v"(tag), "s"(p) : "memory");
}
DI void flag_peek(u32x2& d, unsigned voff, const void* sbase) {
  asm volatile("global_load_dwordx2 %0, %1, %2 sc1" : "+v"(d) : "v"(voff), "s"(sbase) : "memory");
}
DI void flag_wait(u32x2 seen, unsigned voff, const char* sbase, u32x2 tag) {
  if (__builtin_expect(__all(seen[0] == tag[0] && seen[1] == tag[1]), 1)) return;
  const unsigned long long want = ((unsigned long long)tag[1] << 32) | tag[0];
  for (unsigned spins = 0; spins < (1u << 21); ++spins) {
    if (__all(__hip_atomic_load((gu64*)(sbase + voff), __ATOMIC_RELAXED, __HIP_MEMORY_SCOPE_AGENT) == want)) return;
    __builtin_amdgcn_s_sleep(4);
  }
}

DI float dot2bf(unsigned a, unsigned b, float c) {
  return __builtin_amdgcn_fdot2_f32_bf16(__builtin_bit_cast(bf16x2, a), __builtin_bit_cast(bf16x2, b), c, false);
}
DI float wave_sum63(float z) {
#define DPP_ADD(ctrl, rmask) z += __uint_as_float(__builtin_amdgcn_update_dpp(0u, __float_as_uint(z), ctrl, rmask, 0xf, true))
  DPP_ADD(0x111, 0xf); DPP_ADD(0x112, 0xf); DPP_ADD(0x114, 0xf); DPP_ADD(0x118, 0xf);
  DPP_ADD(0x142, 0xa); DPP_ADD(0x143, 0xc);
#undef DPP_ADD
  return z;
}

DI void nonce_load(u32x2& nonce, const char* ws) {
  asm volatile("global_load_dwordx2 %0, %1, %2 sc1" : "=v"(nonce) : "v"(0u), "s"(ws + WS_NONCE) : "memory");
}
DI u32x2 nonce_tag(u32x2 nonce) {
  u32x2 tag;
  tag[0] = nonce[0] + 1u;
  tag[1] = nonce[1] + (tag[0] == 0u ? 1u : 0u);
  return tag;
}

constexpr int P_SLOT = 49152;
constexpr int P_XOFF = 0, P_WOFF = 16384;
constexpr int QX_OFF = 0;
constexpr int KVC = 147456;
struct PDma {
  const char* xsrc; const char* wp[4]; unsigned xo[2], wstep[2], lds0; int w;
  DI void init_uniform(const char* xa, int mb, unsigned lds0_, int w_) {
    lds0 = lds0_; w = w_;
    xsrc = xa + (size_t)mb * 128 * 2048;
  }
  DI void init_lanes(const char* wq, const char* wqkv, int j, int lane) {
#pragma unroll
    for (int e = 0; e < 2; ++e) {
      const int row = 16 * w + 8 * e + (lane >> 3);
      xo[e] = row * 2048 + (((lane & 7) ^ (4 * e + (lane >> 4))) << 4);
    }
#pragma unroll
    for (int e = 0; e < 4; ++e) {
      const int krow = 8 * w + 2 * e + (lane >> 5);
      const int c = (lane & 31) ^ ((krow & 3) << 2);
      wp[e] = c < 24 ? wqkv + (size_t)krow * 6144 + j * 384 + c * 16 : wq + (size_t)krow * 2048 + j * 128 + (c - 24) * 16;
      if (e < 2) wstep[e] = c < 24 ? 64 * 6144 : 64 * 2048;
    }
  }
  DI void piece(int pc, int st, int slot) {
    const unsigned lb = lds0 + slot * P_SLOT;
    if (pc < 2) glds16<0>(xo[pc], xsrc + st * 128, lb + P_XOFF + w * 2048 + pc * 1024);
    else { glds16v(wp[pc - 2], lb + P_WOFF + w * 4096 + (pc - 2) * 1024); wp[pc - 2] += wstep[pc & 1]; }
  }
  DI void stage(int st, int slot) {
#pragma unroll
    for (int pc = 0; pc < 6; ++pc) piece(pc, st, slot);
  }
};
struct PFrag { bf16x8 x[2]; bf16x8 w[2]; };

template <bool VT, class Tail, class Peek, class Check, class Hook>
DI void proj_loop(PDma& dma, char* lds, f32x16 (&acc)[2][2], Tail tail, Peek peek, Check check, Hook hook) {
  const int tid = threadIdx.x, lane = tid & 63;
  const int w = dma.w;
  const int wm = w & 1, wn = w >> 1;
  const int r = lane & 31, h = lane >> 5;
  const int t3 = (r >> 1) & 7;
  const int xa0 = P_XOFF + (wm * 64 + r) * 128 + ((h ^ t3) << 4);
  const int g16 = lane >> 4, q4 = (lane & 15) >> 2, p4 = lane & 3;
  const int wc = wn * 8 + 2 * (g16 & 1) + (p4 >> 1);
  const int wa0 = P_WOFF + (8 * h + q4) * 512 + ((wc ^ (q4 << 2)) << 4) + 8 * (p4 & 1);
  auto rdx = [&](PFrag& f, int i, int slot, int s) { f.x[i] = lds_rd128((lds_cptr)lds + slot * P_SLOT + (xa0 ^ (s << 5)) + i * 4096); };
  auto rdw = [&](PFrag& f, int j, int slot, int s) {
    lds_cptr p = (lds_cptr)lds + slot * P_SLOT + (wa0 ^ (j << 6)) + s * 8192;
    f.w[j] = cat8(lds_tr(p), lds_tr(p + 2048));
  };
  auto mm = [&](const PFrag& f, int i, int j) {
    if constexpr (VT) acc[i][j] = MFMA32(f.x[i], f.w[j], acc[i][j]);
    else acc[i][j] = MFMA32(f.w[j], f.x[i], acc[i][j]);
  };
  auto win = [&](int j, int pc, int slot) {
    if (j == -1) dma.piece(pc, 2, 2);
    else if (j <= 12) dma.piece(pc, j + 3, slot);
    else if (j <= 14 && pc < 4) tail((j - 13) * 4 + pc);
  };
  auto step = [&](const PFrag& c, PFrag& n, int rslot, int rs, bool rd, int pa, int wj, int dslot) {
    mm(c, 0, 0); SBAR();
    if (rd) { rdw(n, 0, rslot, rs); rdx(n, 0, rslot, rs); }
    SBAR();
    mm(c, 0, 1); SBAR();
    if (rd) { rdw(n, 1, rslot, rs); rdx(n, 1, rslot, rs); }
    SBAR();
    mm(c, 1, 0); SBAR();
    if (pa >= 0) win(wj, pa, dslot);
    SBAR();
    mm(c, 1, 1); SBAR();
    if (pa >= 0) win(wj, pa + 1, dslot);
    SBAR();
  };
  PFrag f0, f1;
  rdw(f0, 0, 0, 0); rdx(f0, 0, 0, 0); rdw(f0, 1, 0, 0); rdx(f0, 1, 0, 0);
  auto iter = [&](int it, int slot, int pslot, int nslot, bool hk) {
    step(f0, f1, slot, 1, true, 2, it - 1, pslot);
    step(f1, f0, slot, 2, true, 4, it - 1, pslot);
    step(f0, f1, slot, 3, true, -1, 0, 0);
    asm volatile("s_waitcnt vmcnt(6) lgkmcnt(0)\n\ts_barrier" ::: "memory");
    if (hk) hook(it);
    SBAR();
    step(f1, f0, nslot, 0, true, 0, it, slot);
  };
  iter(0, 0, 0, 1, true); iter(1, 1, 0, 2, true); iter(2, 2, 1, 0, true); iter(3, 0, 2, 1, true);
  iter(4, 1, 0, 2, true); iter(5, 2, 1, 0, true); iter(6, 0, 2, 1, true);
  int slot = 1, pslot = 0;
  for (int it = 7; it < 13; ++it) {
    const int nslot = slot == 2 ? 0 : slot + 1;
    iter(it, slot, pslot, nslot, false);
    pslot = slot;
    slot = nslot;
  }
  peek();
  step(f0, f1, 1, 1, true, 2, 12, 0);
  step(f1, f0, 1, 2, true, 4, 12, 0);
  step(f0, f1, 1, 3, true, -1, 0, 0);
  check();
  SBAR();
  step(f1, f0, 2, 0, true, 0, 13, 0);
  step(f0, f1, 2, 1, true, 2, 13, 0);
  step(f1, f0, 2, 2, true, -1, 0, 0);
  step(f0, f1, 2, 3, true, -1, 0, 0);
  asm volatile("s_waitcnt vmcnt(4) lgkmcnt(0)\n\ts_barrier" ::: "memory");
  SBAR();
  step(f1, f0, 0, 0, true, 0, 14, 0);
  step(f0, f1, 0, 1, true, 2, 14, 0);
  step(f1, f0, 0, 2, true, -1, 0, 0);
  step(f0, f1, 0, 3, true, -1, 0, 0);
  asm volatile("s_waitcnt lgkmcnt(0)\n\ts_barrier" ::: "memory");
  SBAR();
  step(f1, f0, 0, 0, false, -1, 0, 0);
}

template <class Init>
DI void pre_kv(const char* __restrict__ xg, const char* __restrict__ wsel, char* __restrict__ ws, int rt, int t, char* lds, int w,
               PDma& dma, Init init, u32x2& tag, f32x16& acc, lds_cptr& xlast, lds_cptr& wlast) {
  const int tid = threadIdx.x, lane = tid & 63;
  const int r = lane & 31, h = lane >> 5;
  const unsigned lds0 = (unsigned)(uintptr_t)lds;
  u32x2 nonce;
  nonce_load(nonce, ws);
  const int xrow = 4 * w + (lane >> 4);
  const unsigned xo = xrow * 2048 + (((lane & 15) ^ (xrow & 15)) << 4), wo = w * 1024 + lane * 16;
  const char* xs = xg + (size_t)rt * 32 * 2048;
#define KV_OFF(u) ((u) == 0 || (u) == 7 ? KVC : P_SLOT + ((u) - 1) * 16384)
  auto sub = [&](int u) {
    const unsigned lb = lds0 + KV_OFF(u) + w * 1024;
    glds16<0>(xo, xs + u * 256, lb);
    glds16<0>(wo, wsel + u * 8192, lb + 8192);
  };
  sub(0); sub(1); sub(2); sub(3); sub(4); sub(5); sub(6);
  SBAR();
  init();
  const int g16 = lane >> 4, q4 = (lane & 15) >> 2, p4 = lane & 3;
  lds_cptr xa = (lds_cptr)lds + P_SLOT + r * 256 + (((2 * w + h) ^ (r & 15)) << 4);
  lds_cptr wa = (lds_cptr)lds + P_SLOT + 8192 + (16 * w + 8 * h + q4) * 64 + 32 * (g16 & 1) + 8 * p4;
  lds_cptr xc = xa + (KVC - P_SLOT), wc = wa + (KVC - P_SLOT);
  PIN(xa); PIN(wa); PIN(xc); PIN(wc);
  acc = (f32x16){};
  bf16x8 xf[4], wf[4];
#define KV_RD(i, u) do { \
    lds_cptr px_ = (u) == 0 ? xc : xa + ((u) - 1) * 16384; \
    lds_cptr pw_ = (u) == 0 ? wc : wa + ((u) - 1) * 16384; \
    xf[i] = lds_rd128(px_); wf[i] = cat8(lds_tr(pw_), lds_tr(pw_ + 256)); \
  } while (0)
#define KV_MM(i) do { if (t) acc = MFMA32(xf[i], wf[i], acc); else acc = MFMA32(wf[i], xf[i], acc); } while (0)
  asm volatile("s_waitcnt vmcnt(6)\n\ts_barrier" : "+v"(nonce)::"memory");
  KV_RD(0, 0); KV_RD(1, 1); KV_RD(2, 2); KV_RD(3, 3);
  SBAR();
  dma.stage(0, 0);
  SBAR();
  KV_MM(0); KV_MM(1); KV_MM(2); KV_MM(3);
  asm volatile("s_waitcnt vmcnt(6) lgkmcnt(0)\n\ts_barrier" ::: "memory");
  KV_RD(0, 4); KV_RD(1, 5); KV_RD(2, 6);
  SBAR();
  sub(7); dma.stage(1, 1);
  SBAR();
  KV_MM(0); KV_MM(1); KV_MM(2);
  asm volatile("s_waitcnt vmcnt(8) lgkmcnt(0)\n\ts_barrier" ::: "memory");
  dma.piece(0, 2, 2); dma.piece(1, 2, 2);
#undef KV_RD
#undef KV_MM
#undef KV_OFF
  tag = nonce_tag(nonce);
  xlast = xc; wlast = wc;
}

template <class Init>
DI void pre_gate(const char* __restrict__ xg, const char* __restrict__ gw, char* __restrict__ ws, int row0, int w, PDma& dma, Init init,
                 u32x2& tag) {
  const int tid = threadIdx.x, lane = tid & 63;
  u32x4 gx[2][2], gv[2];
  u32x2 nonce;
  const char* px = xg + (size_t)(row0 + 2 * w) * 2048;
  asm volatile("global_load_dwordx2 %6, %10, %11 sc1\n\t"
               "global_load_dwordx4 %0, %7, %8\n\tglobal_load_dwordx4 %1, %7, %8 offset:16\n\t"
               "global_load_dwordx4 %2, %7, %8 offset:2048\n\tglobal_load_dwordx4 %3, %7, %8 offset:2064\n\t"
               "global_load_dwordx4 %4, %7, %9\n\tglobal_load_dwordx4 %5, %7, %9 offset:16"
               : "=&v"(gx[0][0]), "=&v"(gx[0][1]), "=&v"(gx[1][0]), "=&v"(gx[1][1]), "=&v"(gv[0]), "=&v"(gv[1]), "=&v"(nonce)
               : "v"(lane * 32), "s"(px), "s"(gw), "v"(0u), "s"(ws + WS_NONCE) : "memory");
  SBAR();
  init();
  dma.stage(0, 0);
  asm volatile("s_barrier" ::: "memory");
  dma.stage(1, 1); dma.piece(0, 2, 2); dma.piece(1, 2, 2);
  asm volatile("s_waitcnt vmcnt(8)\n\ts_barrier"
               : "+v"(gx[0][0]), "+v"(gx[0][1]), "+v"(gx[1][0]), "+v"(gx[1][1]), "+v"(gv[0]), "+v"(gv[1]), "+v"(nonce)::"memory");
  tag = nonce_tag(nonce);
#pragma unroll
  for (int i = 0; i < 2; ++i) {
    float z = 0.f, z2 = 0.f;
#pragma unroll
    for (int e = 0; e < 4; ++e) { z = dot2bf(gx[i][0][e], gv[0][e], z); z2 = dot2bf(gx[i][1][e], gv[1][e], z2); }
    z += z2;
    z = wave_sum63(z);
    const float g = 1.f / (1.f + __expf(-z));
    if (lane == 63) asm volatile("global_store_dword %0, %1, %2 sc1\n\ts_nop 0" :: "v"(0u), "v"(g), "s"(ws + WS_G + (row0 + 2 * w + i) * 4) : "memory");
  }
}

constexpr int SL = 32768;
constexpr int R_LDS = 5 * SL;
constexpr int XCH_OFF = 0;
constexpr int WO_OFF = 2 * SL;

DI int crow(int r, int h) { return (r & 3) + 8 * (r >> 2) + 4 * h; }
DI float swap_half(float x) {
  auto rr = __builtin_amdgcn_permlane32_swap(__float_as_uint(x), __float_as_uint(x), false, false);
  return __uint_as_float((threadIdx.x & 32) ? rr[0] : rr[1]);
}
DI void exp16(f32x16& s) {
#pragma unroll
  for (int i = 0; i < 16; ++i) s[i] = __builtin_amdgcn_exp2f(s[i]);
}
#define AE(S, i) S[i] = __builtin_amdgcn_exp2f(S[i])
#define AC(P, S, i) P[(i) & 3] = cvtpk(S[2 * (i)], S[2 * (i) + 1])
#define ABC(x) __builtin_bit_cast(bf16x8, x)
#define APHASE(QK, PV, SX, PXa, PXb, SY, CINY, PYa, PYb, QY0, QY1, OY, LY, KF0, KF1, VF0, VF1, R1, R2) do { \
  if (QK) { SY = MFMA32(KF0, QY0, CINY); PIN(SY); } SBAR(); \
  AE(SX, 0); AE(SX, 1); AE(SX, 2); AE(SX, 3); PIN(SX); SBAR(); \
  if (QK) { SY = MFMA32(KF1, QY1, SY); PIN(SY); } SBAR(); \
  R1; \
  AC(PXa, SX, 0); AC(PXa, SX, 1); AE(SX, 4); AE(SX, 5); AE(SX, 6); PIN(SX); PIN(PXa); SBAR(); \
  if (PV) { OY = MFMA32(VF0, ABC(PYa), OY); PIN(OY); } SBAR(); \
  AE(SX, 7); AC(PXa, SX, 2); AE(SX, 8); AE(SX, 9); AC(PXa, SX, 3); PIN(SX); PIN(PXa); SBAR(); \
  if (PV) { LY = MFMA16(ones, ABC(PYa), LY); PIN(LY); } SBAR(); \
  R2; \
  AE(SX, 10); AE(SX, 11); PIN(SX); SBAR(); \
  if (PV) { OY = MFMA32(VF1, ABC(PYb), OY); PIN(OY); } SBAR(); \
  AC(PXb, SX, 4); AC(PXb, SX, 5); AE(SX, 12); AE(SX, 13); AE(SX, 14); PIN(SX); PIN(PXb); SBAR(); \
  if (PV) { LY = MFMA16(ones, ABC(PYb), LY); PIN(LY); } SBAR(); \
  AE(SX, 15); AC(PXb, SX, 6); AC(PXb, SX, 7); PIN(PXb); SBAR(); \
} while (0)

template <bool MASKED>
DI void attn_exact(const char* __restrict__ kv, int t0, int t1, int qt, const bf16x8& q0, const bf16x8& q1, const bf16x8& ones,
                   f32x16& o, float& l) {
  const int lane = threadIdx.x & 63, r = lane & 31, h = lane >> 5;
  float m = -3.0e38f;
  for (int t = t0; t < t1; ++t) {
    const char* p = kv + (size_t)t * 4096 + lane * 16;
    bf16x8 k0 = gload16_now(p), k1 = gload16_now(p + 1024);
    f32x16 s = {};
    s = MFMA32(k0, q0, s); s = MFMA32(k1, q1, s);
#pragma unroll
    for (int i = 0; i < 16; ++i) {
      bool ok = true;
      if (MASKED) { const int c = crow(i, h); ok = (t == qt) ? (c <= r) : ((t == qt - 8) ? (c > r) : true); }
      m = fmaxf(m, ok ? s[i] : -3.0e38f);
    }
  }
  m = fmaxf(m, swap_half(m));
  o = (f32x16){};
  f32x4 la = {};
  for (int t = t0; t < t1; ++t) {
    const char* p = kv + (size_t)t * 4096 + lane * 16;
    bf16x8 k0 = gload16_now(p), k1 = gload16_now(p + 1024);
    bf16x8 v0 = gload16_now(p + 2048), v1 = gload16_now(p + 3072);
    f32x16 s = {};
    s = MFMA32(k0, q0, s); s = MFMA32(k1, q1, s);
#pragma unroll
    for (int i = 0; i < 16; ++i) {
      bool ok = true;
      if (MASKED) { const int c = crow(i, h); ok = (t == qt) ? (c <= r) : ((t == qt - 8) ? (c > r) : true); }
      s[i] = ok ? __builtin_amdgcn_exp2f(s[i] - m) : 0.f;
    }
    bf16x8 pa = pack8(s, 0), pb = pack8(s, 8);
    o = MFMA32(v0, pa, o); o = MFMA32(v1, pb, o);
    la = MFMA16(ones, pa, la); la = MFMA16(ones, pb, la);
  }
  l = la[0];
}
DI bool ref_unsafe(const f32x16& o, float l) {
  float z = 0.f;
#pragma unroll
  for (int i = 0; i < 16; ++i) z = __builtin_fmaf(o[i], 0.f, z);
  const bool ok = (l >= 5.421010862e-20f) && (l <= 1.8446744e19f) && (z == 0.f);
  return __any(!ok);
}

__global__ __launch_bounds__(512, 2) void k_fused(const char* __restrict__ xa, const char* __restrict__ wq,
                                                  const char* __restrict__ wk, const char* __restrict__ wv,
                                                  const char* __restrict__ woa, const char* __restrict__ wqkv,
                                                  const char* __restrict__ wob, const char* __restrict__ gw,
                                                  char* __restrict__ ws, char* __restrict__ out) {
  extern __shared__ __attribute__((aligned(16))) char lds[];
  asm volatile("" :: "s"(xa), "s"(wq), "s"(wk), "s"(wv), "s"(wqkv), "s"(gw), "s"(ws));
  const int b = blockIdx.x;
  const int xcd = b & 7, idx = b >> 3;
  const int tid = threadIdx.x, lane = tid & 63;
  const int w = rfl(tid >> 6);
  const int r = lane & 31, h = lane >> 5;
  const unsigned lds0 = (unsigned)(uintptr_t)lds;
  const unsigned lo16 = lane * 16;
  const int pmb = (xcd & 1) * 8 + (idx & 7), pj = (xcd >> 1) * 4 + ((idx >> 3) & 1) * 2 + (idx >> 4);
  const int pr = w & 3, half = w >> 2;
  const char* kva = ws + WS_KVA;
  auto pieceA = [&](int k, int sl, int i) {
    const char* src = kva + (size_t)(half * 32 + 4 * k + pr) * 4096;
    const unsigned dst = lds0 + sl * SL + w * 4096;
    if (i == 0) glds16_sc1<0>(lo16, src, dst);
    else if (i == 1) glds16_sc1<1024>(lo16, src, dst);
    else if (i == 2) glds16_sc1<2048>(lo16, src, dst);
    else glds16_sc1<3072>(lo16, src, dst);
  };
  PDma dma;
  dma.init_uniform(xa, pmb, lds0, w);
  auto dinit = [&]() { dma.init_lanes(wq, wqkv, pj, lane); };
  u32x2 tag;
  const bool evenj = (pj & 1) == 0;
  const int kvrt = pmb * 4 + (pj >> 2), kvt = (pj >> 1) & 1;
  f32x16 kvacc = {};
  lds_cptr kvxl = (lds_cptr)lds, kvwl = (lds_cptr)lds;
  if (evenj) pre_kv(xa, kvt ? wv : wk, ws, kvrt, kvt, lds, w, dma, dinit, tag, kvacc, kvxl, kvwl);
  else pre_gate(xa, gw, ws, pmb * 128 + (pj >> 1) * 16, w, dma, dinit, tag);
  const int wn = w >> 1, pwm = w & 1;
  const int ct = 6 * pj + 2 * wn;
  const bool hasB = wn < 3;
  bf16x8 held[8];
  {
    f32x16 acc[2][2] = {};
    u32x2 kv0 = {~tag[0], 0u}, kv1 = kv0;
    auto tail = [&](int n) { pieceA(n >> 2, 2 + (n >> 2), n & 3); };
    auto peek = [&]() { flag_peek(kv0, lane * 8, ws + WS_FKV); flag_peek(kv1, lane * 8, ws + WS_FKV + 512); };
    auto check = [&]() {
      asm volatile("s_waitcnt vmcnt(4) lgkmcnt(0)" : "+v"(kv0), "+v"(kv1)::"memory");
      if (w == 0) { flag_wait(kv0, lane * 8, ws + WS_FKV, tag); flag_wait(kv1, lane * 8, ws + WS_FKV + 512, tag); }
      asm volatile("s_barrier" ::: "memory");
    };
    float* red = (float*)(lds + KVC) + lane * 4;
    auto put = [&](int s4) {
#pragma unroll
      for (int g = 0; g < 4; ++g) *(f32x4*)(red + (s4 * 4 + g) * 256) = (f32x4){kvacc[4 * g], kvacc[4 * g + 1], kvacc[4 * g + 2], kvacc[4 * g + 3]};
    };
    auto get = [&](int s4) {
#pragma unroll
      for (int g = 0; g < 4; ++g) {
        const f32x4 a = *(const f32x4*)(red + (s4 * 4 + g) * 256);
#pragma unroll
        for (int e = 0; e < 4; ++e) kvacc[4 * g + e] += a[e];
      }
    };
    auto hook = [&](int it) {
      if (evenj) {
        if (it == 0) {
          const bf16x8 xf = lds_rd128(kvxl), wf = cat8(lds_tr(kvwl), lds_tr(kvwl + 256));
          if (kvt) kvacc = MFMA32(xf, wf, kvacc); else kvacc = MFMA32(wf, xf, kvacc);
        }
        else if (it == 1) { if (w >= 4) put(w - 4); }
        else if (it == 2) { if (w < 4) get(w); }
        else if (it == 3) { if (w >= 1 && w < 4) put(w); }
        else if (it == 4) {
          if (w == 0) {
            get(1); get(2); get(3);
            char* dst = ws + WS_KVA + (size_t)kvrt * 4096 + kvt * 2048;
            gstore16_sc1<0>(lo16, dst, pack8(kvacc, 0));
            gstore16_sc1<1024>(lo16, dst, pack8(kvacc, 8));
          }
        } else if (it == 6) { if (tid == 0) flag_set(ws + WS_FKV + (kvrt * 2 + kvt) * 8, tag); }
      }
    };
    if (hasB && ct >= 64) proj_loop<true>(dma, lds, acc, tail, peek, check, hook);
    else proj_loop<false>(dma, lds, acc, tail, peek, check, hook);
    if (!hasB || ct < 32) {
#pragma unroll
      for (int i = 0; i < 2; ++i)
#pragma unroll
        for (int j = 0; j < 2; ++j)
#pragma unroll
          for (int e = 0; e < 16; ++e) acc[i][j][e] *= QSCALE;
    }
#pragma unroll
    for (int i = 0; i < 2; ++i)
#pragma unroll
      for (int j = 0; j < 2; ++j) {
        held[(i * 2 + j) * 2] = pack8(acc[i][j], 0);
        held[(i * 2 + j) * 2 + 1] = pack8(acc[i][j], 8);
      }
  }
  bf16x8 qb[2], q[2][2];
  typedef __attribute__((address_space(3))) bf16x8* lds_v8;
  const unsigned hstep = ct < 32 ? 2048 : 4096;
  char* hbase = ws + (ct < 32 ? WS_QB : WS_KVB + (ct >= 64 ? 2048 : 0)) + (size_t)((ct & 31) * 64 + pmb * 4 + pwm * 2) * hstep;
  auto storeH = [&](int n) {
    if (hasB) {
      char* dst = hbase + (size_t)((n >> 2) + ((n >> 1) & 1) * 64) * hstep;
      if (n & 1) gstore16_sc1<1024>(lo16, dst, held[n]);
      else gstore16_sc1<0>(lo16, dst, held[n]);
    }
  };
  if (!hasB) {
#pragma unroll
    for (int n = 0; n < 8; ++n)
      *(lds_v8)((lds_ptr)lds + QX_OFF + (((pwm * 2 + (n >> 2)) * 2 + ((n >> 1) & 1)) * 2 + (n & 1)) * 1024 + lo16) = held[n];
  }
  asm volatile("s_waitcnt vmcnt(4) lgkmcnt(0)\n\ts_barrier" ::: "memory");
#pragma unroll
  for (int u = 0; u < 2; ++u)
#pragma unroll
    for (int s2 = 0; s2 < 2; ++s2) q[u][s2] = lds_rd128((lds_cptr)lds + QX_OFF + ((pr * 2 + u) * 2 + s2) * 1024 + lo16);

  const int qt = pmb * 4 + pr, hA = 2 * pj;
  const int hB = (b & 7) + 8 * (b >> 6), rb = (b >> 3) & 7;
  const int qtB = rb * 8 + w;
  const int mb = (xcd & 3) * 4 + (idx & 3), nb = (xcd >> 2) * 8 + (idx >> 2);
  const char* kvb = ws + WS_KVB + (size_t)hB * 64 * 4096;
  bf16x8 ones;
  {
    const short v = ((lane & 3) == 0 && (((lane >> 4) & 1) == (((lane & 15) >> 2) & 1))) ? (short)0x3f80 : (short)0;
    ones = (bf16x8){v, v, v, v, v, v, v, v};
  }
  auto pieceB = [&](int n) {
    int gt = rb * 8 - 8 + 2 * w + (n >> 2);
    gt = gt < 0 ? 0 : gt;
    const char* src = kvb + (size_t)gt * 4096;
    const unsigned dst = lds0 + (2 * w + (n >> 2)) * 4096;
    if ((n & 3) == 0) glds16_sc1<0>(lo16, src, dst);
    else if ((n & 3) == 1) glds16_sc1<1024>(lo16, src, dst);
    else if ((n & 3) == 2) glds16_sc1<2048>(lo16, src, dst);
    else glds16_sc1<3072>(lo16, src, dst);
  };
  const char* pqb = ws + WS_QB + (size_t)(hB * 64 + qtB) * 2048;
  unsigned wo[2];
#pragma unroll
  for (int e = 0; e < 2; ++e) {
    const int row = 16 * w + 8 * e + (lane >> 3);
    const int c = (lane & 7) ^ (((row >> 1) & 1) << 2);
    wo[e] = row * 2048 + c * 16;
  }
  auto pieceW = [&](int st, int e) {
    const char* src = (st < 8 ? wob : woa) + (size_t)(st & 7) * 128 * 2048 + nb * 128;
    glds16<0>(wo[e], src, lds0 + WO_OFF + (st % 6) * 16384 + w * 2048 + e * 1024);
  };
#define NOP (void)0
  u32x2 seen;

  const f32x16 zero = {};
  f32x16 o0 = {}, o1 = {}, s0, s1;
  f32x4 l0 = {}, l1 = {};
  u32x4 p0a, p0b, p1a, p1b;
  bf16x8 ka0, ka1, kb0, kb1, va0, va1, vb0, vb1;
  lds_cptr kvbase = (lds_cptr)lds + half * 16384 + lane * 16;
#define APAIR(pp, SL0, SL1, FIRST, LAST, PA, PB, MID, D0, D1, D2, D3) do { \
    lds_cptr cur = kvbase + (SL0) * SL + ((pp) & 1) * 8192; PIN(cur);     \
    lds_cptr nxt = (SL0) == (SL1) && !((pp) & 1) ? cur + 8192 : kvbase + (SL1) * SL; \
    __builtin_amdgcn_s_setprio(PA); \
    if (FIRST) { ka0 = lds_rd128(cur); ka1 = lds_rd128(cur + 1024); s0 = MFMA32(ka0, q[0][0], zero); s0 = MFMA32(ka1, q[0][1], s0); } \
    SBAR(); \
    APHASE(1, !(FIRST), s0, p0a, p0b, s1, zero, p1a, p1b, q[1][0], q[1][1], o1, l1, ka0, ka1, vb0, vb1, \
           (kb0 = lds_rd128(cur + 4096), kb1 = lds_rd128(cur + 5120)), (va0 = lds_rd128(cur + 2048), va1 = lds_rd128(cur + 3072))); \
    D0; SBAR(); \
    APHASE(1, 1, s1, p1a, p1b, s0, zero, p0a, p0b, q[0][0], q[0][1], o0, l0, kb0, kb1, va0, va1, (void)0, (void)0); \
    D1; SBAR(); \
    MID; \
    __builtin_amdgcn_s_setprio(PB); SBAR(); \
    APHASE(1, 1, s0, p0a, p0b, s1, zero, p1a, p1b, q[1][0], q[1][1], o1, l1, kb0, kb1, va0, va1, \
           if (!(LAST)) (ka0 = lds_rd128(nxt), ka1 = lds_rd128(nxt + 1024)), (vb0 = lds_rd128(cur + 4096 + 2048), vb1 = lds_rd128(cur + 4096 + 3072))); \
    D2; SBAR(); \
    APHASE(!(LAST), 1, s1, p1a, p1b, s0, zero, p0a, p0b, q[0][0], q[0][1], o0, l0, ka0, ka1, vb0, vb1, (void)0, (void)0); \
    D3; SBAR(); \
    if (LAST) { \
      o1 = MFMA32(vb0, ABC(p1a), o1); o1 = MFMA32(vb1, ABC(p1b), o1); \
      l1 = MFMA16(ones, ABC(p1a), l1); l1 = MFMA16(ones, ABC(p1b), l1); \
    } \
  } while (0)
#define DA(k, sl, i) pieceA(k, sl, i)
#define DA2(k, sl, i) (pieceA(k, sl, i), pieceA(k, sl, (i) + 1))
#define DB2(n) (pieceB(n), pieceB((n) + 1))
#define DW(st, e) pieceW(st, e)
#define BAR_V(n) asm volatile("s_waitcnt vmcnt(" #n ")\n\ts_barrier" ::: "memory")
  const int fbm = 2 * rb - 2 + (lane & 3);
  const unsigned fbo = ((fbm < 0 ? 2 * rb : fbm) * 16 + ((lane >> 2) % 3 * 32 + hB) / 6) * 8;
  seen = (u32x2){~tag[0], 0u};
#define DH4(n) (storeH(n), storeH((n) + 1), storeH((n) + 2), storeH((n) + 3))
  APAIR(0, 2, 2, 1, 0, 2, 1, NOP, DA2(2, 4, 0), DA2(2, 4, 2), DH4(0), DH4(4));
  APAIR(1, 2, 3, 0, 0, 0, 3, { if (hasB) BAR_V(12); else BAR_V(4); }, NOP, NOP, NOP, NOP);
  APAIR(2, 3, 3, 0, 0, 2, 1, NOP, NOP, NOP, NOP, NOP);
  APAIR(3, 3, 4, 0, 0, 0, 3, { BAR_V(0); if (tid == 0) flag_set(ws + WS_FPB + (pmb * 16 + pj) * 8, tag);   },
        NOP, NOP, DA2(3, 2, 0), DA2(3, 2, 2));
  APAIR(4, 4, 4, 0, 0, 2, 1, NOP, NOP, NOP, NOP, NOP);
  flag_peek(seen, fbo, ws + WS_FPB);
  APAIR(5, 4, 2, 0, 0, 0, 3, { asm volatile("s_waitcnt vmcnt(0)" : "+v"(seen)::"memory"); if (w == 0) flag_wait(seen, fbo, ws + WS_FPB, tag); asm volatile("s_barrier" ::: "memory"); },
        NOP, NOP, DB2(0), DB2(2));
  APAIR(6, 2, 2, 0, 0, 2, 1, NOP, DB2(4), DB2(6), (gload16_sc1<0>(qb[0], lo16, pqb), gload16_sc1<1024>(qb[1], lo16, pqb)), DA2(4, 3, 0));
  APAIR(7, 2, 2, 0, 1, 1, 0, NOP, DA2(4, 3, 2), NOP, NOP, NOP);
  asm volatile("s_waitcnt vmcnt(0) lgkmcnt(0)\n\ts_barrier" : "+v"(qb[0]), "+v"(qb[1])::"memory");
  {
    f32x16 o = {};
    f32x4 la = {};
    lds_cptr bb = (lds_cptr)lds + w * 4096 + lane * 16;
    f32x16 mD;
#pragma unroll
    for (int i = 0; i < 16; ++i) mD[i] = crow(i, h) <= r ? 0.f : -3.0e38f;
    if (rb != 0) {
      f32x16 sa, sb;
#pragma unroll
      for (int i = 0; i < 16; ++i) sa[i] = -3.0e38f - mD[i];
      u32x4 pa0 = {}, pa1 = {}, pb0 = {}, pb1 = {};
      bf16x8 k0, k1, k2, k3, v0, v1, v2, v3;
      k0 = lds_rd128(bb); k1 = lds_rd128(bb + 1024);
      k2 = lds_rd128(bb + 4096); k3 = lds_rd128(bb + 4096 + 1024);
      sa = MFMA32(k0, qb[0], sa); sa = MFMA32(k1, qb[1], sa);
      SBAR();
#define BRDK(KA, KB, j) (KA = lds_rd128(bb + (j) * 4096), KB = lds_rd128(bb + (j) * 4096 + 1024))
#define BRDV(VA, VB, j) (VA = lds_rd128(bb + (j) * 4096 + 2048), VB = lds_rd128(bb + (j) * 4096 + 3072))
#define B_DMA(n) do { pieceA(5, 4, n); SBAR(); } while (0)
      __builtin_amdgcn_s_setprio(3);
      APHASE(1, 0, sa, pa0, pa1, sb, zero, pb0, pb1, qb[0], qb[1], o, la, k2, k3, v2, v3, BRDK(k0, k1, 2), BRDV(v0, v1, 0));
      B_DMA(0); B_DMA(1);
      APHASE(1, 1, sb, pb0, pb1, sa, zero, pa0, pa1, qb[0], qb[1], o, la, k0, k1, v0, v1, BRDK(k2, k3, 3), BRDV(v2, v3, 1));
      B_DMA(2); B_DMA(3);
      __builtin_amdgcn_s_setprio(2);
      APHASE(1, 1, sa, pa0, pa1, sb, zero, pb0, pb1, qb[0], qb[1], o, la, k2, k3, v2, v3, BRDK(k0, k1, 4), BRDV(v0, v1, 2));
      APHASE(1, 1, sb, pb0, pb1, sa, zero, pa0, pa1, qb[0], qb[1], o, la, k0, k1, v0, v1, BRDK(k2, k3, 5), BRDV(v2, v3, 3));
      __builtin_amdgcn_s_setprio(1);
      APHASE(1, 1, sa, pa0, pa1, sb, zero, pb0, pb1, qb[0], qb[1], o, la, k2, k3, v2, v3, BRDK(k0, k1, 6), BRDV(v0, v1, 4));
      APHASE(1, 1, sb, pb0, pb1, sa, zero, pa0, pa1, qb[0], qb[1], o, la, k0, k1, v0, v1, BRDK(k2, k3, 7), BRDV(v2, v3, 5));
      __builtin_amdgcn_s_setprio(0);
      APHASE(1, 1, sa, pa0, pa1, sb, zero, pb0, pb1, qb[0], qb[1], o, la, k2, k3, v2, v3, BRDK(k0, k1, 8), BRDV(v0, v1, 6));
      APHASE(1, 1, sb, pb0, pb1, sa, mD, pa0, pa1, qb[0], qb[1], o, la, k0, k1, v0, v1, (void)0, BRDV(v2, v3, 7));
      APHASE(0, 1, sa, pa0, pa1, sb, zero, pb0, pb1, qb[0], qb[1], o, la, k2, k3, v2, v3, (void)0, BRDV(v0, v1, 8));
      o = MFMA32(v0, ABC(pa0), o); o = MFMA32(v1, ABC(pa1), o);
      la = MFMA16(ones, ABC(pa0), la); la = MFMA16(ones, ABC(pa1), la);
    } else {
#pragma unroll
      for (int n = 0; n < 4; ++n) B_DMA(n);
      auto tile = [&](int j, const f32x16& cin) {
        lds_cptr p = bb + j * 4096;
        bf16x8 k0 = lds_rd128(p), k1 = lds_rd128(p + 1024), v0 = lds_rd128(p + 2048), v1 = lds_rd128(p + 3072);
        f32x16 s = MFMA32(k0, qb[0], cin);
        s = MFMA32(k1, qb[1], s);
        exp16(s);
        bf16x8 pa = pack8(s, 0), pb = pack8(s, 8);
        o = MFMA32(v0, pa, o); o = MFMA32(v1, pb, o);
        la = MFMA16(ones, pa, la); la = MFMA16(ones, pb, la);
      };
      for (int j = 8 - w; j < 8; ++j) tile(j, zero);
      tile(8, mD);
    }
    float l = la[0];
    if (__builtin_expect(ref_unsafe(o, l), 0)) attn_exact<true>(kvb, qtB >= 8 ? qtB - 8 : 0, qtB + 1, qtB, qb[0], qb[1], ones, o, l);
    const float sc = __builtin_amdgcn_rcpf(l);
    char* dst = ws + WS_O + (size_t)(qtB * 64 + 32 + hB) * 2048;
    gstore16_sc1<0>(lo16, dst, pack8s(o, 0, sc));
    gstore16_sc1<1024>(lo16, dst, pack8s(o, 8, sc));
  }
  asm volatile("s_waitcnt vmcnt(2) lgkmcnt(0)\n\ts_barrier" ::: "memory");
  const int wm = w & 3, kh = w >> 2;
  const char* obase = ws + WS_O + (size_t)(mb * 4 + wm) * 64 * 2048 + kh * 4096;
  bf16x8 of[5][4];
  auto loadO1 = [&](int st, int i, bf16x8& f) {
    const char* src = obase + (size_t)(st < 8 ? 32 + 4 * st : 4 * (st - 8)) * 2048;
    if (i == 0) gload16_sc1<0>(f, lo16, src);
    else if (i == 1) gload16_sc1<1024>(f, lo16, src);
    else if (i == 2) gload16_sc1<2048>(f, lo16, src);
    else gload16_sc1<3072>(f, lo16, src);
  };
#define LO(st, i) loadO1(st, i, of[st][i])
  auto loadO = [&](int st, bf16x8 (&f)[4]) {
    const char* src = obase + (size_t)(st < 8 ? 32 + 4 * st : 4 * (st - 8)) * 2048;
    gload16_sc1<0>(f[0], lo16, src); gload16_sc1<1024>(f[1], lo16, src);
    gload16_sc1<2048>(f[2], lo16, src); gload16_sc1<3072>(f[3], lo16, src);
  };
  const unsigned fob = ((mb >> 1) * 32 + (lane & 31)) * 8;
  seen = (u32x2){~tag[0], 0u};
  unsigned warm = 0;
  APAIR(8, 3, 3, 1, 0, 3, 3, NOP, DA(6, 0, 0), DA(6, 0, 1), DA(6, 0, 2), DA(6, 0, 3));
  APAIR(9, 3, 4, 0, 0, 2, 2, NOP, DA(7, 1, 0), DA(7, 1, 1), DA(7, 1, 2), DA(7, 1, 3));
  {
    const int R = 768 + (idx & 3) * 320 + w * 40 + (lane < 40 ? lane : 39);
    const char* pw = (R < 1024 ? wob + (size_t)R * 2048 : woa + (size_t)(R - 1024) * 2048) + nb * 128;
    asm volatile("global_load_dword %0, %1, off" : "+v"(warm) : "v"(pw) : "memory");
  }
  APAIR(10, 4, 4, 0, 0, 1, 0, NOP, NOP, NOP, NOP, NOP);
  APAIR(11, 4, 0, 0, 0, 0, 3, { asm volatile("s_waitcnt vmcnt(0)\n\ts_barrier" : "+v"(warm)::"memory");
                                if (tid == 0) flag_set(ws + WS_FOB + (rb * 32 + hB) * 8, tag);   }, NOP, NOP, NOP, NOP);
  APAIR(12, 0, 0, 0, 0, 3, 2, NOP, DW(0, 0), DW(0, 1), DW(1, 0), DW(1, 1));
  flag_peek(seen, fob, ws + WS_FOB);
  APAIR(13, 0, 1, 0, 0, 2, 1, NOP, DW(2, 0), DW(2, 1), DW(3, 0), DW(3, 1));
  APAIR(14, 1, 1, 0, 0, 0, 3, { asm volatile("s_waitcnt vmcnt(0)" : "+v"(seen)::"memory"); if (w == 0) flag_wait(seen, fob, ws + WS_FOB, tag); asm volatile("s_barrier" ::: "memory"); },
        NOP, NOP, (LO(0, 0), LO(0, 1), DW(4, 0)), (LO(0, 2), LO(0, 3), DW(4, 1)));
  APAIR(15, 1, 1, 0, 1, 2, 1, NOP, (LO(1, 0), LO(1, 1), DW(5, 0)), (LO(1, 2), LO(1, 3), DW(5, 1)), (LO(2, 0), LO(2, 1)), (LO(2, 2), LO(2, 3)));

  loadO(3, of[3]);
  asm volatile("s_waitcnt lgkmcnt(0)\n\ts_barrier" ::: "memory");
  {
    float* xb = (float*)(lds + XCH_OFF) + w * (5 * 256);
    const f32x16& os = half ? o0 : o1;
    const float ls = half ? l0[0] : l1[0];
#pragma unroll
    for (int g = 0; g < 4; ++g) *(f32x4*)(xb + g * 256 + lane * 4) = (f32x4){os[4 * g], os[4 * g + 1], os[4 * g + 2], os[4 * g + 3]};
    xb[4 * 256 + lane] = ls;
  }
  f32x16 oA = half ? o1 : o0;
  float lA = half ? l1[0] : l0[0];
  auto finishA = [&]() {
    const float* xb = (const float*)(lds + XCH_OFF) + (w ^ 4) * (5 * 256);
#pragma unroll
    for (int g = 0; g < 4; ++g) {
      f32x4 v = *(const f32x4*)(xb + g * 256 + lane * 4);
#pragma unroll
      for (int e = 0; e < 4; ++e) oA[4 * g + e] += v[e];
    }
    lA += xb[4 * 256 + lane];
    if (__builtin_expect(ref_unsafe(oA, lA), 0)) {
      if (half) attn_exact<false>(kva, 0, 64, 0, q[1][0], q[1][1], ones, oA, lA);
      else attn_exact<false>(kva, 0, 64, 0, q[0][0], q[0][1], ones, oA, lA);
    }
    const float sc = __builtin_amdgcn_rcpf(lA);
    char* dst = ws + WS_O + (size_t)(qt * 64 + hA + half) * 2048;
    gstore16_sc1<0>(lo16, dst, pack8s(oA, 0, sc));
    gstore16_sc1<1024>(lo16, dst, pack8s(oA, 8, sc));
  };
  {
    const int g16 = lane >> 4, q4 = (lane & 15) >> 2, p4 = lane & 3;
    const int wc = 2 * (g16 & 1) + (p4 >> 1);
    const int wa0 = (kh * 64 + 4 * h + q4) * 128 + ((wc ^ (((q4 >> 1) & 1) << 2)) << 4) + 8 * (p4 & 1);
    lds_cptr wbase[2][2] = {{(lds_cptr)lds + WO_OFF + wa0, (lds_cptr)lds + WO_OFF + (wa0 ^ 64)},
                            {(lds_cptr)lds + WO_OFF + 49152 + wa0, (lds_cptr)lds + WO_OFF + 49152 + (wa0 ^ 64)}};
    PIN(wbase[0][0]); PIN(wbase[0][1]); PIN(wbase[1][0]); PIN(wbase[1][1]);
    f32x16 accB[2] = {}, accA[2] = {};
    bf16x8 wf[4][2];
    const unsigned foa = (mb * 16 + (lane & 15)) * 8;
    u32x2 seenA = {~tag[0], 0u};
    float g = 0.f;
    auto rdw = [&](bf16x8 (&f)[2], int st, int ks) {
#pragma unroll
      for (int j = 0; j < 2; ++j) {
        lds_cptr p = wbase[(st % 6) / 3][j] + ((st % 6) % 3) * 16384 + ks * 2048;
        f[j] = cat8(lds_tr(p), lds_tr(p + 1024));
      }
    };
    asm volatile("s_waitcnt vmcnt(15)" : "+v"(of[0][0]), "+v"(of[0][1]), "+v"(of[0][2]), "+v"(of[0][3])::"memory");
    rdw(wf[0], 0, 0); rdw(wf[1], 0, 1);
#define O_MM(ACC, f, a) do { ACC[0] = MFMA32(f[0], a, ACC[0]); SBAR(); ACC[1] = MFMA32(f[1], a, ACC[1]); SBAR(); } while (0)
#define O_STAGE(st, ACC, VM, PRE, POST) do { \
      rdw(wf[2], st, 2); SBAR(); O_MM(ACC, wf[0], of[(st) % 5][0]); \
      rdw(wf[3], st, 3); SBAR(); O_MM(ACC, wf[1], of[(st) % 5][1]); \
      if ((st) < 15) { \
        if ((st) == 0) asm volatile("s_waitcnt lgkmcnt(0)" ::: "memory");     \
        asm volatile("s_waitcnt vmcnt(" #VM ")" : "+v"(of[((st) + 1) % 5][0]), "+v"(of[((st) + 1) % 5][1]), \
                     "+v"(of[((st) + 1) % 5][2]), "+v"(of[((st) + 1) % 5][3]), "+v"(seenA)::"memory"); \
        PRE; \
        asm volatile("s_barrier" ::: "memory"); \
        POST; \
        SBAR(); \
        rdw(wf[0], (st) + 1, 0); \
      } \
      SBAR(); O_MM(ACC, wf[2], of[(st) % 5][2]); \
      if ((st) < 15) rdw(wf[1], (st) + 1, 1); \
      SBAR(); O_MM(ACC, wf[3], of[(st) % 5][3]); \
    } while (0)
#define OW(st) (pieceW(st, 0), pieceW(st, 1))
#define OL(st) loadO(st, of[(st) % 5])
    O_STAGE(0, accB, 9, NOP, { OL(4); SBAR(); finishA(); });
    O_STAGE(1, accB, 10, NOP, (OW(6), OL(5)));
    O_STAGE(2, accB, 6, NOP, { if (tid == 0) flag_set(ws + WS_FOA + (pmb * 16 + pj) * 8, tag);   OW(7); OL(6); });
    O_STAGE(3, accB, 12, NOP, (OW(8), OL(7)));
    O_STAGE(4, accB, 12, NOP, (flag_peek(seenA, foa, ws + WS_FOA), OW(9)));
    O_STAGE(5, accB, 2, if (w == 0) flag_wait(seenA, foa, ws + WS_FOA, tag),
            (gload4_sc1(g, (wm * 32 + r) * 4, ws + WS_G + mb * 512), OW(10), OL(8)));
    O_STAGE(6, accB, 9, NOP, (OW(11), OL(9)));
    O_STAGE(7, accB, 6, NOP, (OW(12), OL(10)));
    O_STAGE(8, accA, 6, NOP, (OW(13), OL(11)));
    O_STAGE(9, accA, 6, NOP, (OW(14), OL(12)));
    O_STAGE(10, accA, 6, NOP, (OW(15), OL(13)));
    O_STAGE(11, accA, 6, NOP, OL(14));
    O_STAGE(12, accA, 4, NOP, OL(15));
    O_STAGE(13, accA, 4, NOP, NOP);
    O_STAGE(14, accA, 0, NOP, NOP);
    O_STAGE(15, accA, 0, NOP, NOP);
    asm volatile("" : "+v"(g));
    float* xw = (float*)(lds + XCH_OFF) + w * 1024 + lane * 4;
    f32x16 acc[2];
#pragma unroll
    for (int j = 0; j < 2; ++j)
#pragma unroll
      for (int i = 0; i < 16; ++i) acc[j][i] = g * accA[j][i] + (1.f - g) * accB[j][i];
    const f32x16& as = kh ? acc[0] : acc[1];
#pragma unroll
    for (int g = 0; g < 4; ++g) *(f32x4*)(xw + g * 256) = (f32x4){as[4 * g], as[4 * g + 1], as[4 * g + 2], as[4 * g + 3]};
    asm volatile("s_waitcnt lgkmcnt(0)\n\ts_barrier" ::: "memory");
    f32x16 t = kh ? acc[1] : acc[0];
    const float* xr = (const float*)(lds + XCH_OFF) + (w ^ 4) * 1024 + lane * 4;
#pragma unroll
    for (int g = 0; g < 4; ++g) {
      f32x4 v = *(const f32x4*)(xr + g * 256);
#pragma unroll
      for (int e = 0; e < 4; ++e) t[4 * g + e] += v[e];
    }
    const int row = mb * 128 + wm * 32 + r;
    char* dst = out + (size_t)row * 2048 + (nb * 64 + kh * 32) * 2;
#pragma unroll
    for (int g = 0; g < 4; g += 2) {
      u32x2 a = {cvtpk(t[4 * g], t[4 * g + 1]), cvtpk(t[4 * g + 2], t[4 * g + 3])};
      u32x2 c = {cvtpk(t[4 * g + 4], t[4 * g + 5]), cvtpk(t[4 * g + 6], t[4 * g + 7])};
      auto x = __builtin_amdgcn_permlane32_swap(a[0], c[0], false, false);
      auto y = __builtin_amdgcn_permlane32_swap(a[1], c[1], false, false);
      *(u32x4*)(dst + g * 16 + h * 16) = (u32x4){x[0], y[0], x[1], y[1]};
    }
  }
  if (b == 0 && w == 0) {
    const unsigned long long want = ((unsigned long long)tag[1] << 32) | tag[0];
    gu64* f = (gu64*)(ws + WS_FOA) + lane;
    for (unsigned spins = 0; spins < (1u << 21); ++spins) {
      const unsigned long long a0 = __hip_atomic_load(f, __ATOMIC_RELAXED, __HIP_MEMORY_SCOPE_AGENT),
                               a1 = __hip_atomic_load(f + 64, __ATOMIC_RELAXED, __HIP_MEMORY_SCOPE_AGENT),
                               a2 = __hip_atomic_load(f + 128, __ATOMIC_RELAXED, __HIP_MEMORY_SCOPE_AGENT),
                               a3 = __hip_atomic_load(f + 192, __ATOMIC_RELAXED, __HIP_MEMORY_SCOPE_AGENT);
      if (__all(a0 == want && a1 == want && a2 == want && a3 == want)) break;
      __builtin_amdgcn_s_sleep(4);
    }
    if (lane == 0) flag_set(ws + WS_NONCE, tag);
  }
}

extern "C" void kernel_launch(void* const* d_in, const int* in_sizes, int n_in, void* d_out, int out_size, void* d_ws,
                              size_t ws_size, hipStream_t stream) {
  const char* xa = (const char*)d_in[0];
  const char* wq = (const char*)d_in[1];
  const char* wk = (const char*)d_in[2];
  const char* wv = (const char*)d_in[3];
  const char* woa = (const char*)d_in[4];
  const char* wqkv = (const char*)d_in[6];
  const char* wob = (const char*)d_in[7];
  const char* gw = (const char*)d_in[8];
  (void)hipFuncSetAttribute((const void*)k_fused, hipFuncAttributeMaxDynamicSharedMemorySize, R_LDS);
  k_fused<<<256, 512, R_LDS, stream>>>(xa, wq, wk, wv, woa, wqkv, wob, gw, (char*)d_ws, (char*)d_out);
}
```

```cpp
#include <hip/hip_runtime.h>
#include <hip/hip_bf16.h>
#include <stdint.h>

typedef float f32x16 __attribute__((ext_vector_type(16)));
typedef float f32x4 __attribute__((ext_vector_type(4)));
typedef float f32x2 __attribute__((ext_vector_type(2)));
typedef short bf16x8 __attribute__((ext_vector_type(8)));
typedef short s16x4 __attribute__((ext_vector_type(4)));
typedef __bf16 bf16x2 __attribute__((ext_vector_type(2)));
typedef unsigned u32x4 __attribute__((ext_vector_type(4)));
typedef unsigned u32x2 __attribute__((ext_vector_type(2)));
typedef __attribute__((address_space(3))) const char* lds_cptr;
typedef __attribute__((address_space(3))) char* lds_ptr;

#define DI __device__ __forceinline__
#define SBAR() __builtin_amdgcn_sched_barrier(0)
#define PIN(x) asm volatile("" : "+v"(x))
#define MFMA32(a, b, c) __builtin_amdgcn_mfma_f32_32x32x16_bf16(a, b, c, 0, 0, 0)
#define MFMA16(a, b, c) __builtin_amdgcn_mfma_f32_16x16x32_bf16(a, b, c, 0, 0, 0)

constexpr int SEQ = 2048, DM = 1024;
constexpr float QSCALE = 0.17677669529663687f * 1.4426950408889634f;

constexpr size_t WS_KVA = 4u << 20;
constexpr size_t WS_G = WS_KVA + (256u << 10);
constexpr size_t WS_NONCE = WS_G + 8192;
constexpr size_t WS_FKV = WS_NONCE + 1024;
constexpr size_t WS_FPB = WS_FKV + 1024;
constexpr size_t WS_FOA = WS_FPB + 2048;
constexpr size_t WS_FOB = WS_FOA + 2048;
constexpr size_t WS_QB = 5u << 20;
constexpr size_t WS_KVB = 9u << 20;
constexpr size_t WS_O = 17u << 20;

#define TRACE(i) do {} while (0)
#define TRACE_DECL
#define TRACE_OUT(wsp) do {} while (0)
#define STAMP_ARG
#define STAMP_PASS
#define STAMP_DECL
#define STAMP(i) do {} while (0)
#define STAMP_OUT(k, wsp) do {} while (0)
#define PT(i) do {} while (0)
#define PTV(i) do {} while (0)
#define PT_ARG
#define PT_PASS
DI unsigned rfl(unsigned x) { return __builtin_amdgcn_readfirstlane(x); }
DI unsigned cvtpk(float lo, float hi) {
  f32x2 v = {lo, hi};
  bf16x2 b = __builtin_convertvector(v, bf16x2);
  return __builtin_bit_cast(unsigned, b);
}
DI bf16x8 pack8(const f32x16& a, int o) {
  u32x4 r = {cvtpk(a[o], a[o + 1]), cvtpk(a[o + 2], a[o + 3]), cvtpk(a[o + 4], a[o + 5]), cvtpk(a[o + 6], a[o + 7])};
  return __builtin_bit_cast(bf16x8, r);
}
DI bf16x8 pack8s(const f32x16& a, int o, float s) {
  u32x4 r = {cvtpk(a[o] * s, a[o + 1] * s), cvtpk(a[o + 2] * s, a[o + 3] * s), cvtpk(a[o + 4] * s, a[o + 5] * s),
             cvtpk(a[o + 6] * s, a[o + 7] * s)};
  return __builtin_bit_cast(bf16x8, r);
}
template <int OFF>
DI void glds16(unsigned voff, const void* sbase, unsigned lds_base) {
  asm volatile("s_mov_b32 m0, %2\n\ts_nop 0\n\tglobal_load_lds_dwordx4 %0, %1 offset:%3"
               :: "v"(voff), "s"(sbase), "s"(rfl(lds_base)), "i"(OFF) : "memory");
}
template <int OFF>
DI void glds16_sc1(unsigned voff, const void* sbase, unsigned lds_base) {
  asm volatile("s_mov_b32 m0, %2\n\ts_nop 0\n\tglobal_load_lds_dwordx4 %0, %1 offset:%3 sc1"
               :: "v"(voff), "s"(sbase), "s"(rfl(lds_base)), "i"(OFF) : "memory");
}
DI void glds16v(const char* vaddr, unsigned lds_base) {
  asm volatile("s_mov_b32 m0, %1\n\ts_nop 0\n\tglobal_load_lds_dwordx4 %0, off" :: "v"(vaddr), "s"(rfl(lds_base)) : "memory");
}
DI bf16x8 lds_rd128(lds_cptr p) { return *(const __attribute__((address_space(3))) bf16x8*)p; }
DI s16x4 lds_tr(lds_cptr p) {
  typedef short v4i16_t __attribute__((ext_vector_type(4)));
  return __builtin_bit_cast(s16x4, __builtin_amdgcn_ds_read_tr16_b64_v4i16((__attribute__((address_space(3))) v4i16_t*)p));
}
DI bf16x8 cat8(s16x4 lo, s16x4 hi) { return (bf16x8){lo[0], lo[1], lo[2], lo[3], hi[0], hi[1], hi[2], hi[3]}; }
DI float bf2f(unsigned short b) { return __uint_as_float((unsigned)b << 16); }

template <int OFF>
DI void gload16_sc1(bf16x8& d, unsigned voff, const void* sbase) {
  asm volatile("global_load_dwordx4 %0, %1, %2 offset:%3 sc1" : "=v"(d) : "v"(voff), "s"(sbase), "i"(OFF) : "memory");
}
template <int OFF>
DI void gstore16_sc1(unsigned voff, void* sbase, const bf16x8& d) {
  asm volatile("global_store_dwordx4 %0, %1, %2 offset:%3 sc1\n\ts_nop 1" :: "v"(voff), "v"(d), "s"(sbase), "i"(OFF) : "memory");
}
DI void gload4_sc1(float& d, unsigned voff, const void* sbase) {
  asm volatile("global_load_dword %0, %1, %2 sc1" : "=v"(d) : "v"(voff), "s"(sbase) : "memory");
}
DI bf16x8 gload16_now(const char* p) {
  bf16x8 d;
  asm volatile("global_load_dwordx4 %0, %1, off sc1\n\ts_waitcnt vmcnt(0)" : "=&v"(d) : "v"(p) : "memory");
  return d;
}
typedef __attribute__((address_space(1))) unsigned long long gu64;
DI void flag_set(char* p, u32x2 tag) {
  asm volatile("global_store_dwordx2 %0, %1, %2 sc1\n\ts_nop 1" :: "v"(0u), "v"(tag), "s"(p) : "memory");
}
DI void flag_peek(u32x2& d, unsigned voff, const void* sbase) {
  asm volatile("global_load_dwordx2 %0, %1, %2 sc1" : "+v"(d) : "v"(voff), "s"(sbase) : "memory");
}
DI void flag_wait(u32x2 seen, unsigned voff, const char* sbase, u32x2 tag) {
  if (__builtin_expect(__all(seen[0] == tag[0] && seen[1] == tag[1]), 1)) return;
  const unsigned long long want = ((unsigned long long)tag[1] << 32) | tag[0];
  for (unsigned spins = 0; spins < (1u << 21); ++spins) {
    if (__all(__hip_atomic_load((gu64*)(sbase + voff), __ATOMIC_RELAXED, __HIP_MEMORY_SCOPE_AGENT) == want)) return;
    __builtin_amdgcn_s_sleep(4);
  }
}

DI float dot2bf(unsigned a, unsigned b, float c) {
  return __builtin_amdgcn_fdot2_f32_bf16(__builtin_bit_cast(bf16x2, a), __builtin_bit_cast(bf16x2, b), c, false);
}
DI float wave_sum63(float z) {
#define DPP_ADD(ctrl, rmask) z += __uint_as_float(__builtin_amdgcn_update_dpp(0u, __float_as_uint(z), ctrl, rmask, 0xf, true))
  DPP_ADD(0x111, 0xf); DPP_ADD(0x112, 0xf); DPP_ADD(0x114, 0xf); DPP_ADD(0x118, 0xf);
  DPP_ADD(0x142, 0xa); DPP_ADD(0x143, 0xc);
#undef DPP_ADD
  return z;
}

DI void nonce_load(u32x2& nonce, const char* ws) {
  asm volatile("global_load_dwordx2 %0, %1, %2 sc1" : "=v"(nonce) : "v"(0u), "s"(ws + WS_NONCE) : "memory");
}
DI u32x2 nonce_tag(u32x2 nonce) {
  u32x2 tag;
  tag[0] = nonce[0] + 1u;
  tag[1] = nonce[1] + (tag[0] == 0u ? 1u : 0u);
  return tag;
}

constexpr int P_SLOT = 49152;
constexpr int P_XOFF = 0, P_WOFF = 16384;
constexpr int QX_OFF = 0;
constexpr int KVC = 147456;
struct PDma {
  const char* xsrc; const char* wp[4]; unsigned xo[2], wstep[2], lds0; int w;
  DI void init_uniform(const char* xa, int mb, unsigned lds0_, int w_) {
    lds0 = lds0_; w = w_;
    xsrc = xa + (size_t)mb * 128 * 2048;
  }
  DI void init_lanes(const char* wq, const char* wqkv, int j, int lane) {
#pragma unroll
    for (int e = 0; e < 2; ++e) {
      const int row = 16 * w + 8 * e + (lane >> 3);
      xo[e] = row * 2048 + (((lane & 7) ^ (4 * e + (lane >> 4))) << 4);
    }
#pragma unroll
    for (int e = 0; e < 4; ++e) {
      const int krow = 8 * w + 2 * e + (lane >> 5);
      const int c = (lane & 31) ^ ((krow & 3) << 2);
      wp[e] = c < 24 ? wqkv + (size_t)krow * 6144 + j * 384 + c * 16 : wq + (size_t)krow * 2048 + j * 128 + (c - 24) * 16;
      if (e < 2) wstep[e] = c < 24 ? 64 * 6144 : 64 * 2048;
    }
  }
  DI void piece(int pc, int st, int slot) {
    const unsigned lb = lds0 + slot * P_SLOT;
    if (pc < 2) glds16<0>(xo[pc], xsrc + st * 128, lb + P_XOFF + w * 2048 + pc * 1024);
    else { glds16v(wp[pc - 2], lb + P_WOFF + w * 4096 + (pc - 2) * 1024); wp[pc - 2] += wstep[pc & 1]; }
  }
  DI void stage(int st, int slot) {
#pragma unroll
    for (int pc = 0; pc < 6; ++pc) piece(pc, st, slot);
  }
};
struct PFrag { bf16x8 x[2]; bf16x8 w[2]; };

template <bool VT, class Tail, class Peek, class Check, class Hook>
DI void proj_loop(PDma& dma, char* lds, f32x16 (&acc)[2][2], Tail tail, Peek peek, Check check, Hook hook PT_ARG) {
  const int tid = threadIdx.x, lane = tid & 63;
  const int w = dma.w;
  const int wm = w & 1, wn = w >> 1;
  const int r = lane & 31, h = lane >> 5;
  const int t3 = (r >> 1) & 7;
  const int xa0 = P_XOFF + (wm * 64 + r) * 128 + ((h ^ t3) << 4);
  const int g16 = lane >> 4, q4 = (lane & 15) >> 2, p4 = lane & 3;
  const int wc = wn * 8 + 2 * (g16 & 1) + (p4 >> 1);
  const int wa0 = P_WOFF + (8 * h + q4) * 512 + ((wc ^ (q4 << 2)) << 4) + 8 * (p4 & 1);
  auto rdx = [&](PFrag& f, int i, int slot, int s) { f.x[i] = lds_rd128((lds_cptr)lds + slot * P_SLOT + (xa0 ^ (s << 5)) + i * 4096); };
  auto rdw = [&](PFrag& f, int j, int slot, int s) {
    lds_cptr p = (lds_cptr)lds + slot * P_SLOT + (wa0 ^ (j << 6)) + s * 8192;
    f.w[j] = cat8(lds_tr(p), lds_tr(p + 2048));
  };
  auto mm = [&](const PFrag& f, int i, int j) {
    if constexpr (VT) acc[i][j] = MFMA32(f.x[i], f.w[j], acc[i][j]);
    else acc[i][j] = MFMA32(f.w[j], f.x[i], acc[i][j]);
  };
  auto win = [&](int j, int pc, int slot) {
    if (j == -1) dma.piece(pc, 2, 2);
    else if (j <= 12) dma.piece(pc, j + 3, slot);
    else if (j <= 14 && pc < 4) tail((j - 13) * 4 + pc);
  };
  auto step = [&](const PFrag& c, PFrag& n, int rslot, int rs, bool rd, int pa, int wj, int dslot) {
    mm(c, 0, 0); SBAR();
    if (rd) { rdw(n, 0, rslot, rs); rdx(n, 0, rslot, rs); }
    SBAR();
    mm(c, 0, 1); SBAR();
    if (rd) { rdw(n, 1, rslot, rs); rdx(n, 1, rslot, rs); }
    SBAR();
    mm(c, 1, 0); SBAR();
    if (pa >= 0) win(wj, pa, dslot);
    SBAR();
    mm(c, 1, 1); SBAR();
    if (pa >= 0) win(wj, pa + 1, dslot);
    SBAR();
  };
  PFrag f0, f1;
  rdw(f0, 0, 0, 0); rdx(f0, 0, 0, 0); rdw(f0, 1, 0, 0); rdx(f0, 1, 0, 0);
  auto iter = [&](int it, int slot, int pslot, int nslot, bool hk) {
    step(f0, f1, slot, 1, true, 2, it - 1, pslot);
    step(f1, f0, slot, 2, true, 4, it - 1, pslot);
    step(f0, f1, slot, 3, true, -1, 0, 0);
    PTV(16 + 2 * it);
    asm volatile("s_waitcnt vmcnt(6) lgkmcnt(0)\n\ts_barrier" ::: "memory");
    PTV(17 + 2 * it);
    if (hk) hook(it);
    SBAR();
    step(f1, f0, nslot, 0, true, 0, it, slot);
  };
  iter(0, 0, 0, 1, true); iter(1, 1, 0, 2, true); iter(2, 2, 1, 0, true); iter(3, 0, 2, 1, true);
  iter(4, 1, 0, 2, true); iter(5, 2, 1, 0, true); iter(6, 0, 2, 1, true);
  int slot = 1, pslot = 0;
  for (int it = 7; it < 13; ++it) {
    const int nslot = slot == 2 ? 0 : slot + 1;
    iter(it, slot, pslot, nslot, false);
    pslot = slot;
    slot = nslot;
  }
  peek();
  step(f0, f1, 1, 1, true, 2, 12, 0);
  step(f1, f0, 1, 2, true, 4, 12, 0);
  step(f0, f1, 1, 3, true, -1, 0, 0);
  check();
  SBAR();
  step(f1, f0, 2, 0, true, 0, 13, 0);
  step(f0, f1, 2, 1, true, 2, 13, 0);
  step(f1, f0, 2, 2, true, -1, 0, 0);
  step(f0, f1, 2, 3, true, -1, 0, 0);
  asm volatile("s_waitcnt vmcnt(4) lgkmcnt(0)\n\ts_barrier" ::: "memory");
  SBAR();
  step(f1, f0, 0, 0, true, 0, 14, 0);
  step(f0, f1, 0, 1, true, 2, 14, 0);
  step(f1, f0, 0, 2, true, -1, 0, 0);
  step(f0, f1, 0, 3, true, -1, 0, 0);
  asm volatile("s_waitcnt lgkmcnt(0)\n\ts_barrier" ::: "memory");
  SBAR();
  step(f1, f0, 0, 0, false, -1, 0, 0);
}

template <class Init>
DI void pre_kv(const char* __restrict__ xg, const char* __restrict__ wsel, char* __restrict__ ws, int rt, int t, char* lds, int w,
               PDma& dma, Init init, u32x2& tag, f32x16& acc, lds_cptr& xlast, lds_cptr& wlast PT_ARG) {
  const int tid = threadIdx.x, lane = tid & 63;
  const int r = lane & 31, h = lane >> 5;
  const unsigned lds0 = (unsigned)(uintptr_t)lds;
  u32x2 nonce;
  const int xrow = 4 * w + (lane >> 4);
  const unsigned xo = xrow * 2048 + (((lane & 15) ^ (xrow & 15)) << 4), wo = w * 1024 + lane * 16;
  const char* xs = xg + (size_t)rt * 32 * 2048;
#define KV_OFF(u) ((u) == 0 || (u) == 7 ? KVC : P_SLOT + ((u) - 1) * 16384)
  auto sub = [&](int u) {
    const unsigned lb = lds0 + KV_OFF(u) + w * 1024;
    glds16<0>(xo, xs + u * 256, lb);
    glds16<0>(wo, wsel + u * 8192, lb + 8192);
  };
  sub(0); sub(1); sub(2); sub(3); sub(4); sub(5); sub(6);
  nonce_load(nonce, ws);
  SBAR();
  init();
  PT(0);
  const int g16 = lane >> 4, q4 = (lane & 15) >> 2, p4 = lane & 3;
  lds_cptr xa = (lds_cptr)lds + P_SLOT + r * 256 + (((2 * w + h) ^ (r & 15)) << 4);
  lds_cptr wa = (lds_cptr)lds + P_SLOT + 8192 + (16 * w + 8 * h + q4) * 64 + 32 * (g16 & 1) + 8 * p4;
  lds_cptr xc = xa + (KVC - P_SLOT), wc = wa + (KVC - P_SLOT);
  PIN(xa); PIN(wa); PIN(xc); PIN(wc);
  acc = (f32x16){};
  bf16x8 xf[4], wf[4];
#define KV_RD(i, u) do { \
    lds_cptr px_ = (u) == 0 ? xc : xa + ((u) - 1) * 16384; \
    lds_cptr pw_ = (u) == 0 ? wc : wa + ((u) - 1) * 16384; \
    xf[i] = lds_rd128(px_); wf[i] = cat8(lds_tr(pw_), lds_tr(pw_ + 256)); \
  } while (0)
#define KV_MM(i) do { if (t) acc = MFMA32(xf[i], wf[i], acc); else acc = MFMA32(wf[i], xf[i], acc); } while (0)
  asm volatile("s_waitcnt vmcnt(7)\n\ts_barrier" ::: "memory");
  PT(1);
  KV_RD(0, 0); KV_RD(1, 1); KV_RD(2, 2); KV_RD(3, 3);
  SBAR();
  dma.stage(0, 0);
  SBAR();
  KV_MM(0); KV_MM(1); KV_MM(2); KV_MM(3);
  asm volatile("s_waitcnt vmcnt(7) lgkmcnt(0)\n\ts_barrier" ::: "memory");
  PT(2);
  KV_RD(0, 4); KV_RD(1, 5); KV_RD(2, 6);
  SBAR();
  sub(7); dma.stage(1, 1);
  SBAR();
  KV_MM(0); KV_MM(1); KV_MM(2);
  asm volatile("s_waitcnt vmcnt(8) lgkmcnt(0)\n\ts_barrier" : "+v"(nonce)::"memory");
  PT(3);
  dma.piece(0, 2, 2); dma.piece(1, 2, 2);
#undef KV_RD
#undef KV_MM
#undef KV_OFF
  tag = nonce_tag(nonce);
  xlast = xc; wlast = wc;
  PT(9);
}

template <class Init>
DI void pre_gate(const char* __restrict__ xg, const char* __restrict__ gw, char* __restrict__ ws, int row0, int w, PDma& dma, Init init,
                 u32x2& tag PT_ARG) {
  const int tid = threadIdx.x, lane = tid & 63;
  u32x4 gx[2][2], gv[2];
  u32x2 nonce;
  const char* px = xg + (size_t)(row0 + 2 * w) * 2048;
  asm volatile("global_load_dwordx2 %6, %10, %11 sc1\n\t"
               "global_load_dwordx4 %0, %7, %8\n\tglobal_load_dwordx4 %1, %7, %8 offset:16\n\t"
               "global_load_dwordx4 %2, %7, %8 offset:2048\n\tglobal_load_dwordx4 %3, %7, %8 offset:2064\n\t"
               "global_load_dwordx4 %4, %7, %9\n\tglobal_load_dwordx4 %5, %7, %9 offset:16"
               : "=&v"(gx[0][0]), "=&v"(gx[0][1]), "=&v"(gx[1][0]), "=&v"(gx[1][1]), "=&v"(gv[0]), "=&v"(gv[1]), "=&v"(nonce)
               : "v"(lane * 32), "s"(px), "s"(gw), "v"(0u), "s"(ws + WS_NONCE) : "memory");
  SBAR();
  init();
  dma.stage(0, 0);
  PT(0);
  asm volatile("s_barrier" ::: "memory");
  PT(1);
  dma.stage(1, 1); dma.piece(0, 2, 2); dma.piece(1, 2, 2);
  PT(2);
  asm volatile("s_waitcnt vmcnt(8)\n\ts_barrier"
               : "+v"(gx[0][0]), "+v"(gx[0][1]), "+v"(gx[1][0]), "+v"(gx[1][1]), "+v"(gv[0]), "+v"(gv[1]), "+v"(nonce)::"memory");
  PT(3);
  tag = nonce_tag(nonce);
#pragma unroll
  for (int i = 0; i < 2; ++i) {
    float z = 0.f, z2 = 0.f;
#pragma unroll
    for (int e = 0; e < 4; ++e) { z = dot2bf(gx[i][0][e], gv[0][e], z); z2 = dot2bf(gx[i][1][e], gv[1][e], z2); }
    z += z2;
    z = wave_sum63(z);
    const float g = 1.f / (1.f + __expf(-z));
    if (lane == 63) asm volatile("global_store_dword %0, %1, %2 sc1\n\ts_nop 0" :: "v"(0u), "v"(g), "s"(ws + WS_G + (row0 + 2 * w + i) * 4) : "memory");
  }
}

constexpr int SL = 32768;
constexpr int R_LDS = 5 * SL;
constexpr int XCH_OFF = 0;
constexpr int WO_OFF = 2 * SL;

DI int crow(int r, int h) { return (r & 3) + 8 * (r >> 2) + 4 * h; }
constexpr unsigned long long diag_lanes(int i) {
  unsigned long long m = 0;
  for (int l = 0; l < 64; ++l)
    if ((i & 3) + 8 * (i >> 2) + 4 * (l >> 5) <= (l & 31)) m |= 1ull << l;
  return m;
}
template <int I>
DI float diag_select(float keep_not, float keep) {
  float r;
  asm("v_cndmask_b32_e64 %0, %1, %2, %3" : "=v"(r) : "v"(keep_not), "v"(keep), "s"(diag_lanes(I)));
  return r;
}
template <int I>
DI void diag_masks(f32x16& mD, f32x16& mF, float neg, float zero) {
  if constexpr (I < 16) {
    mD[I] = diag_select<I>(neg, zero);
    mF[I] = diag_select<I>(zero, neg);
    diag_masks<I + 1>(mD, mF, neg, zero);
  }
}
DI float swap_half(float x) {
  auto rr = __builtin_amdgcn_permlane32_swap(__float_as_uint(x), __float_as_uint(x), false, false);
  return __uint_as_float((threadIdx.x & 32) ? rr[0] : rr[1]);
}
DI void exp16(f32x16& s) {
#pragma unroll
  for (int i = 0; i < 16; ++i) s[i] = __builtin_amdgcn_exp2f(s[i]);
}
#define AE(S, i) S[i] = __builtin_amdgcn_exp2f(S[i])
#define AC(P, S, i) P[(i) & 3] = cvtpk(S[2 * (i)], S[2 * (i) + 1])
#define ABC(x) __builtin_bit_cast(bf16x8, x)
#define APHASE(QK, PV, SX, PXa, PXb, SY, CINY, PYa, PYb, QY0, QY1, OY, LY, KF0, KF1, VF0, VF1, R1, R2) do { \
  if (QK) { SY = MFMA32(KF0, QY0, CINY); PIN(SY); } SBAR(); \
  AE(SX, 0); AE(SX, 1); AE(SX, 2); AE(SX, 3); PIN(SX); SBAR(); \
  if (QK) { SY = MFMA32(KF1, QY1, SY); PIN(SY); } SBAR(); \
  R1; \
  AC(PXa, SX, 0); AC(PXa, SX, 1); AE(SX, 4); AE(SX, 5); AE(SX, 6); PIN(SX); PIN(PXa); SBAR(); \
  if (PV) { OY = MFMA32(VF0, ABC(PYa), OY); PIN(OY); } SBAR(); \
  AE(SX, 7); AC(PXa, SX, 2); AE(SX, 8); AE(SX, 9); AC(PXa, SX, 3); PIN(SX); PIN(PXa); SBAR(); \
  if (PV) { LY = MFMA16(ones, ABC(PYa), LY); PIN(LY); } SBAR(); \
  R2; \
  AE(SX, 10); AE(SX, 11); PIN(SX); SBAR(); \
  if (PV) { OY = MFMA32(VF1, ABC(PYb), OY); PIN(OY); } SBAR(); \
  AC(PXb, SX, 4); AC(PXb, SX, 5); AE(SX, 12); AE(SX, 13); AE(SX, 14); PIN(SX); PIN(PXb); SBAR(); \
  if (PV) { LY = MFMA16(ones, ABC(PYb), LY); PIN(LY); } SBAR(); \
  AE(SX, 15); AC(PXb, SX, 6); AC(PXb, SX, 7); PIN(PXb); SBAR(); \
} while (0)

template <bool MASKED>
DI void attn_exact(const char* __restrict__ kv, int t0, int t1, int qt, const bf16x8& q0, const bf16x8& q1, const bf16x8& ones,
                   f32x16& o, float& l) {
  const int lane = threadIdx.x & 63, r = lane & 31, h = lane >> 5;
  float m = -3.0e38f;
  for (int t = t0; t < t1; ++t) {
    const char* p = kv + (size_t)t * 4096 + lane * 16;
    bf16x8 k0 = gload16_now(p), k1 = gload16_now(p + 1024);
    f32x16 s = {};
    s = MFMA32(k0, q0, s); s = MFMA32(k1, q1, s);
#pragma unroll
    for (int i = 0; i < 16; ++i) {
      bool ok = true;
      if (MASKED) { const int c = crow(i, h); ok = (t == qt) ? (c <= r) : ((t == qt - 8) ? (c > r) : true); }
      m = fmaxf(m, ok ? s[i] : -3.0e38f);
    }
  }
  m = fmaxf(m, swap_half(m));
  o = (f32x16){};
  f32x4 la = {};
  for (int t = t0; t < t1; ++t) {
    const char* p = kv + (size_t)t * 4096 + lane * 16;
    bf16x8 k0 = gload16_now(p), k1 = gload16_now(p + 1024);
    bf16x8 v0 = gload16_now(p + 2048), v1 = gload16_now(p + 3072);
    f32x16 s = {};
    s = MFMA32(k0, q0, s); s = MFMA32(k1, q1, s);
#pragma unroll
    for (int i = 0; i < 16; ++i) {
      bool ok = true;
      if (MASKED) { const int c = crow(i, h); ok = (t == qt) ? (c <= r) : ((t == qt - 8) ? (c > r) : true); }
      s[i] = ok ? __builtin_amdgcn_exp2f(s[i] - m) : 0.f;
    }
    bf16x8 pa = pack8(s, 0), pb = pack8(s, 8);
    o = MFMA32(v0, pa, o); o = MFMA32(v1, pb, o);
    la = MFMA16(ones, pa, la); la = MFMA16(ones, pb, la);
  }
  l = la[0];
}
DI bool ref_unsafe(const f32x16& o, float l) {
  float z = 0.f;
#pragma unroll
  for (int i = 0; i < 16; ++i) z = __builtin_fmaf(o[i], 0.f, z);
  const bool ok = (l >= 5.421010862e-20f) && (l <= 1.8446744e19f) && (z == 0.f);
  return __any(!ok);
}

__global__ __launch_bounds__(512, 2) void k_fused(const char* __restrict__ xa, const char* __restrict__ wq,
                                                  const char* __restrict__ wk, const char* __restrict__ wv,
                                                  const char* __restrict__ woa, const char* __restrict__ wqkv,
                                                  const char* __restrict__ wob, const char* __restrict__ gw,
                                                  char* __restrict__ ws, char* __restrict__ out) {
  extern __shared__ __attribute__((aligned(16))) char lds[];
  STAMP_DECL STAMP(0); TRACE_DECL
  asm volatile("" :: "s"(xa), "s"(wq), "s"(wk), "s"(wv), "s"(wqkv), "s"(gw), "s"(ws));
  const int b = blockIdx.x;
  const int xcd = b & 7, idx = b >> 3;
  const int tid = threadIdx.x, lane = tid & 63;
  const int w = rfl(tid >> 6);
  const int r = lane & 31, h = lane >> 5;
  const unsigned lds0 = (unsigned)(uintptr_t)lds;
  const unsigned lo16 = lane * 16;
  const int pmb = (xcd & 1) * 8 + (idx & 7), pj = (xcd >> 1) * 4 + ((idx >> 3) & 1) * 2 + (idx >> 4);
  const int pr = w & 3, half = w >> 2;
  const char* kva = ws + WS_KVA;
  auto pieceA = [&](int k, int sl, int i) {
    const char* src = kva + (size_t)(half * 32 + 4 * k + pr) * 4096;
    const unsigned dst = lds0 + sl * SL + w * 4096;
    if (i == 0) glds16_sc1<0>(lo16, src, dst);
    else if (i == 1) glds16_sc1<1024>(lo16, src, dst);
    else if (i == 2) glds16_sc1<2048>(lo16, src, dst);
    else glds16_sc1<3072>(lo16, src, dst);
  };
  PDma dma;
  dma.init_uniform(xa, pmb, lds0, w);
  auto dinit = [&]() { dma.init_lanes(wq, wqkv, pj, lane); };
  u32x2 tag;
  const bool evenj = (pj & 1) == 0;
  const int kvrt = pmb * 4 + (pj >> 2), kvt = (pj >> 1) & 1;
  PT(11);
  f32x16 kvacc = {};
  lds_cptr kvxl = (lds_cptr)lds, kvwl = (lds_cptr)lds;
  if (evenj) pre_kv(xa, kvt ? wv : wk, ws, kvrt, kvt, lds, w, dma, dinit, tag, kvacc, kvxl, kvwl PT_PASS);
  else pre_gate(xa, gw, ws, pmb * 128 + (pj >> 1) * 16, w, dma, dinit, tag PT_PASS);
  PT(12);
  STAMP(1);
  const int wn = w >> 1, pwm = w & 1;
  const int ct = 6 * pj + 2 * wn;
  const bool hasB = wn < 3;
  bf16x8 held[8];
  {
    f32x16 acc[2][2] = {};
    u32x2 kv0 = {~tag[0], 0u}, kv1 = kv0;
    auto tail = [&](int n) { pieceA(n >> 2, 2 + (n >> 2), n & 3); };
    auto peek = [&]() { flag_peek(kv0, lane * 8, ws + WS_FKV); flag_peek(kv1, lane * 8, ws + WS_FKV + 512); };
    auto check = [&]() {
      asm volatile("s_waitcnt vmcnt(4) lgkmcnt(0)" : "+v"(kv0), "+v"(kv1)::"memory");
      if (w == 0) { flag_wait(kv0, lane * 8, ws + WS_FKV, tag); flag_wait(kv1, lane * 8, ws + WS_FKV + 512, tag); }
      asm volatile("s_barrier" ::: "memory");
    };
    float* red = (float*)(lds + KVC) + lane * 4;
    auto put = [&](int s4) {
#pragma unroll
      for (int g = 0; g < 4; ++g) *(f32x4*)(red + (s4 * 4 + g) * 256) = (f32x4){kvacc[4 * g], kvacc[4 * g + 1], kvacc[4 * g + 2], kvacc[4 * g + 3]};
    };
    auto get = [&](int s4) {
#pragma unroll
      for (int g = 0; g < 4; ++g) {
        const f32x4 a = *(const f32x4*)(red + (s4 * 4 + g) * 256);
#pragma unroll
        for (int e = 0; e < 4; ++e) kvacc[4 * g + e] += a[e];
      }
    };
    auto hook = [&](int it) {
      if (evenj) {
        if (it == 0) {
          const bf16x8 xf = lds_rd128(kvxl), wf = cat8(lds_tr(kvwl), lds_tr(kvwl + 256));
          if (kvt) kvacc = MFMA32(xf, wf, kvacc); else kvacc = MFMA32(wf, xf, kvacc);
        }
        else if (it == 1) { if (w >= 4) put(w - 4); }
        else if (it == 2) { if (w < 4) get(w); }
        else if (it == 3) { if (w >= 1 && w < 4) put(w); }
        else if (it == 4) {
          if (w == 0) {
            get(1); get(2); get(3);
            char* dst = ws + WS_KVA + (size_t)kvrt * 4096 + kvt * 2048;
            gstore16_sc1<0>(lo16, dst, pack8(kvacc, 0));
            gstore16_sc1<1024>(lo16, dst, pack8(kvacc, 8));
          }
        } else if (it == 6) { if (tid == 0) flag_set(ws + WS_FKV + (kvrt * 2 + kvt) * 8, tag); }
      }
    };
    if (hasB && ct >= 64) proj_loop<true>(dma, lds, acc, tail, peek, check, hook PT_PASS);
    else proj_loop<false>(dma, lds, acc, tail, peek, check, hook PT_PASS);
    STAMP(2);
    if (!hasB || ct < 32) {
#pragma unroll
      for (int i = 0; i < 2; ++i)
#pragma unroll
        for (int j = 0; j < 2; ++j)
#pragma unroll
          for (int e = 0; e < 16; ++e) acc[i][j][e] *= QSCALE;
    }
#pragma unroll
    for (int i = 0; i < 2; ++i)
#pragma unroll
      for (int j = 0; j < 2; ++j) {
        held[(i * 2 + j) * 2] = pack8(acc[i][j], 0);
        held[(i * 2 + j) * 2 + 1] = pack8(acc[i][j], 8);
      }
  }
  bf16x8 qb[2], q[2][2];
  typedef __attribute__((address_space(3))) bf16x8* lds_v8;
  const unsigned hstep = ct < 32 ? 2048 : 4096;
  char* hbase = ws + (ct < 32 ? WS_QB : WS_KVB + (ct >= 64 ? 2048 : 0)) + (size_t)((ct & 31) * 64 + pmb * 4 + pwm * 2) * hstep;
  auto storeH = [&](int n) {
    if (hasB) {
      char* dst = hbase + (size_t)((n >> 2) + ((n >> 1) & 1) * 64) * hstep;
      if (n & 1) gstore16_sc1<1024>(lo16, dst, held[n]);
      else gstore16_sc1<0>(lo16, dst, held[n]);
    }
  };
  if (!hasB) {
#pragma unroll
    for (int n = 0; n < 8; ++n)
      *(lds_v8)((lds_ptr)lds + QX_OFF + (((pwm * 2 + (n >> 2)) * 2 + ((n >> 1) & 1)) * 2 + (n & 1)) * 1024 + lo16) = held[n];
  }
  asm volatile("s_waitcnt vmcnt(4) lgkmcnt(0)\n\ts_barrier" ::: "memory");
#pragma unroll
  for (int u = 0; u < 2; ++u)
#pragma unroll
    for (int s2 = 0; s2 < 2; ++s2) q[u][s2] = lds_rd128((lds_cptr)lds + QX_OFF + ((pr * 2 + u) * 2 + s2) * 1024 + lo16);
  STAMP(3);

  const int qt = pmb * 4 + pr, hA = 2 * pj;
  const int hB = (b & 7) + 8 * (b >> 6), rb = (b >> 3) & 7;
  const int qtB = rb * 8 + w;
  const int mb = (xcd & 3) * 4 + (idx & 3), nb = (xcd >> 2) * 8 + (idx >> 2);
  const char* kvb = ws + WS_KVB + (size_t)hB * 64 * 4096;
  bf16x8 ones;
  {
    const short v = ((lane & 3) == 0 && (((lane >> 4) & 1) == (((lane & 15) >> 2) & 1))) ? (short)0x3f80 : (short)0;
    ones = (bf16x8){v, v, v, v, v, v, v, v};
  }
  auto pieceB = [&](int n) {
    int gt = rb * 8 - 8 + 2 * w + (n >> 2);
    gt = gt < 0 ? 0 : gt;
    const char* src = kvb + (size_t)gt * 4096;
    const unsigned dst = lds0 + (2 * w + (n >> 2)) * 4096;
    if ((n & 3) == 0) glds16_sc1<0>(lo16, src, dst);
    else if ((n & 3) == 1) glds16_sc1<1024>(lo16, src, dst);
    else if ((n & 3) == 2) glds16_sc1<2048>(lo16, src, dst);
    else glds16_sc1<3072>(lo16, src, dst);
  };
  const char* pqb = ws + WS_QB + (size_t)(hB * 64 + qtB) * 2048;
  unsigned wo[2];
#pragma unroll
  for (int e = 0; e < 2; ++e) {
    const int row = 16 * w + 8 * e + (lane >> 3);
    const int c = (lane & 7) ^ (((row >> 1) & 1) << 2);
    wo[e] = row * 2048 + c * 16;
  }
  auto pieceW = [&](int st, int e) {
    const char* src = (st < 8 ? wob : woa) + (size_t)(st & 7) * 128 * 2048 + nb * 128;
    glds16<0>(wo[e], src, lds0 + WO_OFF + (st % 6) * 16384 + w * 2048 + e * 1024);
  };
#define NOP (void)0
  u32x2 seen;

  const f32x16 zero = {};
  f32x16 o0 = {}, o1 = {}, s0, s1;
  f32x4 l0 = {}, l1 = {};
  u32x4 p0a, p0b, p1a, p1b;
  bf16x8 ka0, ka1, kb0, kb1, va0, va1, vb0, vb1;
  lds_cptr kvbase = (lds_cptr)lds + half * 16384 + lane * 16;
#define APAIR(pp, SL0, SL1, FIRST, LAST, PA, PB, MID, D0, D1, D2, D3) do { \
    lds_cptr cur = kvbase + (SL0) * SL + ((pp) & 1) * 8192; PIN(cur);     \
    lds_cptr nxt = (SL0) == (SL1) && !((pp) & 1) ? cur + 8192 : kvbase + (SL1) * SL; \
    __builtin_amdgcn_s_setprio(PA); \
    if (FIRST) { ka0 = lds_rd128(cur); ka1 = lds_rd128(cur + 1024); s0 = MFMA32(ka0, q[0][0], zero); s0 = MFMA32(ka1, q[0][1], s0); } \
    SBAR(); \
    APHASE(1, !(FIRST), s0, p0a, p0b, s1, zero, p1a, p1b, q[1][0], q[1][1], o1, l1, ka0, ka1, vb0, vb1, \
           (kb0 = lds_rd128(cur + 4096), kb1 = lds_rd128(cur + 5120)), (va0 = lds_rd128(cur + 2048), va1 = lds_rd128(cur + 3072))); \
    D0; SBAR(); \
    APHASE(1, 1, s1, p1a, p1b, s0, zero, p0a, p0b, q[0][0], q[0][1], o0, l0, kb0, kb1, va0, va1, (void)0, (void)0); \
    D1; SBAR(); \
    ATRACE(2 * (pp)); \
    MID; \
    ATRACE(2 * (pp) + 1); \
    __builtin_amdgcn_s_setprio(PB); SBAR(); \
    APHASE(1, 1, s0, p0a, p0b, s1, zero, p1a, p1b, q[1][0], q[1][1], o1, l1, kb0, kb1, va0, va1, \
           if (!(LAST)) (ka0 = lds_rd128(nxt), ka1 = lds_rd128(nxt + 1024)), (vb0 = lds_rd128(cur + 4096 + 2048), vb1 = lds_rd128(cur + 4096 + 3072))); \
    D2; SBAR(); \
    APHASE(!(LAST), 1, s1, p1a, p1b, s0, zero, p0a, p0b, q[0][0], q[0][1], o0, l0, ka0, ka1, vb0, vb1, (void)0, (void)0); \
    D3; SBAR(); \
    if (LAST) { \
      o1 = MFMA32(vb0, ABC(p1a), o1); o1 = MFMA32(vb1, ABC(p1b), o1); \
      l1 = MFMA16(ones, ABC(p1a), l1); l1 = MFMA16(ones, ABC(p1b), l1); \
    } \
  } while (0)
#define ATRACE(i) do {} while (0)
#define DA(k, sl, i) pieceA(k, sl, i)
#define DA2(k, sl, i) (pieceA(k, sl, i), pieceA(k, sl, (i) + 1))
#define DB2(n) (pieceB(n), pieceB((n) + 1))
#define DW(st, e) pieceW(st, e)
#define BAR_V(n) asm volatile("s_waitcnt vmcnt(" #n ")\n\ts_barrier" ::: "memory")
  STAMP(4); STAMP(5);
  const int fbm = 2 * rb - 2 + (lane & 3);
  const unsigned fbo = ((fbm < 0 ? 2 * rb : fbm) * 16 + ((lane >> 2) % 3 * 32 + hB) / 6) * 8;
  seen = (u32x2){~tag[0], 0u};
#define DH4(n) (storeH(n), storeH((n) + 1), storeH((n) + 2), storeH((n) + 3))
  APAIR(0, 2, 2, 1, 0, 2, 1, NOP, DA2(2, 4, 0), DA2(2, 4, 2), DH4(0), DH4(4));
  APAIR(1, 2, 3, 0, 0, 0, 3, { if (hasB) BAR_V(12); else BAR_V(4); }, NOP, NOP, NOP, NOP);
  APAIR(2, 3, 3, 0, 0, 2, 1, NOP, NOP, NOP, NOP, NOP);
  APAIR(3, 3, 4, 0, 0, 0, 3, { BAR_V(0); if (tid == 0) flag_set(ws + WS_FPB + (pmb * 16 + pj) * 8, tag);   },
        NOP, NOP, DA2(3, 2, 0), DA2(3, 2, 2));
  APAIR(4, 4, 4, 0, 0, 2, 1, NOP, NOP, NOP, NOP, NOP);
  flag_peek(seen, fbo, ws + WS_FPB);
  APAIR(5, 4, 2, 0, 0, 0, 3, { asm volatile("s_waitcnt vmcnt(0)" : "+v"(seen)::"memory"); if (w == 0) flag_wait(seen, fbo, ws + WS_FPB, tag); asm volatile("s_barrier" ::: "memory"); },
        NOP, NOP, DB2(0), DB2(2));
  APAIR(6, 2, 2, 0, 0, 2, 1, NOP, DB2(4), DB2(6), (gload16_sc1<0>(qb[0], lo16, pqb), gload16_sc1<1024>(qb[1], lo16, pqb)), DA2(4, 3, 0));
  APAIR(7, 2, 2, 0, 1, 1, 0, NOP, DA2(4, 3, 2), NOP, NOP, NOP);
  asm volatile("s_waitcnt vmcnt(0) lgkmcnt(0)\n\ts_barrier" : "+v"(qb[0]), "+v"(qb[1])::"memory");
  STAMP(6);
  {
    f32x16 o = {};
    f32x4 la = {};
    lds_cptr bb = (lds_cptr)lds + w * 4096 + lane * 16;
    f32x16 mD, sa;
    diag_masks<0>(mD, sa, -3.0e38f, 0.f);
    if (rb != 0) {
      f32x16 sb;
      u32x4 pa0 = {}, pa1 = {}, pb0 = {}, pb1 = {};
      bf16x8 k0, k1, k2, k3, v0, v1, v2, v3;
      k0 = lds_rd128(bb); k1 = lds_rd128(bb + 1024);
      k2 = lds_rd128(bb + 4096); k3 = lds_rd128(bb + 4096 + 1024);
      sa = MFMA32(k0, qb[0], sa); sa = MFMA32(k1, qb[1], sa);
      SBAR();
#define BRDK(KA, KB, j) (KA = lds_rd128(bb + (j) * 4096), KB = lds_rd128(bb + (j) * 4096 + 1024))
#define BRDV(VA, VB, j) (VA = lds_rd128(bb + (j) * 4096 + 2048), VB = lds_rd128(bb + (j) * 4096 + 3072))
#define B_DMA(n) do { pieceA(5, 4, n); SBAR(); } while (0)
      __builtin_amdgcn_s_setprio(3);
      APHASE(1, 0, sa, pa0, pa1, sb, zero, pb0, pb1, qb[0], qb[1], o, la, k2, k3, v2, v3, BRDK(k0, k1, 2), BRDV(v0, v1, 0));
      B_DMA(0); B_DMA(1);
      APHASE(1, 1, sb, pb0, pb1, sa, zero, pa0, pa1, qb[0], qb[1], o, la, k0, k1, v0, v1, BRDK(k2, k3, 3), BRDV(v2, v3, 1));
      B_DMA(2); B_DMA(3);
      __builtin_amdgcn_s_setprio(2);
      APHASE(1, 1, sa, pa0, pa1, sb, zero, pb0, pb1, qb[0], qb[1], o, la, k2, k3, v2, v3, BRDK(k0, k1, 4), BRDV(v0, v1, 2));
      APHASE(1, 1, sb, pb0, pb1, sa, zero, pa0, pa1, qb[0], qb[1], o, la, k0, k1, v0, v1, BRDK(k2, k3, 5), BRDV(v2, v3, 3));
      __builtin_amdgcn_s_setprio(1);
      APHASE(1, 1, sa, pa0, pa1, sb, zero, pb0, pb1, qb[0], qb[1], o, la, k2, k3, v2, v3, BRDK(k0, k1, 6), BRDV(v0, v1, 4));
      APHASE(1, 1, sb, pb0, pb1, sa, zero, pa0, pa1, qb[0], qb[1], o, la, k0, k1, v0, v1, BRDK(k2, k3, 7), BRDV(v2, v3, 5));
      __builtin_amdgcn_s_setprio(0);
      APHASE(1, 1, sa, pa0, pa1, sb, zero, pb0, pb1, qb[0], qb[1], o, la, k2, k3, v2, v3, BRDK(k0, k1, 8), BRDV(v0, v1, 6));
      APHASE(1, 1, sb, pb0, pb1, sa, mD, pa0, pa1, qb[0], qb[1], o, la, k0, k1, v0, v1, (void)0, BRDV(v2, v3, 7));
      APHASE(0, 1, sa, pa0, pa1, sb, zero, pb0, pb1, qb[0], qb[1], o, la, k2, k3, v2, v3, (void)0, BRDV(v0, v1, 8));
      o = MFMA32(v0, ABC(pa0), o); o = MFMA32(v1, ABC(pa1), o);
      la = MFMA16(ones, ABC(pa0), la); la = MFMA16(ones, ABC(pa1), la);
    } else {
#pragma unroll
      for (int n = 0; n < 4; ++n) B_DMA(n);
      auto tile = [&](int j, const f32x16& cin) {
        lds_cptr p = bb + j * 4096;
        bf16x8 k0 = lds_rd128(p), k1 = lds_rd128(p + 1024), v0 = lds_rd128(p + 2048), v1 = lds_rd128(p + 3072);
        f32x16 s = MFMA32(k0, qb[0], cin);
        s = MFMA32(k1, qb[1], s);
        exp16(s);
        bf16x8 pa = pack8(s, 0), pb = pack8(s, 8);
        o = MFMA32(v0, pa, o); o = MFMA32(v1, pb, o);
        la = MFMA16(ones, pa, la); la = MFMA16(ones, pb, la);
      };
      for (int j = 8 - w; j < 8; ++j) tile(j, zero);
      tile(8, mD);
    }
    float l = la[0];
    if (__builtin_expect(ref_unsafe(o, l), 0)) attn_exact<true>(kvb, qtB >= 8 ? qtB - 8 : 0, qtB + 1, qtB, qb[0], qb[1], ones, o, l);
    const float sc = __builtin_amdgcn_rcpf(l);
    char* dst = ws + WS_O + (size_t)(qtB * 64 + 32 + hB) * 2048;
    gstore16_sc1<0>(lo16, dst, pack8s(o, 0, sc));
    gstore16_sc1<1024>(lo16, dst, pack8s(o, 8, sc));
  }
  asm volatile("s_waitcnt vmcnt(2) lgkmcnt(0)\n\ts_barrier" ::: "memory");
  STAMP(7);
  const int wm = w & 3, kh = w >> 2;
  const char* obase = ws + WS_O + (size_t)(mb * 4 + wm) * 64 * 2048 + kh * 4096;
  bf16x8 of[5][4];
  auto loadO1 = [&](int st, int i, bf16x8& f) {
    const char* src = obase + (size_t)(st < 8 ? 32 + 4 * st : 4 * (st - 8)) * 2048;
    if (i == 0) gload16_sc1<0>(f, lo16, src);
    else if (i == 1) gload16_sc1<1024>(f, lo16, src);
    else if (i == 2) gload16_sc1<2048>(f, lo16, src);
    else gload16_sc1<3072>(f, lo16, src);
  };
#define LO(st, i) loadO1(st, i, of[st][i])
  auto loadO = [&](int st, bf16x8 (&f)[4]) {
    const char* src = obase + (size_t)(st < 8 ? 32 + 4 * st : 4 * (st - 8)) * 2048;
    gload16_sc1<0>(f[0], lo16, src); gload16_sc1<1024>(f[1], lo16, src);
    gload16_sc1<2048>(f[2], lo16, src); gload16_sc1<3072>(f[3], lo16, src);
  };
  const unsigned fob = ((mb >> 1) * 32 + (lane & 31)) * 8;
  seen = (u32x2){~tag[0], 0u};
  unsigned warm = 0;
  APAIR(8, 3, 3, 1, 0, 3, 3, NOP, DA(6, 0, 0), DA(6, 0, 1), DA(6, 0, 2), DA(6, 0, 3));
  APAIR(9, 3, 4, 0, 0, 2, 2, NOP, DA(7, 1, 0), DA(7, 1, 1), DA(7, 1, 2), DA(7, 1, 3));
  {
    const int R = 768 + (idx & 3) * 320 + w * 40 + (lane < 40 ? lane : 39);
    const char* pw = (R < 1024 ? wob + (size_t)R * 2048 : woa + (size_t)(R - 1024) * 2048) + nb * 128;
    asm volatile("global_load_dword %0, %1, off" : "+v"(warm) : "v"(pw) : "memory");
  }
  APAIR(10, 4, 4, 0, 0, 1, 0, NOP, NOP, NOP, NOP, NOP);
  APAIR(11, 4, 0, 0, 0, 0, 3, { asm volatile("s_waitcnt vmcnt(0)\n\ts_barrier" : "+v"(warm)::"memory");
                                if (tid == 0) flag_set(ws + WS_FOB + (rb * 32 + hB) * 8, tag);   }, NOP, NOP, NOP, NOP);
  APAIR(12, 0, 0, 0, 0, 3, 2, NOP, DW(0, 0), DW(0, 1), DW(1, 0), DW(1, 1));
  flag_peek(seen, fob, ws + WS_FOB);
  APAIR(13, 0, 1, 0, 0, 2, 1, NOP, DW(2, 0), DW(2, 1), DW(3, 0), DW(3, 1));
  APAIR(14, 1, 1, 0, 0, 0, 3, { asm volatile("s_waitcnt vmcnt(0)" : "+v"(seen)::"memory"); if (w == 0) flag_wait(seen, fob, ws + WS_FOB, tag); asm volatile("s_barrier" ::: "memory"); },
        NOP, NOP, (LO(0, 0), LO(0, 1), DW(4, 0)), (LO(0, 2), LO(0, 3), DW(4, 1)));
  APAIR(15, 1, 1, 0, 1, 2, 1, NOP, (LO(1, 0), LO(1, 1), DW(5, 0)), (LO(1, 2), LO(1, 3), DW(5, 1)), (LO(2, 0), LO(2, 1)), (LO(2, 2), LO(2, 3)));
  STAMP(8);

  loadO(3, of[3]);
  asm volatile("s_waitcnt lgkmcnt(0)\n\ts_barrier" ::: "memory");
  {
    float* xb = (float*)(lds + XCH_OFF) + w * (5 * 256);
    const f32x16& os = half ? o0 : o1;
    const float ls = half ? l0[0] : l1[0];
#pragma unroll
    for (int g = 0; g < 4; ++g) *(f32x4*)(xb + g * 256 + lane * 4) = (f32x4){os[4 * g], os[4 * g + 1], os[4 * g + 2], os[4 * g + 3]};
    xb[4 * 256 + lane] = ls;
  }
  f32x16 oA = half ? o1 : o0;
  float lA = half ? l1[0] : l0[0];
  auto finishA = [&]() {
    const float* xb = (const float*)(lds + XCH_OFF) + (w ^ 4) * (5 * 256);
#pragma unroll
    for (int g = 0; g < 4; ++g) {
      f32x4 v = *(const f32x4*)(xb + g * 256 + lane * 4);
#pragma unroll
      for (int e = 0; e < 4; ++e) oA[4 * g + e] += v[e];
    }
    lA += xb[4 * 256 + lane];
    if (__builtin_expect(ref_unsafe(oA, lA), 0)) {
      if (half) attn_exact<false>(kva, 0, 64, 0, q[1][0], q[1][1], ones, oA, lA);
      else attn_exact<false>(kva, 0, 64, 0, q[0][0], q[0][1], ones, oA, lA);
    }
    const float sc = __builtin_amdgcn_rcpf(lA);
    char* dst = ws + WS_O + (size_t)(qt * 64 + hA + half) * 2048;
    gstore16_sc1<0>(lo16, dst, pack8s(oA, 0, sc));
    gstore16_sc1<1024>(lo16, dst, pack8s(oA, 8, sc));
  };
  STAMP(9);
  {
    const int g16 = lane >> 4, q4 = (lane & 15) >> 2, p4 = lane & 3;
    const int wc = 2 * (g16 & 1) + (p4 >> 1);
    const int wa0 = (kh * 64 + 4 * h + q4) * 128 + ((wc ^ (((q4 >> 1) & 1) << 2)) << 4) + 8 * (p4 & 1);
    lds_cptr wbase[2][2] = {{(lds_cptr)lds + WO_OFF + wa0, (lds_cptr)lds + WO_OFF + (wa0 ^ 64)},
                            {(lds_cptr)lds + WO_OFF + 49152 + wa0, (lds_cptr)lds + WO_OFF + 49152 + (wa0 ^ 64)}};
    PIN(wbase[0][0]); PIN(wbase[0][1]); PIN(wbase[1][0]); PIN(wbase[1][1]);
    f32x16 accB[2] = {}, accA[2] = {};
    bf16x8 wf[4][2];
    const unsigned foa = (mb * 16 + (lane & 15)) * 8;
    u32x2 seenA = {~tag[0], 0u};
    float g = 0.f;
    auto rdw = [&](bf16x8 (&f)[2], int st, int ks) {
#pragma unroll
      for (int j = 0; j < 2; ++j) {
        lds_cptr p = wbase[(st % 6) / 3][j] + ((st % 6) % 3) * 16384 + ks * 2048;
        f[j] = cat8(lds_tr(p), lds_tr(p + 1024));
      }
    };
    asm volatile("s_waitcnt vmcnt(15)" : "+v"(of[0][0]), "+v"(of[0][1]), "+v"(of[0][2]), "+v"(of[0][3])::"memory");
    rdw(wf[0], 0, 0); rdw(wf[1], 0, 1);
#define OTRACE(i) do {} while (0)
#define O_MM(ACC, f, a) do { ACC[0] = MFMA32(f[0], a, ACC[0]); SBAR(); ACC[1] = MFMA32(f[1], a, ACC[1]); SBAR(); } while (0)
#define O_STAGE(st, ACC, VM, PRE, POST) do { \
      rdw(wf[2], st, 2); SBAR(); O_MM(ACC, wf[0], of[(st) % 5][0]); \
      rdw(wf[3], st, 3); SBAR(); O_MM(ACC, wf[1], of[(st) % 5][1]); \
      if ((st) < 15) { \
        OTRACE(3 * (st)); \
        if ((st) == 0) asm volatile("s_waitcnt lgkmcnt(0)" ::: "memory");     \
        asm volatile("s_waitcnt vmcnt(" #VM ")" : "+v"(of[((st) + 1) % 5][0]), "+v"(of[((st) + 1) % 5][1]), \
                     "+v"(of[((st) + 1) % 5][2]), "+v"(of[((st) + 1) % 5][3]), "+v"(seenA)::"memory"); \
        PRE; \
        OTRACE(3 * (st) + 1); \
        if ((st) < 13) asm volatile("s_barrier" ::: "memory");     \
        OTRACE(3 * (st) + 2); \
        POST; \
        SBAR(); \
        rdw(wf[0], (st) + 1, 0); \
      } \
      SBAR(); O_MM(ACC, wf[2], of[(st) % 5][2]); \
      if ((st) < 15) rdw(wf[1], (st) + 1, 1); \
      SBAR(); O_MM(ACC, wf[3], of[(st) % 5][3]); \
    } while (0)
#define OW(st) (pieceW(st, 0), pieceW(st, 1))
#define OL(st) loadO(st, of[(st) % 5])
    O_STAGE(0, accB, 9, NOP, { OL(4); SBAR(); finishA(); });
    O_STAGE(1, accB, 10, NOP, (OW(6), OL(5)));
    O_STAGE(2, accB, 6, NOP, { if (tid == 0) flag_set(ws + WS_FOA + (pmb * 16 + pj) * 8, tag);   OW(7); OL(6); });
    O_STAGE(3, accB, 12, NOP, (OW(8), OL(7)));
    O_STAGE(4, accB, 12, NOP, (flag_peek(seenA, foa, ws + WS_FOA), OW(9)));
    O_STAGE(5, accB, 2, if (w == 0) flag_wait(seenA, foa, ws + WS_FOA, tag),
            (gload4_sc1(g, (wm * 32 + r) * 4, ws + WS_G + mb * 512), OW(10), OL(8)));
    O_STAGE(6, accB, 9, NOP, (OW(11), OL(9)));
    O_STAGE(7, accB, 6, NOP, (OW(12), OL(10)));
    O_STAGE(8, accA, 6, NOP, (OW(13), OL(11)));
    O_STAGE(9, accA, 6, NOP, (OW(14), OL(12)));
    O_STAGE(10, accA, 6, NOP, (OW(15), OL(13)));
    O_STAGE(11, accA, 6, NOP, OL(14));
    O_STAGE(12, accA, 4, NOP, OL(15));
    O_STAGE(13, accA, 4, NOP, NOP);
    O_STAGE(14, accA, 0, NOP, NOP);
    O_STAGE(15, accA, 0, NOP, NOP);
    asm volatile("" : "+v"(g));
    STAMP(10);
    float* xw = (float*)(lds + XCH_OFF) + w * 1024 + lane * 4;
    f32x16 acc[2];
#pragma unroll
    for (int j = 0; j < 2; ++j)
#pragma unroll
      for (int i = 0; i < 16; ++i) acc[j][i] = g * accA[j][i] + (1.f - g) * accB[j][i];
    const f32x16& as = kh ? acc[0] : acc[1];
#pragma unroll
    for (int g = 0; g < 4; ++g) *(f32x4*)(xw + g * 256) = (f32x4){as[4 * g], as[4 * g + 1], as[4 * g + 2], as[4 * g + 3]};
    asm volatile("s_waitcnt lgkmcnt(0)\n\ts_barrier" ::: "memory");
    f32x16 t = kh ? acc[1] : acc[0];
    const float* xr = (const float*)(lds + XCH_OFF) + (w ^ 4) * 1024 + lane * 4;
#pragma unroll
    for (int g = 0; g < 4; ++g) {
      f32x4 v = *(const f32x4*)(xr + g * 256);
#pragma unroll
      for (int e = 0; e < 4; ++e) t[4 * g + e] += v[e];
    }
    const int row = mb * 128 + wm * 32 + r;
    char* dst = out + (size_t)row * 2048 + (nb * 64 + kh * 32) * 2;
#pragma unroll
    for (int g = 0; g < 4; g += 2) {
      u32x2 a = {cvtpk(t[4 * g], t[4 * g + 1]), cvtpk(t[4 * g + 2], t[4 * g + 3])};
      u32x2 c = {cvtpk(t[4 * g + 4], t[4 * g + 5]), cvtpk(t[4 * g + 6], t[4 * g + 7])};
      auto x = __builtin_amdgcn_permlane32_swap(a[0], c[0], false, false);
      auto y = __builtin_amdgcn_permlane32_swap(a[1], c[1], false, false);
      *(u32x4*)(dst + g * 16 + h * 16) = (u32x4){x[0], y[0], x[1], y[1]};
    }
  }
  if (b == 0 && w == 0) {
    const unsigned long long want = ((unsigned long long)tag[1] << 32) | tag[0];
    gu64* f = (gu64*)(ws + WS_FOA) + lane;
    for (unsigned spins = 0; spins < (1u << 21); ++spins) {
      const unsigned long long a0 = __hip_atomic_load(f, __ATOMIC_RELAXED, __HIP_MEMORY_SCOPE_AGENT),
                               a1 = __hip_atomic_load(f + 64, __ATOMIC_RELAXED, __HIP_MEMORY_SCOPE_AGENT),
                               a2 = __hip_atomic_load(f + 128, __ATOMIC_RELAXED, __HIP_MEMORY_SCOPE_AGENT),
                               a3 = __hip_atomic_load(f + 192, __ATOMIC_RELAXED, __HIP_MEMORY_SCOPE_AGENT);
      if (__all(a0 == want && a1 == want && a2 == want && a3 == want)) break;
      __builtin_amdgcn_s_sleep(4);
    }
    if (lane == 0) flag_set(ws + WS_NONCE, tag);
  }
  STAMP(11);
  STAMP_OUT(0, ws); TRACE_OUT(ws);
}

extern "C" void kernel_launch(void* const* d_in, const int* in_sizes, int n_in, void* d_out, int out_size, void* d_ws,
                              size_t ws_size, hipStream_t stream) {
  const char* xa = (const char*)d_in[0];
  const char* wq = (const char*)d_in[1];
  const char* wk = (const char*)d_in[2];
  const char* wv = (const char*)d_in[3];
  const char* woa = (const char*)d_in[4];
  const char* wqkv = (const char*)d_in[6];
  const char* wob = (const char*)d_in[7];
  const char* gw = (const char*)d_in[8];
  (void)hipFuncSetAttribute((const void*)k_fused, hipFuncAttributeMaxDynamicSharedMemorySize, R_LDS);
  k_fused<<<256, 512, R_LDS, stream>>>(xa, wq, wk, wv, woa, wqkv, wob, gw, (char*)d_ws, (char*)d_out);
}
```

```cpp
#include <hip/hip_runtime.h>
#include <hip/hip_bf16.h>
#include <stdint.h>

typedef float f32x16 __attribute__((ext_vector_type(16)));
typedef float f32x4 __attribute__((ext_vector_type(4)));
typedef float f32x2 __attribute__((ext_vector_type(2)));
typedef short bf16x8 __attribute__((ext_vector_type(8)));
typedef short s16x4 __attribute__((ext_vector_type(4)));
typedef __bf16 bf16x2 __attribute__((ext_vector_type(2)));
typedef unsigned u32x4 __attribute__((ext_vector_type(4)));
typedef unsigned u32x2 __attribute__((ext_vector_type(2)));
typedef __attribute__((address_space(3))) const char* lds_cptr;
typedef __attribute__((address_space(3))) char* lds_ptr;

#define DI __device__ __forceinline__
#define SBAR() __builtin_amdgcn_sched_barrier(0)
#define PIN(x) asm volatile("" : "+v"(x))
#define MFMA32(a, b, c) __builtin_amdgcn_mfma_f32_32x32x16_bf16(a, b, c, 0, 0, 0)
#define MFMA16(a, b, c) __builtin_amdgcn_mfma_f32_16x16x32_bf16(a, b, c, 0, 0, 0)

constexpr int SEQ = 2048, DM = 1024;
constexpr float QSCALE = 0.17677669529663687f * 1.4426950408889634f;

constexpr size_t WS_KVA = 4u << 20;
constexpr size_t WS_G = WS_KVA + (256u << 10);
constexpr size_t WS_NONCE = WS_G + 8192;
constexpr size_t WS_FKV = WS_NONCE + 1024;
constexpr size_t WS_FPB = WS_FKV + 1024;
constexpr size_t WS_FOA = WS_FPB + 2048;
constexpr size_t WS_FOB = WS_FOA + 2048;
constexpr size_t WS_QB = 5u << 20;
constexpr size_t WS_KVB = 9u << 20;
constexpr size_t WS_O = 17u << 20;

DI unsigned rfl(unsigned x) { return __builtin_amdgcn_readfirstlane(x); }
DI unsigned cvtpk(float lo, float hi) {
  f32x2 v = {lo, hi};
  bf16x2 b = __builtin_convertvector(v, bf16x2);
  return __builtin_bit_cast(unsigned, b);
}
DI bf16x8 pack8(const f32x16& a, int o) {
  u32x4 r = {cvtpk(a[o], a[o + 1]), cvtpk(a[o + 2], a[o + 3]), cvtpk(a[o + 4], a[o + 5]), cvtpk(a[o + 6], a[o + 7])};
  return __builtin_bit_cast(bf16x8, r);
}
DI bf16x8 pack8s(const f32x16& a, int o, float s) {
  u32x4 r = {cvtpk(a[o] * s, a[o + 1] * s), cvtpk(a[o + 2] * s, a[o + 3] * s), cvtpk(a[o + 4] * s, a[o + 5] * s),
             cvtpk(a[o + 6] * s, a[o + 7] * s)};
  return __builtin_bit_cast(bf16x8, r);
}
template <int OFF>
DI void glds16(unsigned voff, const void* sbase, unsigned lds_base) {
  asm volatile("s_mov_b32 m0, %2\n\ts_nop 0\n\tglobal_load_lds_dwordx4 %0, %1 offset:%3"
               :: "v"(voff), "s"(sbase), "s"(rfl(lds_base)), "i"(OFF) : "memory");
}
template <int OFF>
DI void glds16_sc1(unsigned voff, const void* sbase, unsigned lds_base) {
  asm volatile("s_mov_b32 m0, %2\n\ts_nop 0\n\tglobal_load_lds_dwordx4 %0, %1 offset:%3 sc1"
               :: "v"(voff), "s"(sbase), "s"(rfl(lds_base)), "i"(OFF) : "memory");
}
DI void glds16v(const char* vaddr, unsigned lds_base) {
  asm volatile("s_mov_b32 m0, %1\n\ts_nop 0\n\tglobal_load_lds_dwordx4 %0, off" :: "v"(vaddr), "s"(rfl(lds_base)) : "memory");
}
DI bf16x8 lds_rd128(lds_cptr p) { return *(const __attribute__((address_space(3))) bf16x8*)p; }
DI s16x4 lds_tr(lds_cptr p) {
  typedef short v4i16_t __attribute__((ext_vector_type(4)));
  return __builtin_bit_cast(s16x4, __builtin_amdgcn_ds_read_tr16_b64_v4i16((__attribute__((address_space(3))) v4i16_t*)p));
}
DI bf16x8 cat8(s16x4 lo, s16x4 hi) { return (bf16x8){lo[0], lo[1], lo[2], lo[3], hi[0], hi[1], hi[2], hi[3]}; }
DI float bf2f(unsigned short b) { return __uint_as_float((unsigned)b << 16); }

template <int OFF>
DI void gload16_sc1(bf16x8& d, unsigned voff, const void* sbase) {
  asm volatile("global_load_dwordx4 %0, %1, %2 offset:%3 sc1" : "=v"(d) : "v"(voff), "s"(sbase), "i"(OFF) : "memory");
}
template <int OFF>
DI void gstore16_sc1(unsigned voff, void* sbase, const bf16x8& d) {
  asm volatile("global_store_dwordx4 %0, %1, %2 offset:%3 sc1\n\ts_nop 1" :: "v"(voff), "v"(d), "s"(sbase), "i"(OFF) : "memory");
}
DI void gload4_sc1(float& d, unsigned voff, const void* sbase) {
  asm volatile("global_load_dword %0, %1, %2 sc1" : "=v"(d) : "v"(voff), "s"(sbase) : "memory");
}
DI bf16x8 gload16_now(const char* p) {
  bf16x8 d;
  asm volatile("global_load_dwordx4 %0, %1, off sc1\n\ts_waitcnt vmcnt(0)" : "=&v"(d) : "v"(p) : "memory");
  return d;
}
typedef __attribute__((address_space(1))) unsigned long long gu64;
DI void flag_set(char* p, u32x2 tag) {
  asm volatile("global_store_dwordx2 %0, %1, %2 sc1\n\ts_nop 1" :: "v"(0u), "v"(tag), "s"(p) : "memory");
}
DI void flag_peek(u32x2& d, unsigned voff, const void* sbase) {
  asm volatile("global_load_dwordx2 %0, %1, %2 sc1" : "+v"(d) : "v"(voff), "s"(sbase) : "memory");
}
DI void flag_peek2(u32x4& d, unsigned voff, const void* sbase) {
  asm volatile("global_load_dwordx4 %0, %1, %2 sc1" : "+v"(d) : "v"(voff), "s"(sbase) : "memory");
}
DI void flag_wait(u32x2 seen, unsigned voff, const char* sbase, u32x2 tag) {
  if (__builtin_expect(__all(seen[0] == tag[0] && seen[1] == tag[1]), 1)) return;
  const unsigned long long want = ((unsigned long long)tag[1] << 32) | tag[0];
  for (unsigned spins = 0; spins < (1u << 21); ++spins) {
    if (__all(__hip_atomic_load((gu64*)(sbase + voff), __ATOMIC_RELAXED, __HIP_MEMORY_SCOPE_AGENT) == want)) return;
    __builtin_amdgcn_s_sleep(4);
  }
}

DI float dot2bf(unsigned a, unsigned b, float c) {
  return __builtin_amdgcn_fdot2_f32_bf16(__builtin_bit_cast(bf16x2, a), __builtin_bit_cast(bf16x2, b), c, false);
}
DI float wave_sum63(float z) {
#define DPP_ADD(ctrl, rmask) z += __uint_as_float(__builtin_amdgcn_update_dpp(0u, __float_as_uint(z), ctrl, rmask, 0xf, true))
  DPP_ADD(0x111, 0xf); DPP_ADD(0x112, 0xf); DPP_ADD(0x114, 0xf); DPP_ADD(0x118, 0xf);
  DPP_ADD(0x142, 0xa); DPP_ADD(0x143, 0xc);
#undef DPP_ADD
  return z;
}

DI void nonce_load(u32x2& nonce, const char* ws) {
  asm volatile("global_load_dwordx2 %0, %1, %2 sc1" : "=v"(nonce) : "v"(0u), "s"(ws + WS_NONCE) : "memory");
}
DI u32x2 nonce_tag(u32x2 nonce) {
  u32x2 tag;
  tag[0] = nonce[0] + 1u;
  tag[1] = nonce[1] + (tag[0] == 0u ? 1u : 0u);
  return tag;
}

constexpr int P_SLOT = 49152;
constexpr int P_XOFF = 0, P_WOFF = 16384;
constexpr int QX_OFF = 0;
constexpr int KVC = 147456;
struct PDma {
  const char* xsrc; const char* wp[4]; unsigned xo[2], wstep[2], lds0; int w;
  DI void init_uniform(const char* xa, int mb, unsigned lds0_, int w_) {
    lds0 = lds0_; w = w_;
    xsrc = xa + (size_t)mb * 128 * 2048;
  }
  DI void init_lanes(const char* wq, const char* wqkv, int j, int lane) {
#pragma unroll
    for (int e = 0; e < 2; ++e) {
      const int row = 16 * w + 8 * e + (lane >> 3);
      xo[e] = row * 2048 + (((lane & 7) ^ (4 * e + (lane >> 4))) << 4);
    }
#pragma unroll
    for (int e = 0; e < 4; ++e) {
      const int krow = 8 * w + 2 * e + (lane >> 5);
      const int c = (lane & 31) ^ ((krow & 3) << 2);
      wp[e] = c < 24 ? wqkv + (size_t)krow * 6144 + j * 384 + c * 16 : wq + (size_t)krow * 2048 + j * 128 + (c - 24) * 16;
      if (e < 2) wstep[e] = c < 24 ? 64 * 6144 : 64 * 2048;
    }
  }
  DI void piece(int pc, int st, int slot) {
    const unsigned lb = lds0 + slot * P_SLOT;
    if (pc < 2) glds16<0>(xo[pc], xsrc + st * 128, lb + P_XOFF + w * 2048 + pc * 1024);
    else { glds16v(wp[pc - 2], lb + P_WOFF + w * 4096 + (pc - 2) * 1024); wp[pc - 2] += wstep[pc & 1]; }
  }
  DI void stage(int st, int slot) {
#pragma unroll
    for (int pc = 0; pc < 6; ++pc) piece(pc, st, slot);
  }
};
struct PFrag { bf16x8 x[2]; bf16x8 w[2]; };

template <bool VT, class Tail, class Peek, class Check, class Hook>
DI void proj_loop(PDma& dma, char* lds, f32x16 (&acc)[2][2], Tail tail, Peek peek, Check check, Hook hook) {
  const int tid = threadIdx.x, lane = tid & 63;
  const int w = dma.w;
  const int wm = w & 1, wn = w >> 1;
  const int r = lane & 31, h = lane >> 5;
  const int t3 = (r >> 1) & 7;
  const int xa0 = P_XOFF + (wm * 64 + r) * 128 + ((h ^ t3) << 4);
  const int g16 = lane >> 4, q4 = (lane & 15) >> 2, p4 = lane & 3;
  const int wc = wn * 8 + 2 * (g16 & 1) + (p4 >> 1);
  const int wa0 = P_WOFF + (8 * h + q4) * 512 + ((wc ^ (q4 << 2)) << 4) + 8 * (p4 & 1);
  auto rdx = [&](PFrag& f, int i, int slot, int s) { f.x[i] = lds_rd128((lds_cptr)lds + slot * P_SLOT + (xa0 ^ (s << 5)) + i * 4096); };
  auto rdw = [&](PFrag& f, int j, int slot, int s) {
    lds_cptr p = (lds_cptr)lds + slot * P_SLOT + (wa0 ^ (j << 6)) + s * 8192;
    f.w[j] = cat8(lds_tr(p), lds_tr(p + 2048));
  };
  auto mm = [&](const PFrag& f, int i, int j) {
    if constexpr (VT) acc[i][j] = MFMA32(f.x[i], f.w[j], acc[i][j]);
    else acc[i][j] = MFMA32(f.w[j], f.x[i], acc[i][j]);
  };
  auto win = [&](int j, int pc, int slot) {
    if (j == -1) dma.piece(pc, 2, 2);
    else if (j <= 12) dma.piece(pc, j + 3, slot);
    else if (j <= 14 && pc < 4) tail((j - 13) * 4 + pc);
  };
  auto step = [&](const PFrag& c, PFrag& n, int rslot, int rs, bool rd, int pa, int wj, int dslot) {
    mm(c, 0, 0); SBAR();
    if (rd) { rdw(n, 0, rslot, rs); rdx(n, 0, rslot, rs); }
    SBAR();
    mm(c, 0, 1); SBAR();
    if (rd) { rdw(n, 1, rslot, rs); rdx(n, 1, rslot, rs); }
    SBAR();
    mm(c, 1, 0); SBAR();
    if (pa >= 0) win(wj, pa, dslot);
    SBAR();
    mm(c, 1, 1); SBAR();
    if (pa >= 0) win(wj, pa + 1, dslot);
    SBAR();
  };
  PFrag f0, f1;
  rdw(f0, 0, 0, 0); rdx(f0, 0, 0, 0); rdw(f0, 1, 0, 0); rdx(f0, 1, 0, 0);
  auto iter = [&](int it, int slot, int pslot, int nslot, bool hk) {
    step(f0, f1, slot, 1, true, 2, it - 1, pslot);
    step(f1, f0, slot, 2, true, 4, it - 1, pslot);
    step(f0, f1, slot, 3, true, -1, 0, 0);
    asm volatile("s_waitcnt vmcnt(6) lgkmcnt(0)\n\ts_barrier" ::: "memory");
    if (hk) hook(it);
    SBAR();
    step(f1, f0, nslot, 0, true, 0, it, slot);
  };
  iter(0, 0, 0, 1, true); iter(1, 1, 0, 2, true); iter(2, 2, 1, 0, true); iter(3, 0, 2, 1, true);
  iter(4, 1, 0, 2, true); iter(5, 2, 1, 0, true); iter(6, 0, 2, 1, true);
  int slot = 1, pslot = 0;
  for (int it = 7; it < 13; ++it) {
    const int nslot = slot == 2 ? 0 : slot + 1;
    iter(it, slot, pslot, nslot, false);
    pslot = slot;
    slot = nslot;
  }
  peek();
  step(f0, f1, 1, 1, true, 2, 12, 0);
  step(f1, f0, 1, 2, true, 4, 12, 0);
  step(f0, f1, 1, 3, true, -1, 0, 0);
  check();
  SBAR();
  step(f1, f0, 2, 0, true, 0, 13, 0);
  step(f0, f1, 2, 1, true, 2, 13, 0);
  step(f1, f0, 2, 2, true, -1, 0, 0);
  step(f0, f1, 2, 3, true, -1, 0, 0);
  asm volatile("s_waitcnt vmcnt(4) lgkmcnt(0)\n\ts_barrier" ::: "memory");
  SBAR();
  step(f1, f0, 0, 0, true, 0, 14, 0);
  step(f0, f1, 0, 1, true, 2, 14, 0);
  step(f1, f0, 0, 2, true, -1, 0, 0);
  step(f0, f1, 0, 3, true, -1, 0, 0);
  asm volatile("s_waitcnt lgkmcnt(0)\n\ts_barrier" ::: "memory");
  SBAR();
  step(f1, f0, 0, 0, false, -1, 0, 0);
}

template <class Init>
DI void pre_kv(const char* __restrict__ xg, const char* __restrict__ wsel, char* __restrict__ ws, int rt, int t, char* lds, int w,
               PDma& dma, Init init, u32x2& tag, f32x16& acc, lds_cptr& xlast, lds_cptr& wlast) {
  const int tid = threadIdx.x, lane = tid & 63;
  const int r = lane & 31, h = lane >> 5;
  const unsigned lds0 = (unsigned)(uintptr_t)lds;
  u32x2 nonce;
  nonce_load(nonce, ws);
  const int xrow = 4 * w + (lane >> 4);
  const unsigned xo = xrow * 2048 + (((lane & 15) ^ (xrow & 15)) << 4), wo = w * 1024 + lane * 16;
  const char* xs = xg + (size_t)rt * 32 * 2048;
#define KV_OFF(u) ((u) == 0 || (u) == 7 ? KVC : P_SLOT + ((u) - 1) * 16384)
  auto sub = [&](int u) {
    const unsigned lb = lds0 + KV_OFF(u) + w * 1024;
    glds16<0>(xo, xs + u * 256, lb);
    glds16<0>(wo, wsel + u * 8192, lb + 8192);
  };
  sub(0); sub(1); sub(2); sub(3); sub(4); sub(5); sub(6);
  SBAR();
  init();
  const int g16 = lane >> 4, q4 = (lane & 15) >> 2, p4 = lane & 3;
  lds_cptr xa = (lds_cptr)lds + P_SLOT + r * 256 + (((2 * w + h) ^ (r & 15)) << 4);
  lds_cptr wa = (lds_cptr)lds + P_SLOT + 8192 + (16 * w + 8 * h + q4) * 64 + 32 * (g16 & 1) + 8 * p4;
  lds_cptr xc = xa + (KVC - P_SLOT), wc = wa + (KVC - P_SLOT);
  PIN(xa); PIN(wa); PIN(xc); PIN(wc);
  acc = (f32x16){};
  bf16x8 xf[4], wf[4];
#define KV_RD(i, u) do { \
    lds_cptr px_ = (u) == 0 ? xc : xa + ((u) - 1) * 16384; \
    lds_cptr pw_ = (u) == 0 ? wc : wa + ((u) - 1) * 16384; \
    xf[i] = lds_rd128(px_); wf[i] = cat8(lds_tr(pw_), lds_tr(pw_ + 256)); \
  } while (0)
#define KV_MM(i) do { if (t) acc = MFMA32(xf[i], wf[i], acc); else acc = MFMA32(wf[i], xf[i], acc); } while (0)
  asm volatile("s_waitcnt vmcnt(6)\n\ts_barrier" : "+v"(nonce)::"memory");
  KV_RD(0, 0); KV_RD(1, 1); KV_RD(2, 2); KV_RD(3, 3);
  SBAR();
  dma.stage(0, 0);
  SBAR();
  KV_MM(0); KV_MM(1); KV_MM(2); KV_MM(3);
  asm volatile("s_waitcnt vmcnt(6) lgkmcnt(0)\n\ts_barrier" ::: "memory");
  KV_RD(0, 4); KV_RD(1, 5); KV_RD(2, 6);
  SBAR();
  sub(7); dma.stage(1, 1);
  SBAR();
  KV_MM(0); KV_MM(1); KV_MM(2);
  asm volatile("s_waitcnt vmcnt(8) lgkmcnt(0)\n\ts_barrier" ::: "memory");
  dma.piece(0, 2, 2); dma.piece(1, 2, 2);
#undef KV_RD
#undef KV_MM
#undef KV_OFF
  tag = nonce_tag(nonce);
  xlast = xc; wlast = wc;
}

template <class Init>
DI void pre_gate(const char* __restrict__ xg, const char* __restrict__ gw, char* __restrict__ ws, int row0, int w, PDma& dma, Init init,
                 u32x2& tag) {
  const int tid = threadIdx.x, lane = tid & 63;
  u32x4 gx[2][2], gv[2];
  u32x2 nonce;
  const char* px = xg + (size_t)(row0 + 2 * w) * 2048;
  asm volatile("global_load_dwordx2 %6, %10, %11 sc1\n\t"
               "global_load_dwordx4 %0, %7, %8\n\tglobal_load_dwordx4 %1, %7, %8 offset:16\n\t"
               "global_load_dwordx4 %2, %7, %8 offset:2048\n\tglobal_load_dwordx4 %3, %7, %8 offset:2064\n\t"
               "global_load_dwordx4 %4, %7, %9\n\tglobal_load_dwordx4 %5, %7, %9 offset:16"
               : "=&v"(gx[0][0]), "=&v"(gx[0][1]), "=&v"(gx[1][0]), "=&v"(gx[1][1]), "=&v"(gv[0]), "=&v"(gv[1]), "=&v"(nonce)
               : "v"(lane * 32), "s"(px), "s"(gw), "v"(0u), "s"(ws + WS_NONCE) : "memory");
  SBAR();
  init();
  dma.stage(0, 0);
  asm volatile("s_barrier" ::: "memory");
  dma.stage(1, 1); dma.piece(0, 2, 2); dma.piece(1, 2, 2);
  asm volatile("s_waitcnt vmcnt(8)\n\ts_barrier"
               : "+v"(gx[0][0]), "+v"(gx[0][1]), "+v"(gx[1][0]), "+v"(gx[1][1]), "+v"(gv[0]), "+v"(gv[1]), "+v"(nonce)::"memory");
  tag = nonce_tag(nonce);
#pragma unroll
  for (int i = 0; i < 2; ++i) {
    float z = 0.f, z2 = 0.f;
#pragma unroll
    for (int e = 0; e < 4; ++e) { z = dot2bf(gx[i][0][e], gv[0][e], z); z2 = dot2bf(gx[i][1][e], gv[1][e], z2); }
    z += z2;
    z = wave_sum63(z);
    const float g = 1.f / (1.f + __expf(-z));
    if (lane == 63) asm volatile("global_store_dword %0, %1, %2 sc1\n\ts_nop 0" :: "v"(0u), "v"(g), "s"(ws + WS_G + (row0 + 2 * w + i) * 4) : "memory");
  }
}

constexpr int SL = 32768;
constexpr int R_LDS = 5 * SL;
constexpr int XCH_OFF = 0;
constexpr int WO_OFF = 2 * SL;

DI int crow(int r, int h) { return (r & 3) + 8 * (r >> 2) + 4 * h; }
constexpr unsigned long long diag_lanes(int i) {
  unsigned long long m = 0;
  for (int l = 0; l < 64; ++l)
    if ((i & 3) + 8 * (i >> 2) + 4 * (l >> 5) <= (l & 31)) m |= 1ull << l;
  return m;
}
template <int I>
DI float diag_select(float keep_not, float keep) {
  float r;
  asm("v_cndmask_b32_e64 %0, %1, %2, %3" : "=v"(r) : "v"(keep_not), "v"(keep), "s"(diag_lanes(I)));
  return r;
}
template <int I>
DI void diag_masks(f32x16& mD, f32x16& mF, float neg, float zero) {
  if constexpr (I < 16) {
    mD[I] = diag_select<I>(neg, zero);
    mF[I] = diag_select<I>(zero, neg);
    diag_masks<I + 1>(mD, mF, neg, zero);
  }
}
DI float swap_half(float x) {
  auto rr = __builtin_amdgcn_permlane32_swap(__float_as_uint(x), __float_as_uint(x), false, false);
  return __uint_as_float((threadIdx.x & 32) ? rr[0] : rr[1]);
}
DI void exp16(f32x16& s) {
#pragma unroll
  for (int i = 0; i < 16; ++i) s[i] = __builtin_amdgcn_exp2f(s[i]);
}
#define AE(S, i) S[i] = __builtin_amdgcn_exp2f(S[i])
#define AC(P, S, i) P[(i) & 3] = cvtpk(S[2 * (i)], S[2 * (i) + 1])
#define ABC(x) __builtin_bit_cast(bf16x8, x)
#define APHASE(QK, PV, SX, PXa, PXb, SY, CINY, PYa, PYb, QY0, QY1, OY, LY, KF0, KF1, VF0, VF1, R1, R2) do { \
  if (QK) { SY = MFMA32(KF0, QY0, CINY); PIN(SY); } SBAR(); \
  AE(SX, 0); AE(SX, 1); AE(SX, 2); AE(SX, 3); PIN(SX); SBAR(); \
  if (QK) { SY = MFMA32(KF1, QY1, SY); PIN(SY); } SBAR(); \
  R1; \
  AC(PXa, SX, 0); AC(PXa, SX, 1); AE(SX, 4); AE(SX, 5); AE(SX, 6); PIN(SX); PIN(PXa); SBAR(); \
  if (PV) { OY = MFMA32(VF0, ABC(PYa), OY); PIN(OY); } SBAR(); \
  AE(SX, 7); AC(PXa, SX, 2); AE(SX, 8); AE(SX, 9); AC(PXa, SX, 3); PIN(SX); PIN(PXa); SBAR(); \
  if (PV) { LY = MFMA16(ones, ABC(PYa), LY); PIN(LY); } SBAR(); \
  R2; \
  AE(SX, 10); AE(SX, 11); PIN(SX); SBAR(); \
  if (PV) { OY = MFMA32(VF1, ABC(PYb), OY); PIN(OY); } SBAR(); \
  AC(PXb, SX, 4); AC(PXb, SX, 5); AE(SX, 12); AE(SX, 13); AE(SX, 14); PIN(SX); PIN(PXb); SBAR(); \
  if (PV) { LY = MFMA16(ones, ABC(PYb), LY); PIN(LY); } SBAR(); \
  AE(SX, 15); AC(PXb, SX, 6); AC(PXb, SX, 7); PIN(PXb); SBAR(); \
} while (0)

template <bool MASKED>
DI void attn_exact(const char* __restrict__ kv, int t0, int t1, int qt, const bf16x8& q0, const bf16x8& q1, const bf16x8& ones,
                   f32x16& o, float& l) {
  const int lane = threadIdx.x & 63, r = lane & 31, h = lane >> 5;
  float m = -3.0e38f;
  for (int t = t0; t < t1; ++t) {
    const char* p = kv + (size_t)t * 4096 + lane * 16;
    bf16x8 k0 = gload16_now(p), k1 = gload16_now(p + 1024);
    f32x16 s = {};
    s = MFMA32(k0, q0, s); s = MFMA32(k1, q1, s);
#pragma unroll
    for (int i = 0; i < 16; ++i) {
      bool ok = true;
      if (MASKED) { const int c = crow(i, h); ok = (t == qt) ? (c <= r) : ((t == qt - 8) ? (c > r) : true); }
      m = fmaxf(m, ok ? s[i] : -3.0e38f);
    }
  }
  m = fmaxf(m, swap_half(m));
  o = (f32x16){};
  f32x4 la = {};
  for (int t = t0; t < t1; ++t) {
    const char* p = kv + (size_t)t * 4096 + lane * 16;
    bf16x8 k0 = gload16_now(p), k1 = gload16_now(p + 1024);
    bf16x8 v0 = gload16_now(p + 2048), v1 = gload16_now(p + 3072);
    f32x16 s = {};
    s = MFMA32(k0, q0, s); s = MFMA32(k1, q1, s);
#pragma unroll
    for (int i = 0; i < 16; ++i) {
      bool ok = true;
      if (MASKED) { const int c = crow(i, h); ok = (t == qt) ? (c <= r) : ((t == qt - 8) ? (c > r) : true); }
      s[i] = ok ? __builtin_amdgcn_exp2f(s[i] - m) : 0.f;
    }
    bf16x8 pa = pack8(s, 0), pb = pack8(s, 8);
    o = MFMA32(v0, pa, o); o = MFMA32(v1, pb, o);
    la = MFMA16(ones, pa, la); la = MFMA16(ones, pb, la);
  }
  l = la[0];
}
DI bool ref_unsafe(const f32x16& o, float l) {
  float z = 0.f;
#pragma unroll
  for (int i = 0; i < 16; ++i) z = __builtin_fmaf(o[i], 0.f, z);
  const bool ok = (l >= 5.421010862e-20f) && (l <= 1.8446744e19f) && (z == 0.f);
  return __any(!ok);
}

__global__ __launch_bounds__(512, 2) void k_fused(const char* __restrict__ xa, const char* __restrict__ wq,
                                                  const char* __restrict__ wk, const char* __restrict__ wv,
                                                  const char* __restrict__ woa, const char* __restrict__ wqkv,
                                                  const char* __restrict__ wob, const char* __restrict__ gw,
                                                  char* __restrict__ ws, char* __restrict__ out) {
  extern __shared__ __attribute__((aligned(16))) char lds[];
  asm volatile("" :: "s"(xa), "s"(wq), "s"(wk), "s"(wv), "s"(wqkv), "s"(gw), "s"(ws));
  const int b = blockIdx.x;
  const int xcd = b & 7, idx = b >> 3;
  const int tid = threadIdx.x, lane = tid & 63;
  const int w = rfl(tid >> 6);
  const int r = lane & 31, h = lane >> 5;
  const unsigned lds0 = (unsigned)(uintptr_t)lds;
  const unsigned lo16 = lane * 16;
  const int pmb = (xcd & 1) * 8 + (idx & 7), pj = (xcd >> 1) * 4 + ((idx >> 3) & 1) * 2 + (idx >> 4);
  const int pr = w & 3, half = w >> 2;
  const char* kva = ws + WS_KVA;
  auto pieceA = [&](int k, int sl, int i) {
    const char* src = kva + (size_t)(half * 32 + 4 * k + pr) * 4096;
    const unsigned dst = lds0 + sl * SL + w * 4096;
    if (i == 0) glds16_sc1<0>(lo16, src, dst);
    else if (i == 1) glds16_sc1<1024>(lo16, src, dst);
    else if (i == 2) glds16_sc1<2048>(lo16, src, dst);
    else glds16_sc1<3072>(lo16, src, dst);
  };
  PDma dma;
  dma.init_uniform(xa, pmb, lds0, w);
  auto dinit = [&]() { dma.init_lanes(wq, wqkv, pj, lane); };
  u32x2 tag;
  const bool evenj = (pj & 1) == 0;
  const int kvrt = pmb * 4 + (pj >> 2), kvt = (pj >> 1) & 1;
  f32x16 kvacc = {};
  lds_cptr kvxl = (lds_cptr)lds, kvwl = (lds_cptr)lds;
  if (evenj) pre_kv(xa, kvt ? wv : wk, ws, kvrt, kvt, lds, w, dma, dinit, tag, kvacc, kvxl, kvwl);
  else pre_gate(xa, gw, ws, pmb * 128 + (pj >> 1) * 16, w, dma, dinit, tag);
  const int wn = w >> 1, pwm = w & 1;
  const int ct = 6 * pj + 2 * wn;
  const bool hasB = wn < 3;
  bf16x8 held[8];
  {
    f32x16 acc[2][2] = {};
    u32x2 kv0 = {~tag[0], 0u}, kv1 = kv0;
    auto tail = [&](int n) { pieceA(n >> 2, 2 + (n >> 2), n & 3); };
    auto peek = [&]() { flag_peek(kv0, lane * 8, ws + WS_FKV); flag_peek(kv1, lane * 8, ws + WS_FKV + 512); };
    auto check = [&]() {
      asm volatile("s_waitcnt vmcnt(4) lgkmcnt(0)" : "+v"(kv0), "+v"(kv1)::"memory");
      if (w == 0) { flag_wait(kv0, lane * 8, ws + WS_FKV, tag); flag_wait(kv1, lane * 8, ws + WS_FKV + 512, tag); }
      asm volatile("s_barrier" ::: "memory");
    };
    float* red = (float*)(lds + KVC) + lane * 4;
    auto put = [&](int s4) {
#pragma unroll
      for (int g = 0; g < 4; ++g) *(f32x4*)(red + (s4 * 4 + g) * 256) = (f32x4){kvacc[4 * g], kvacc[4 * g + 1], kvacc[4 * g + 2], kvacc[4 * g + 3]};
    };
    auto get = [&](int s4) {
#pragma unroll
      for (int g = 0; g < 4; ++g) {
        const f32x4 a = *(const f32x4*)(red + (s4 * 4 + g) * 256);
#pragma unroll
        for (int e = 0; e < 4; ++e) kvacc[4 * g + e] += a[e];
      }
    };
    auto hook = [&](int it) {
      if (evenj) {
        if (it == 0) {
          const bf16x8 xf = lds_rd128(kvxl), wf = cat8(lds_tr(kvwl), lds_tr(kvwl + 256));
          if (kvt) kvacc = MFMA32(xf, wf, kvacc); else kvacc = MFMA32(wf, xf, kvacc);
        }
        else if (it == 1) { if (w >= 4) put(w - 4); }
        else if (it == 2) { if (w < 4) get(w); }
        else if (it == 3) { if (w >= 1 && w < 4) put(w); }
        else if (it == 4) {
          if (w == 0) {
            get(1); get(2); get(3);
            char* dst = ws + WS_KVA + (size_t)kvrt * 4096 + kvt * 2048;
            gstore16_sc1<0>(lo16, dst, pack8(kvacc, 0));
            gstore16_sc1<1024>(lo16, dst, pack8(kvacc, 8));
          }
        } else if (it == 6) { if (tid == 0) flag_set(ws + WS_FKV + (kvrt * 2 + kvt) * 8, tag); }
      }
    };
    if (hasB && ct >= 64) proj_loop<true>(dma, lds, acc, tail, peek, check, hook);
    else proj_loop<false>(dma, lds, acc, tail, peek, check, hook);
    if (!hasB || ct < 32) {
#pragma unroll
      for (int i = 0; i < 2; ++i)
#pragma unroll
        for (int j = 0; j < 2; ++j)
#pragma unroll
          for (int e = 0; e < 16; ++e) acc[i][j][e] *= QSCALE;
    }
#pragma unroll
    for (int i = 0; i < 2; ++i)
#pragma unroll
      for (int j = 0; j < 2; ++j) {
        held[(i * 2 + j) * 2] = pack8(acc[i][j], 0);
        held[(i * 2 + j) * 2 + 1] = pack8(acc[i][j], 8);
      }
  }
  bf16x8 qb[2], q[2][2];
  typedef __attribute__((address_space(3))) bf16x8* lds_v8;
  const unsigned hstep = ct < 32 ? 2048 : 4096;
  char* hbase = ws + (ct < 32 ? WS_QB : WS_KVB + (ct >= 64 ? 2048 : 0)) + (size_t)((ct & 31) * 64 + pmb * 4 + pwm * 2) * hstep;
  auto storeH = [&](int n) {
    if (hasB) {
      char* dst = hbase + (size_t)((n >> 2) + ((n >> 1) & 1) * 64) * hstep;
      if (n & 1) gstore16_sc1<1024>(lo16, dst, held[n]);
      else gstore16_sc1<0>(lo16, dst, held[n]);
    }
  };
  if (!hasB) {
#pragma unroll
    for (int n = 0; n < 8; ++n)
      *(lds_v8)((lds_ptr)lds + QX_OFF + (((pwm * 2 + (n >> 2)) * 2 + ((n >> 1) & 1)) * 2 + (n & 1)) * 1024 + lo16) = held[n];
  }
  asm volatile("s_waitcnt vmcnt(4) lgkmcnt(0)\n\ts_barrier" ::: "memory");
#pragma unroll
  for (int u = 0; u < 2; ++u)
#pragma unroll
    for (int s2 = 0; s2 < 2; ++s2) q[u][s2] = lds_rd128((lds_cptr)lds + QX_OFF + ((pr * 2 + u) * 2 + s2) * 1024 + lo16);

  const int qt = pmb * 4 + pr, hA = 2 * pj;
  const int hB = (b & 7) + 8 * (b >> 6), rb = (b >> 3) & 7;
  const int qtB = rb * 8 + w;
  const int mb = (xcd & 3) * 4 + (idx & 3), nb = (xcd >> 2) * 8 + (idx >> 2);
  const char* kvb = ws + WS_KVB + (size_t)hB * 64 * 4096;
  bf16x8 ones;
  {
    const short v = ((lane & 3) == 0 && (((lane >> 4) & 1) == (((lane & 15) >> 2) & 1))) ? (short)0x3f80 : (short)0;
    ones = (bf16x8){v, v, v, v, v, v, v, v};
  }
  auto pieceB = [&](int n) {
    int gt = rb * 8 - 8 + 2 * w + (n >> 2);
    gt = gt < 0 ? 0 : gt;
    const char* src = kvb + (size_t)gt * 4096;
    const unsigned dst = lds0 + (2 * w + (n >> 2)) * 4096;
    if ((n & 3) == 0) glds16_sc1<0>(lo16, src, dst);
    else if ((n & 3) == 1) glds16_sc1<1024>(lo16, src, dst);
    else if ((n & 3) == 2) glds16_sc1<2048>(lo16, src, dst);
    else glds16_sc1<3072>(lo16, src, dst);
  };
  const char* pqb = ws + WS_QB + (size_t)(hB * 64 + qtB) * 2048;
  unsigned wo[2];
#pragma unroll
  for (int e = 0; e < 2; ++e) {
    const int row = 16 * w + 8 * e + (lane >> 3);
    const int c = (lane & 7) ^ (((row >> 1) & 1) << 2);
    wo[e] = row * 2048 + c * 16;
  }
  auto pieceW = [&](int st, int e) {
    const char* src = (st < 8 ? wob : woa) + (size_t)(st & 7) * 128 * 2048 + nb * 128;
    glds16<0>(wo[e], src, lds0 + WO_OFF + (st % 6) * 16384 + w * 2048 + e * 1024);
  };
#define NOP (void)0
  u32x2 seen;

  const f32x16 zero = {};
  f32x16 o0 = {}, o1 = {}, s0, s1;
  f32x4 l0 = {}, l1 = {};
  u32x4 p0a, p0b, p1a, p1b;
  bf16x8 ka0, ka1, kb0, kb1, va0, va1, vb0, vb1;
  lds_cptr kvbase = (lds_cptr)lds + half * 16384 + lane * 16;
#define APAIR(pp, SL0, SL1, FIRST, LAST, PA, PB, MID, D0, D1, D2, D3) do { \
    lds_cptr cur = kvbase + (SL0) * SL + ((pp) & 1) * 8192; PIN(cur);     \
    lds_cptr nxt = (SL0) == (SL1) && !((pp) & 1) ? cur + 8192 : kvbase + (SL1) * SL; \
    __builtin_amdgcn_s_setprio(PA); \
    if (FIRST) { ka0 = lds_rd128(cur); ka1 = lds_rd128(cur + 1024); s0 = MFMA32(ka0, q[0][0], zero); s0 = MFMA32(ka1, q[0][1], s0); } \
    SBAR(); \
    APHASE(1, !(FIRST), s0, p0a, p0b, s1, zero, p1a, p1b, q[1][0], q[1][1], o1, l1, ka0, ka1, vb0, vb1, \
           (kb0 = lds_rd128(cur + 4096), kb1 = lds_rd128(cur + 5120)), (va0 = lds_rd128(cur + 2048), va1 = lds_rd128(cur + 3072))); \
    D0; SBAR(); \
    APHASE(1, 1, s1, p1a, p1b, s0, zero, p0a, p0b, q[0][0], q[0][1], o0, l0, kb0, kb1, va0, va1, (void)0, (void)0); \
    D1; SBAR(); \
    MID; \
    __builtin_amdgcn_s_setprio(PB); SBAR(); \
    APHASE(1, 1, s0, p0a, p0b, s1, zero, p1a, p1b, q[1][0], q[1][1], o1, l1, kb0, kb1, va0, va1, \
           if (!(LAST)) (ka0 = lds_rd128(nxt), ka1 = lds_rd128(nxt + 1024)), (vb0 = lds_rd128(cur + 4096 + 2048), vb1 = lds_rd128(cur + 4096 + 3072))); \
    D2; SBAR(); \
    APHASE(!(LAST), 1, s1, p1a, p1b, s0, zero, p0a, p0b, q[0][0], q[0][1], o0, l0, ka0, ka1, vb0, vb1, (void)0, (void)0); \
    D3; SBAR(); \
    if (LAST) { \
      o1 = MFMA32(vb0, ABC(p1a), o1); o1 = MFMA32(vb1, ABC(p1b), o1); \
      l1 = MFMA16(ones, ABC(p1a), l1); l1 = MFMA16(ones, ABC(p1b), l1); \
    } \
  } while (0)
#define DA(k, sl, i) pieceA(k, sl, i)
#define DA2(k, sl, i) (pieceA(k, sl, i), pieceA(k, sl, (i) + 1))
#define DB2(n) (pieceB(n), pieceB((n) + 1))
#define DW(st, e) pieceW(st, e)
#define BAR_V(n) asm volatile("s_waitcnt vmcnt(" #n ")\n\ts_barrier" ::: "memory")
  const int fbm = 2 * rb - 2 + (lane & 3);
  const unsigned fbo = ((fbm < 0 ? 2 * rb : fbm) * 16 + ((lane >> 2) % 3 * 32 + hB) / 6) * 8;
  seen = (u32x2){~tag[0], 0u};
#define DH4(n) (storeH(n), storeH((n) + 1), storeH((n) + 2), storeH((n) + 3))
  APAIR(0, 2, 2, 1, 0, 2, 1, NOP, DA2(2, 4, 0), DA2(2, 4, 2), DH4(0), DH4(4));
  APAIR(1, 2, 3, 0, 0, 0, 3, { if (hasB) BAR_V(12); else BAR_V(4); }, NOP, NOP, NOP, NOP);
  APAIR(2, 3, 3, 0, 0, 2, 1, NOP, NOP, NOP, NOP, NOP);
  APAIR(3, 3, 4, 0, 0, 0, 3, { BAR_V(0); if (tid == 0) flag_set(ws + WS_FPB + (pmb * 16 + pj) * 8, tag);   },
        NOP, NOP, DA2(3, 2, 0), DA2(3, 2, 2));
  APAIR(4, 4, 4, 0, 0, 2, 1, NOP, NOP, NOP, NOP, NOP);
  flag_peek(seen, fbo, ws + WS_FPB);
  APAIR(5, 4, 2, 0, 0, 0, 3, { asm volatile("s_waitcnt vmcnt(0)" : "+v"(seen)::"memory"); if (w == 0) flag_wait(seen, fbo, ws + WS_FPB, tag); asm volatile("s_barrier" ::: "memory"); },
        NOP, NOP, DB2(0), DB2(2));
  APAIR(6, 2, 2, 0, 0, 2, 1, NOP, DB2(4), DB2(6), (gload16_sc1<0>(qb[0], lo16, pqb), gload16_sc1<1024>(qb[1], lo16, pqb)), DA2(4, 3, 0));
  APAIR(7, 2, 2, 0, 1, 1, 0, NOP, DA2(4, 3, 2), NOP, NOP, NOP);
  asm volatile("s_waitcnt vmcnt(0) lgkmcnt(0)\n\ts_barrier" : "+v"(qb[0]), "+v"(qb[1])::"memory");
  {
    f32x16 o = {};
    f32x4 la = {};
    lds_cptr bb = (lds_cptr)lds + w * 4096 + lane * 16;
    f32x16 mD, sa;
    diag_masks<0>(mD, sa, -3.0e38f, 0.f);
    if (rb != 0) {
      f32x16 sb;
      u32x4 pa0 = {}, pa1 = {}, pb0 = {}, pb1 = {};
      bf16x8 k0, k1, k2, k3, v0, v1, v2, v3;
      k0 = lds_rd128(bb); k1 = lds_rd128(bb + 1024);
      k2 = lds_rd128(bb + 4096); k3 = lds_rd128(bb + 4096 + 1024);
      sa = MFMA32(k0, qb[0], sa); sa = MFMA32(k1, qb[1], sa);
      SBAR();
#define BRDK(KA, KB, j) (KA = lds_rd128(bb + (j) * 4096), KB = lds_rd128(bb + (j) * 4096 + 1024))
#define BRDV(VA, VB, j) (VA = lds_rd128(bb + (j) * 4096 + 2048), VB = lds_rd128(bb + (j) * 4096 + 3072))
#define B_DMA(n) do { pieceA(5, 4, n); SBAR(); } while (0)
      __builtin_amdgcn_s_setprio(3);
      APHASE(1, 0, sa, pa0, pa1, sb, zero, pb0, pb1, qb[0], qb[1], o, la, k2, k3, v2, v3, BRDK(k0, k1, 2), BRDV(v0, v1, 0));
      B_DMA(0); B_DMA(1);
      APHASE(1, 1, sb, pb0, pb1, sa, zero, pa0, pa1, qb[0], qb[1], o, la, k0, k1, v0, v1, BRDK(k2, k3, 3), BRDV(v2, v3, 1));
      B_DMA(2); B_DMA(3);
      __builtin_amdgcn_s_setprio(2);
      APHASE(1, 1, sa, pa0, pa1, sb, zero, pb0, pb1, qb[0], qb[1], o, la, k2, k3, v2, v3, BRDK(k0, k1, 4), BRDV(v0, v1, 2));
      APHASE(1, 1, sb, pb0, pb1, sa, zero, pa0, pa1, qb[0], qb[1], o, la, k0, k1, v0, v1, BRDK(k2, k3, 5), BRDV(v2, v3, 3));
      __builtin_amdgcn_s_setprio(1);
      APHASE(1, 1, sa, pa0, pa1, sb, zero, pb0, pb1, qb[0], qb[1], o, la, k2, k3, v2, v3, BRDK(k0, k1, 6), BRDV(v0, v1, 4));
      APHASE(1, 1, sb, pb0, pb1, sa, zero, pa0, pa1, qb[0], qb[1], o, la, k0, k1, v0, v1, BRDK(k2, k3, 7), BRDV(v2, v3, 5));
      __builtin_amdgcn_s_setprio(0);
      APHASE(1, 1, sa, pa0, pa1, sb, zero, pb0, pb1, qb[0], qb[1], o, la, k2, k3, v2, v3, BRDK(k0, k1, 8), BRDV(v0, v1, 6));
      APHASE(1, 1, sb, pb0, pb1, sa, mD, pa0, pa1, qb[0], qb[1], o, la, k0, k1, v0, v1, (void)0, BRDV(v2, v3, 7));
      APHASE(0, 1, sa, pa0, pa1, sb, zero, pb0, pb1, qb[0], qb[1], o, la, k2, k3, v2, v3, (void)0, BRDV(v0, v1, 8));
      o = MFMA32(v0, ABC(pa0), o); o = MFMA32(v1, ABC(pa1), o);
      la = MFMA16(ones, ABC(pa0), la); la = MFMA16(ones, ABC(pa1), la);
    } else {
#pragma unroll
      for (int n = 0; n < 4; ++n) B_DMA(n);
      auto tile = [&](int j, const f32x16& cin) {
        lds_cptr p = bb + j * 4096;
        bf16x8 k0 = lds_rd128(p), k1 = lds_rd128(p + 1024), v0 = lds_rd128(p + 2048), v1 = lds_rd128(p + 3072);
        f32x16 s = MFMA32(k0, qb[0], cin);
        s = MFMA32(k1, qb[1], s);
        exp16(s);
        bf16x8 pa = pack8(s, 0), pb = pack8(s, 8);
        o = MFMA32(v0, pa, o); o = MFMA32(v1, pb, o);
        la = MFMA16(ones, pa, la); la = MFMA16(ones, pb, la);
      };
      for (int j = 8 - w; j < 8; ++j) tile(j, zero);
      tile(8, mD);
    }
    float l = la[0];
    if (__builtin_expect(ref_unsafe(o, l), 0)) attn_exact<true>(kvb, qtB >= 8 ? qtB - 8 : 0, qtB + 1, qtB, qb[0], qb[1], ones, o, l);
    const float sc = __builtin_amdgcn_rcpf(l);
    char* dst = ws + WS_O + (size_t)(qtB * 64 + 32 + hB) * 2048;
    gstore16_sc1<0>(lo16, dst, pack8s(o, 0, sc));
    gstore16_sc1<1024>(lo16, dst, pack8s(o, 8, sc));
  }
  asm volatile("s_waitcnt vmcnt(2) lgkmcnt(0)\n\ts_barrier" ::: "memory");
  const int wm = w & 3, kh = w >> 2;
  const char* obase = ws + WS_O + (size_t)(mb * 4 + wm) * 64 * 2048 + kh * 4096;
  bf16x8 of[5][4];
  auto loadO1 = [&](int st, int i, bf16x8& f) {
    const char* src = obase + (size_t)(st < 8 ? 32 + 4 * st : 4 * (st - 8)) * 2048;
    if (i == 0) gload16_sc1<0>(f, lo16, src);
    else if (i == 1) gload16_sc1<1024>(f, lo16, src);
    else if (i == 2) gload16_sc1<2048>(f, lo16, src);
    else gload16_sc1<3072>(f, lo16, src);
  };
#define LO(st, i) loadO1(st, i, of[st][i])
  auto loadO = [&](int st, bf16x8 (&f)[4]) {
    const char* src = obase + (size_t)(st < 8 ? 32 + 4 * st : 4 * (st - 8)) * 2048;
    gload16_sc1<0>(f[0], lo16, src); gload16_sc1<1024>(f[1], lo16, src);
    gload16_sc1<2048>(f[2], lo16, src); gload16_sc1<3072>(f[3], lo16, src);
  };
  const unsigned fob = ((mb >> 1) * 32 + (lane & 31)) * 8;
  seen = (u32x2){~tag[0], 0u};
  unsigned warm = 0;
  APAIR(8, 3, 3, 1, 0, 3, 3, NOP, DA(6, 0, 0), DA(6, 0, 1), DA(6, 0, 2), DA(6, 0, 3));
  APAIR(9, 3, 4, 0, 0, 2, 2, NOP, DA(7, 1, 0), DA(7, 1, 1), DA(7, 1, 2), DA(7, 1, 3));
  {
    const int R = 768 + (idx & 3) * 320 + w * 40 + (lane < 40 ? lane : 39);
    const char* pw = (R < 1024 ? wob + (size_t)R * 2048 : woa + (size_t)(R - 1024) * 2048) + nb * 128;
    asm volatile("global_load_dword %0, %1, off" : "+v"(warm) : "v"(pw) : "memory");
  }
  APAIR(10, 4, 4, 0, 0, 1, 0, NOP, NOP, NOP, NOP, NOP);
  APAIR(11, 4, 0, 0, 0, 0, 3, { asm volatile("s_waitcnt vmcnt(0)\n\ts_barrier" : "+v"(warm)::"memory");
                                if (tid == 0) flag_set(ws + WS_FOB + (rb * 32 + hB) * 8, tag);   }, NOP, NOP, NOP, NOP);
  APAIR(12, 0, 0, 0, 0, 3, 2, NOP, DW(0, 0), DW(0, 1), DW(1, 0), DW(1, 1));
  flag_peek(seen, fob, ws + WS_FOB);
  APAIR(13, 0, 1, 0, 0, 2, 1, NOP, DW(2, 0), DW(2, 1), DW(3, 0), DW(3, 1));
  APAIR(14, 1, 1, 0, 0, 0, 3, { asm volatile("s_waitcnt vmcnt(0)" : "+v"(seen)::"memory"); if (w == 0) flag_wait(seen, fob, ws + WS_FOB, tag); asm volatile("s_barrier" ::: "memory"); },
        NOP, NOP, (LO(0, 0), LO(0, 1), DW(4, 0)), (LO(0, 2), LO(0, 3), DW(4, 1)));
  APAIR(15, 1, 1, 0, 1, 2, 1, NOP, (LO(1, 0), LO(1, 1), DW(5, 0)), (LO(1, 2), LO(1, 3), DW(5, 1)), (LO(2, 0), LO(2, 1)), (LO(2, 2), LO(2, 3)));

  loadO(3, of[3]);
  asm volatile("s_waitcnt lgkmcnt(0)\n\ts_barrier" ::: "memory");
  {
    float* xb = (float*)(lds + XCH_OFF) + w * (5 * 256);
    const f32x16& os = half ? o0 : o1;
    const float ls = half ? l0[0] : l1[0];
#pragma unroll
    for (int g = 0; g < 4; ++g) *(f32x4*)(xb + g * 256 + lane * 4) = (f32x4){os[4 * g], os[4 * g + 1], os[4 * g + 2], os[4 * g + 3]};
    xb[4 * 256 + lane] = ls;
  }
  f32x16 oA = half ? o1 : o0;
  float lA = half ? l1[0] : l0[0];
  auto finishA = [&]() {
    const float* xb = (const float*)(lds + XCH_OFF) + (w ^ 4) * (5 * 256);
#pragma unroll
    for (int g = 0; g < 4; ++g) {
      f32x4 v = *(const f32x4*)(xb + g * 256 + lane * 4);
#pragma unroll
      for (int e = 0; e < 4; ++e) oA[4 * g + e] += v[e];
    }
    lA += xb[4 * 256 + lane];
    if (__builtin_expect(ref_unsafe(oA, lA), 0)) {
      if (half) attn_exact<false>(kva, 0, 64, 0, q[1][0], q[1][1], ones, oA, lA);
      else attn_exact<false>(kva, 0, 64, 0, q[0][0], q[0][1], ones, oA, lA);
    }
    const float sc = __builtin_amdgcn_rcpf(lA);
    char* dst = ws + WS_O + (size_t)(qt * 64 + hA + half) * 2048;
    gstore16_sc1<0>(lo16, dst, pack8s(oA, 0, sc));
    gstore16_sc1<1024>(lo16, dst, pack8s(oA, 8, sc));
  };
  {
    const int g16 = lane >> 4, q4 = (lane & 15) >> 2, p4 = lane & 3;
    const int wc = 2 * (g16 & 1) + (p4 >> 1);
    const int wa0 = (kh * 64 + 4 * h + q4) * 128 + ((wc ^ (((q4 >> 1) & 1) << 2)) << 4) + 8 * (p4 & 1);
    lds_cptr wbase[2][2] = {{(lds_cptr)lds + WO_OFF + wa0, (lds_cptr)lds + WO_OFF + (wa0 ^ 64)},
                            {(lds_cptr)lds + WO_OFF + 49152 + wa0, (lds_cptr)lds + WO_OFF + 49152 + (wa0 ^ 64)}};
    PIN(wbase[0][0]); PIN(wbase[0][1]); PIN(wbase[1][0]); PIN(wbase[1][1]);
    f32x16 accB[2] = {}, accA[2] = {};
    bf16x8 wf[4][2];
    const unsigned foa = (mb * 16 + (lane & 15)) * 8;
    u32x2 seenA = {~tag[0], 0u};
    float g = 0.f;
    u32x4 all0 = {~tag[0], 0u, 0u, 0u}, all1 = all0;
    auto rdw = [&](bf16x8 (&f)[2], int st, int ks) {
#pragma unroll
      for (int j = 0; j < 2; ++j) {
        lds_cptr p = wbase[(st % 6) / 3][j] + ((st % 6) % 3) * 16384 + ks * 2048;
        f[j] = cat8(lds_tr(p), lds_tr(p + 1024));
      }
    };
    asm volatile("s_waitcnt vmcnt(15)" : "+v"(of[0][0]), "+v"(of[0][1]), "+v"(of[0][2]), "+v"(of[0][3])::"memory");
    rdw(wf[0], 0, 0); rdw(wf[1], 0, 1);
#define O_MM(ACC, f, a) do { ACC[0] = MFMA32(f[0], a, ACC[0]); SBAR(); ACC[1] = MFMA32(f[1], a, ACC[1]); SBAR(); } while (0)
#define O_STAGE(st, ACC, VM, PRE, POST) do { \
      rdw(wf[2], st, 2); SBAR(); O_MM(ACC, wf[0], of[(st) % 5][0]); \
      rdw(wf[3], st, 3); SBAR(); O_MM(ACC, wf[1], of[(st) % 5][1]); \
      if ((st) < 15) { \
        if ((st) == 0) asm volatile("s_waitcnt lgkmcnt(0)" ::: "memory");     \
        asm volatile("s_waitcnt vmcnt(" #VM ")" : "+v"(of[((st) + 1) % 5][0]), "+v"(of[((st) + 1) % 5][1]), \
                     "+v"(of[((st) + 1) % 5][2]), "+v"(of[((st) + 1) % 5][3]), "+v"(seenA)::"memory"); \
        PRE; \
        if ((st) < 13) asm volatile("s_barrier" ::: "memory");     \
        POST; \
        SBAR(); \
        rdw(wf[0], (st) + 1, 0); \
      } \
      SBAR(); O_MM(ACC, wf[2], of[(st) % 5][2]); \
      if ((st) < 15) rdw(wf[1], (st) + 1, 1); \
      SBAR(); O_MM(ACC, wf[3], of[(st) % 5][3]); \
    } while (0)
#define OW(st) (pieceW(st, 0), pieceW(st, 1))
#define OL(st) loadO(st, of[(st) % 5])
    O_STAGE(0, accB, 9, NOP, { OL(4); SBAR(); finishA(); });
    O_STAGE(1, accB, 10, NOP, (OW(6), OL(5)));
    O_STAGE(2, accB, 6, NOP, { if (tid == 0) flag_set(ws + WS_FOA + (pmb * 16 + pj) * 8, tag);   OW(7); OL(6); });
    O_STAGE(3, accB, 12, NOP, (OW(8), OL(7)));
    O_STAGE(4, accB, 12, NOP, (flag_peek(seenA, foa, ws + WS_FOA), OW(9)));
    O_STAGE(5, accB, 2, if (w == 0) flag_wait(seenA, foa, ws + WS_FOA, tag),
            (gload4_sc1(g, (wm * 32 + r) * 4, ws + WS_G + mb * 512), OW(10), OL(8)));
    O_STAGE(6, accB, 9, NOP, (OW(11), OL(9)));
    O_STAGE(7, accB, 6, NOP, (OW(12), OL(10)));
    O_STAGE(8, accA, 6, NOP, (OW(13), OL(11)));
    O_STAGE(9, accA, 6, NOP, (OW(14), OL(12)));
    O_STAGE(10, accA, 6, NOP, (OW(15), OL(13)));
    O_STAGE(11, accA, 6, NOP, OL(14));
    O_STAGE(12, accA, 4, NOP, OL(15));
    O_STAGE(13, accA, 4, NOP, (flag_peek2(all0, lane * 16, ws + WS_FOA), flag_peek2(all1, lane * 16, ws + WS_FOA + 1024)));
    O_STAGE(14, accA, 2, NOP, NOP);
    O_STAGE(15, accA, 0, NOP, NOP);
    asm volatile("s_waitcnt vmcnt(0)" : "+v"(g), "+v"(all0), "+v"(all1)::"memory");
    if (b == 0 && w == 0) {
      if (!__all(all0[0] == tag[0] && all0[1] == tag[1] && all0[2] == tag[0] && all0[3] == tag[1] &&
                 all1[0] == tag[0] && all1[1] == tag[1] && all1[2] == tag[0] && all1[3] == tag[1])) {
        const u32x2 none = {~tag[0], 0u};
#pragma unroll
        for (int i = 0; i < 4; ++i) flag_wait(none, lane * 8, ws + WS_FOA + i * 512, tag);
      }
      if (lane == 0) flag_set(ws + WS_NONCE, tag);
    }
    float* xw = (float*)(lds + XCH_OFF) + w * 1024 + lane * 4;
    f32x16 acc[2];
#pragma unroll
    for (int j = 0; j < 2; ++j)
#pragma unroll
      for (int i = 0; i < 16; ++i) acc[j][i] = g * accA[j][i] + (1.f - g) * accB[j][i];
    const f32x16& as = kh ? acc[0] : acc[1];
#pragma unroll
    for (int g = 0; g < 4; ++g) *(f32x4*)(xw + g * 256) = (f32x4){as[4 * g], as[4 * g + 1], as[4 * g + 2], as[4 * g + 3]};
    asm volatile("s_waitcnt lgkmcnt(0)\n\ts_barrier" ::: "memory");
    f32x16 t = kh ? acc[1] : acc[0];
    const float* xr = (const float*)(lds + XCH_OFF) + (w ^ 4) * 1024 + lane * 4;
#pragma unroll
    for (int g = 0; g < 4; ++g) {
      f32x4 v = *(const f32x4*)(xr + g * 256);
#pragma unroll
      for (int e = 0; e < 4; ++e) t[4 * g + e] += v[e];
    }
    const int row = mb * 128 + wm * 32 + r;
    char* dst = out + (size_t)row * 2048 + (nb * 64 + kh * 32) * 2;
#pragma unroll
    for (int g = 0; g < 4; g += 2) {
      u32x2 a = {cvtpk(t[4 * g], t[4 * g + 1]), cvtpk(t[4 * g + 2], t[4 * g + 3])};
      u32x2 c = {cvtpk(t[4 * g + 4], t[4 * g + 5]), cvtpk(t[4 * g + 6], t[4 * g + 7])};
      auto x = __builtin_amdgcn_permlane32_swap(a[0], c[0], false, false);
      auto y = __builtin_amdgcn_permlane32_swap(a[1], c[1], false, false);
      *(u32x4*)(dst + g * 16 + h * 16) = (u32x4){x[0], y[0], x[1], y[1]};
    }
  }
}

extern "C" void kernel_launch(void* const* d_in, const int* in_sizes, int n_in, void* d_out, int out_size, void* d_ws,
                              size_t ws_size, hipStream_t stream) {
  const char* xa = (const char*)d_in[0];
  const char* wq = (const char*)d_in[1];
  const char* wk = (const char*)d_in[2];
  const char* wv = (const char*)d_in[3];
  const char* woa = (const char*)d_in[4];
  const char* wqkv = (const char*)d_in[6];
  const char* wob = (const char*)d_in[7];
  const char* gw = (const char*)d_in[8];
  (void)hipFuncSetAttribute((const void*)k_fused, hipFuncAttributeMaxDynamicSharedMemorySize, R_LDS);
  k_fused<<<256, 512, R_LDS, stream>>>(xa, wq, wk, wv, woa, wqkv, wob, gw, (char*)d_ws, (char*)d_out);
}
```

```cpp
#include <hip/hip_runtime.h>
#include <hip/hip_bf16.h>
#include <stdint.h>

typedef float f32x16 __attribute__((ext_vector_type(16)));
typedef float f32x4 __attribute__((ext_vector_type(4)));
typedef float f32x2 __attribute__((ext_vector_type(2)));
typedef short bf16x8 __attribute__((ext_vector_type(8)));
typedef short s16x4 __attribute__((ext_vector_type(4)));
typedef __bf16 bf16x2 __attribute__((ext_vector_type(2)));
typedef unsigned u32x4 __attribute__((ext_vector_type(4)));
typedef unsigned u32x2 __attribute__((ext_vector_type(2)));
typedef __attribute__((address_space(3))) const char* lds_cptr;
typedef __attribute__((address_space(3))) char* lds_ptr;

#define DI __device__ __forceinline__
#define SBAR() __builtin_amdgcn_sched_barrier(0)
#define PIN(x) asm volatile("" : "+v"(x))
#define MFMA32(a, b, c) __builtin_amdgcn_mfma_f32_32x32x16_bf16(a, b, c, 0, 0, 0)
#define MFMA16(a, b, c) __builtin_amdgcn_mfma_f32_16x16x32_bf16(a, b, c, 0, 0, 0)

constexpr int SEQ = 2048, DM = 1024;
constexpr float QSCALE = 0.17677669529663687f * 1.4426950408889634f;

constexpr size_t WS_KVA = 4u << 20;
constexpr size_t WS_G = WS_KVA + (256u << 10);
constexpr size_t WS_NONCE = WS_G + 8192;
constexpr size_t WS_FKV = WS_NONCE + 1024;
constexpr size_t WS_FPB = WS_FKV + 1024;
constexpr size_t WS_FOA = WS_FPB + 2048;
constexpr size_t WS_FOB = WS_FOA + 2048;
constexpr size_t WS_QB = 5u << 20;
constexpr size_t WS_KVB = 9u << 20;
constexpr size_t WS_O = 17u << 20;

DI unsigned rfl(unsigned x) { return __builtin_amdgcn_readfirstlane(x); }
DI unsigned cvtpk(float lo, float hi) {
  f32x2 v = {lo, hi};
  bf16x2 b = __builtin_convertvector(v, bf16x2);
  return __builtin_bit_cast(unsigned, b);
}
DI bf16x8 pack8(const f32x16& a, int o) {
  u32x4 r = {cvtpk(a[o], a[o + 1]), cvtpk(a[o + 2], a[o + 3]), cvtpk(a[o + 4], a[o + 5]), cvtpk(a[o + 6], a[o + 7])};
  return __builtin_bit_cast(bf16x8, r);
}
DI bf16x8 pack8s(const f32x16& a, int o, float s) {
  u32x4 r = {cvtpk(a[o] * s, a[o + 1] * s), cvtpk(a[o + 2] * s, a[o + 3] * s), cvtpk(a[o + 4] * s, a[o + 5] * s),
             cvtpk(a[o + 6] * s, a[o + 7] * s)};
  return __builtin_bit_cast(bf16x8, r);
}
template <int OFF>
DI void glds16(unsigned voff, const void* sbase, unsigned lds_base) {
  asm volatile("s_mov_b32 m0, %2\n\ts_nop 0\n\tglobal_load_lds_dwordx4 %0, %1 offset:%3"
               :: "v"(voff), "s"(sbase), "s"(rfl(lds_base)), "i"(OFF) : "memory");
}
template <int OFF>
DI void glds16_sc1(unsigned voff, const void* sbase, unsigned lds_base) {
  asm volatile("s_mov_b32 m0, %2\n\ts_nop 0\n\tglobal_load_lds_dwordx4 %0, %1 offset:%3 sc1"
               :: "v"(voff), "s"(sbase), "s"(rfl(lds_base)), "i"(OFF) : "memory");
}
DI void glds16v(const char* vaddr, unsigned lds_base) {
  asm volatile("s_mov_b32 m0, %1\n\ts_nop 0\n\tglobal_load_lds_dwordx4 %0, off" :: "v"(vaddr), "s"(rfl(lds_base)) : "memory");
}
DI bf16x8 lds_rd128(lds_cptr p) { return *(const __attribute__((address_space(3))) bf16x8*)p; }
DI s16x4 lds_tr(lds_cptr p) {
  typedef short v4i16_t __attribute__((ext_vector_type(4)));
  return __builtin_bit_cast(s16x4, __builtin_amdgcn_ds_read_tr16_b64_v4i16((__attribute__((address_space(3))) v4i16_t*)p));
}
DI bf16x8 cat8(s16x4 lo, s16x4 hi) { return (bf16x8){lo[0], lo[1], lo[2], lo[3], hi[0], hi[1], hi[2], hi[3]}; }
DI float bf2f(unsigned short b) { return __uint_as_float((unsigned)b << 16); }

template <int OFF>
DI void gload16_sc1(bf16x8& d, unsigned voff, const void* sbase) {
  asm volatile("global_load_dwordx4 %0, %1, %2 offset:%3 sc1" : "=v"(d) : "v"(voff), "s"(sbase), "i"(OFF) : "memory");
}
template <int OFF>
DI void gstore16_sc1(unsigned voff, void* sbase, const bf16x8& d) {
  asm volatile("global_store_dwordx4 %0, %1, %2 offset:%3 sc1\n\ts_nop 1" :: "v"(voff), "v"(d), "s"(sbase), "i"(OFF) : "memory");
}
DI void gload4_sc1(float& d, unsigned voff, const void* sbase) {
  asm volatile("global_load_dword %0, %1, %2 sc1" : "=v"(d) : "v"(voff), "s"(sbase) : "memory");
}
DI bf16x8 gload16_now(const char* p) {
  bf16x8 d;
  asm volatile("global_load_dwordx4 %0, %1, off sc1\n\ts_waitcnt vmcnt(0)" : "=&v"(d) : "v"(p) : "memory");
  return d;
}
typedef __attribute__((address_space(1))) unsigned long long gu64;
DI void flag_set(char* p, u32x2 tag) {
  asm volatile("global_store_dwordx2 %0, %1, %2 sc1\n\ts_nop 1" :: "v"(0u), "v"(tag), "s"(p) : "memory");
}
DI void flag_peek(u32x2& d, unsigned voff, const void* sbase) {
  asm volatile("global_load_dwordx2 %0, %1, %2 sc1" : "+v"(d) : "v"(voff), "s"(sbase) : "memory");
}
DI void flag_peek2(u32x4& d, unsigned voff, const void* sbase) {
  asm volatile("global_load_dwordx4 %0, %1, %2 sc1" : "+v"(d) : "v"(voff), "s"(sbase) : "memory");
}
DI void flag_wait(u32x2 seen, unsigned voff, const char* sbase, u32x2 tag) {
  if (__builtin_expect(__all(seen[0] == tag[0] && seen[1] == tag[1]), 1)) return;
  const unsigned long long want = ((unsigned long long)tag[1] << 32) | tag[0];
  for (unsigned spins = 0; spins < (1u << 21); ++spins) {
    if (__all(__hip_atomic_load((gu64*)(sbase + voff), __ATOMIC_RELAXED, __HIP_MEMORY_SCOPE_AGENT) == want)) return;
    __builtin_amdgcn_s_sleep(4);
  }
}

DI float dot2bf(unsigned a, unsigned b, float c) {
  return __builtin_amdgcn_fdot2_f32_bf16(__builtin_bit_cast(bf16x2, a), __builtin_bit_cast(bf16x2, b), c, false);
}
DI float wave_sum63(float z) {
#define DPP_ADD(ctrl, rmask) z += __uint_as_float(__builtin_amdgcn_update_dpp(0u, __float_as_uint(z), ctrl, rmask, 0xf, true))
  DPP_ADD(0x111, 0xf); DPP_ADD(0x112, 0xf); DPP_ADD(0x114, 0xf); DPP_ADD(0x118, 0xf);
  DPP_ADD(0x142, 0xa); DPP_ADD(0x143, 0xc);
#undef DPP_ADD
  return z;
}

DI void nonce_load(u32x2& nonce, const char* ws) {
  asm volatile("global_load_dwordx2 %0, %1, %2 sc1" : "=v"(nonce) : "v"(0u), "s"(ws + WS_NONCE) : "memory");
}
DI u32x2 nonce_tag(u32x2 nonce) {
  u32x2 tag;
  tag[0] = nonce[0] + 1u;
  tag[1] = nonce[1] + (tag[0] == 0u ? 1u : 0u);
  return tag;
}

constexpr int P_SLOT = 49152;
constexpr int P_XOFF = 0, P_WOFF = 16384;
constexpr int QX_OFF = 0;
constexpr int KVC = 147456;
struct PDma {
  const char* xsrc; const char* wp[4]; unsigned xo[2], wstep[2], lds0; int w;
  DI void init_uniform(const char* xa, int mb, unsigned lds0_, int w_) {
    lds0 = lds0_; w = w_;
    xsrc = xa + (size_t)mb * 128 * 2048;
  }
  DI void init_lanes(const char* wq, const char* wqkv, int j, int lane) {
#pragma unroll
    for (int e = 0; e < 2; ++e) {
      const int row = 16 * w + 8 * e + (lane >> 3);
      xo[e] = row * 2048 + (((lane & 7) ^ (4 * e + (lane >> 4))) << 4);
    }
#pragma unroll
    for (int e = 0; e < 4; ++e) {
      const int krow = 8 * w + 2 * e + (lane >> 5);
      const int c = (lane & 31) ^ ((krow & 3) << 2);
      wp[e] = c < 24 ? wqkv + (size_t)krow * 6144 + j * 384 + c * 16 : wq + (size_t)krow * 2048 + j * 128 + (c - 24) * 16;
      if (e < 2) wstep[e] = c < 24 ? 64 * 6144 : 64 * 2048;
    }
  }
  DI void piece(int pc, int st, int slot) {
    const unsigned lb = lds0 + slot * P_SLOT;
    if (pc < 2) glds16<0>(xo[pc], xsrc + st * 128, lb + P_XOFF + w * 2048 + pc * 1024);
    else { glds16v(wp[pc - 2], lb + P_WOFF + w * 4096 + (pc - 2) * 1024); wp[pc - 2] += wstep[pc & 1]; }
  }
  DI void stage(int st, int slot) {
#pragma unroll
    for (int pc = 0; pc < 6; ++pc) piece(pc, st, slot);
  }
};
struct PFrag { bf16x8 x[2]; bf16x8 w[2]; };

template <bool VT, class Tail, class Peek, class Check, class Hook>
DI void proj_loop(PDma& dma, char* lds, f32x16 (&acc)[2][2], Tail tail, Peek peek, Check check, Hook hook) {
  const int tid = threadIdx.x, lane = tid & 63;
  const int w = dma.w;
  const int wm = w & 1, wn = w >> 1;
  const int r = lane & 31, h = lane >> 5;
  const int t3 = (r >> 1) & 7;
  const int xa0 = P_XOFF + (wm * 64 + r) * 128 + ((h ^ t3) << 4);
  const int g16 = lane >> 4, q4 = (lane & 15) >> 2, p4 = lane & 3;
  const int wc = wn * 8 + 2 * (g16 & 1) + (p4 >> 1);
  const int wa0 = P_WOFF + (8 * h + q4) * 512 + ((wc ^ (q4 << 2)) << 4) + 8 * (p4 & 1);
  auto rdx = [&](PFrag& f, int i, int slot, int s) { f.x[i] = lds_rd128((lds_cptr)lds + slot * P_SLOT + (xa0 ^ (s << 5)) + i * 4096); };
  auto rdw = [&](PFrag& f, int j, int slot, int s) {
    lds_cptr p = (lds_cptr)lds + slot * P_SLOT + (wa0 ^ (j << 6)) + s * 8192;
    f.w[j] = cat8(lds_tr(p), lds_tr(p + 2048));
  };
  auto mm = [&](const PFrag& f, int i, int j) {
    if constexpr (VT) acc[i][j] = MFMA32(f.x[i], f.w[j], acc[i][j]);
    else acc[i][j] = MFMA32(f.w[j], f.x[i], acc[i][j]);
  };
  auto win = [&](int j, int pc, int slot) {
    if (j == -1) dma.piece(pc, 2, 2);
    else if (j <= 12) dma.piece(pc, j + 3, slot);
    else if (j <= 14 && pc < 4) tail((j - 13) * 4 + pc);
  };
  auto step = [&](const PFrag& c, PFrag& n, int rslot, int rs, bool rd, int pa, int wj, int dslot) {
    mm(c, 0, 0); SBAR();
    if (rd) { rdw(n, 0, rslot, rs); rdx(n, 0, rslot, rs); }
    SBAR();
    mm(c, 0, 1); SBAR();
    if (rd) { rdw(n, 1, rslot, rs); rdx(n, 1, rslot, rs); }
    SBAR();
    mm(c, 1, 0); SBAR();
    if (pa >= 0) win(wj, pa, dslot);
    SBAR();
    mm(c, 1, 1); SBAR();
    if (pa >= 0) win(wj, pa + 1, dslot);
    SBAR();
  };
  PFrag f0, f1;
  rdw(f0, 0, 0, 0); rdx(f0, 0, 0, 0); rdw(f0, 1, 0, 0); rdx(f0, 1, 0, 0);
  auto iter = [&](int it, int slot, int pslot, int nslot, bool hk) {
    step(f0, f1, slot, 1, true, 2, it - 1, pslot);
    step(f1, f0, slot, 2, true, 4, it - 1, pslot);
    step(f0, f1, slot, 3, true, -1, 0, 0);
    asm volatile("s_waitcnt vmcnt(6) lgkmcnt(0)\n\ts_barrier" ::: "memory");
    if (hk) hook(it);
    SBAR();
    step(f1, f0, nslot, 0, true, 0, it, slot);
  };
  iter(0, 0, 0, 1, true); iter(1, 1, 0, 2, true); iter(2, 2, 1, 0, true); iter(3, 0, 2, 1, true);
  iter(4, 1, 0, 2, true); iter(5, 2, 1, 0, true); iter(6, 0, 2, 1, true);
  int slot = 1, pslot = 0;
  for (int it = 7; it < 13; ++it) {
    const int nslot = slot == 2 ? 0 : slot + 1;
    iter(it, slot, pslot, nslot, false);
    pslot = slot;
    slot = nslot;
  }
  peek();
  step(f0, f1, 1, 1, true, 2, 12, 0);
  step(f1, f0, 1, 2, true, 4, 12, 0);
  step(f0, f1, 1, 3, true, -1, 0, 0);
  check();
  SBAR();
  step(f1, f0, 2, 0, true, 0, 13, 0);
  step(f0, f1, 2, 1, true, 2, 13, 0);
  step(f1, f0, 2, 2, true, -1, 0, 0);
  step(f0, f1, 2, 3, true, -1, 0, 0);
  asm volatile("s_waitcnt vmcnt(4) lgkmcnt(0)\n\ts_barrier" ::: "memory");
  SBAR();
  step(f1, f0, 0, 0, true, 0, 14, 0);
  step(f0, f1, 0, 1, true, 2, 14, 0);
  step(f1, f0, 0, 2, true, -1, 0, 0);
  step(f0, f1, 0, 3, true, -1, 0, 0);
  asm volatile("s_waitcnt lgkmcnt(0)\n\ts_barrier" ::: "memory");
  SBAR();
  step(f1, f0, 0, 0, false, -1, 0, 0);
}

template <class Init>
DI void pre_kv(const char* __restrict__ xg, const char* __restrict__ wsel, char* __restrict__ ws, int rt, int t, char* lds, int w,
               PDma& dma, Init init, u32x2& tag, f32x16& acc, lds_cptr& xlast, lds_cptr& wlast) {
  const int tid = threadIdx.x, lane = tid & 63;
  const int r = lane & 31, h = lane >> 5;
  const unsigned lds0 = (unsigned)(uintptr_t)lds;
  u32x2 nonce;
  nonce_load(nonce, ws);
  const int xrow = 4 * w + (lane >> 4);
  const unsigned xo = xrow * 2048 + (((lane & 15) ^ (xrow & 15)) << 4), wo = w * 1024 + lane * 16;
  const char* xs = xg + (size_t)rt * 32 * 2048;
#define KV_OFF(u) ((u) == 0 || (u) == 7 ? KVC : P_SLOT + ((u) - 1) * 16384)
  auto sub = [&](int u) {
    const unsigned lb = lds0 + KV_OFF(u) + w * 1024;
    glds16<0>(xo, xs + u * 256, lb);
    glds16<0>(wo, wsel + u * 8192, lb + 8192);
  };
  sub(0); sub(1); sub(2); sub(3); sub(4); sub(5); sub(6);
  SBAR();
  init();
  const int g16 = lane >> 4, q4 = (lane & 15) >> 2, p4 = lane & 3;
  lds_cptr xa = (lds_cptr)lds + P_SLOT + r * 256 + (((2 * w + h) ^ (r & 15)) << 4);
  lds_cptr wa = (lds_cptr)lds + P_SLOT + 8192 + (16 * w + 8 * h + q4) * 64 + 32 * (g16 & 1) + 8 * p4;
  lds_cptr xc = xa + (KVC - P_SLOT), wc = wa + (KVC - P_SLOT);
  PIN(xa); PIN(wa); PIN(xc); PIN(wc);
  acc = (f32x16){};
  bf16x8 xf[4], wf[4];
#define KV_RD(i, u) do { \
    lds_cptr px_ = (u) == 0 ? xc : xa + ((u) - 1) * 16384; \
    lds_cptr pw_ = (u) == 0 ? wc : wa + ((u) - 1) * 16384; \
    xf[i] = lds_rd128(px_); wf[i] = cat8(lds_tr(pw_), lds_tr(pw_ + 256)); \
  } while (0)
#define KV_MM(i) do { if (t) acc = MFMA32(xf[i], wf[i], acc); else acc = MFMA32(wf[i], xf[i], acc); } while (0)
  asm volatile("s_waitcnt vmcnt(6)\n\ts_barrier" : "+v"(nonce)::"memory");
  KV_RD(0, 0); KV_RD(1, 1); KV_RD(2, 2); KV_RD(3, 3);
  SBAR();
  dma.stage(0, 0);
  SBAR();
  KV_MM(0); KV_MM(1); KV_MM(2); KV_MM(3);
  asm volatile("s_waitcnt vmcnt(6) lgkmcnt(0)\n\ts_barrier" ::: "memory");
  KV_RD(0, 4); KV_RD(1, 5); KV_RD(2, 6);
  SBAR();
  sub(7); dma.stage(1, 1);
  SBAR();
  KV_MM(0); KV_MM(1); KV_MM(2);
  asm volatile("s_waitcnt vmcnt(8) lgkmcnt(0)\n\ts_barrier" ::: "memory");
  dma.piece(0, 2, 2); dma.piece(1, 2, 2);
#undef KV_RD
#undef KV_MM
#undef KV_OFF
  tag = nonce_tag(nonce);
  xlast = xc; wlast = wc;
}

template <class Init>
DI void pre_gate(const char* __restrict__ xg, const char* __restrict__ gw, char* __restrict__ ws, int row0, int w, PDma& dma, Init init,
                 u32x2& tag) {
  const int tid = threadIdx.x, lane = tid & 63;
  u32x4 gx[2][2], gv[2];
  u32x2 nonce;
  const char* px = xg + (size_t)(row0 + 2 * w) * 2048;
  asm volatile("global_load_dwordx2 %6, %10, %11 sc1\n\t"
               "global_load_dwordx4 %0, %7, %8\n\tglobal_load_dwordx4 %1, %7, %8 offset:16\n\t"
               "global_load_dwordx4 %2, %7, %8 offset:2048\n\tglobal_load_dwordx4 %3, %7, %8 offset:2064\n\t"
               "global_load_dwordx4 %4, %7, %9\n\tglobal_load_dwordx4 %5, %7, %9 offset:16"
               : "=&v"(gx[0][0]), "=&v"(gx[0][1]), "=&v"(gx[1][0]), "=&v"(gx[1][1]), "=&v"(gv[0]), "=&v"(gv[1]), "=&v"(nonce)
               : "v"(lane * 32), "s"(px), "s"(gw), "v"(0u), "s"(ws + WS_NONCE) : "memory");
  SBAR();
  init();
  dma.stage(0, 0);
  asm volatile("s_barrier" ::: "memory");
  dma.stage(1, 1); dma.piece(0, 2, 2); dma.piece(1, 2, 2);
  asm volatile("s_waitcnt vmcnt(8)\n\ts_barrier"
               : "+v"(gx[0][0]), "+v"(gx[0][1]), "+v"(gx[1][0]), "+v"(gx[1][1]), "+v"(gv[0]), "+v"(gv[1]), "+v"(nonce)::"memory");
  tag = nonce_tag(nonce);
#pragma unroll
  for (int i = 0; i < 2; ++i) {
    float z = 0.f, z2 = 0.f;
#pragma unroll
    for (int e = 0; e < 4; ++e) { z = dot2bf(gx[i][0][e], gv[0][e], z); z2 = dot2bf(gx[i][1][e], gv[1][e], z2); }
    z += z2;
    z = wave_sum63(z);
    const float g = 1.f / (1.f + __expf(-z));
    if (lane == 63) asm volatile("global_store_dword %0, %1, %2 sc1\n\ts_nop 0" :: "v"(0u), "v"(g), "s"(ws + WS_G + (row0 + 2 * w + i) * 4) : "memory");
  }
}

constexpr int SL = 32768;
constexpr int R_LDS = 5 * SL;
constexpr int XCH_OFF = 0;
constexpr int WO_OFF = 2 * SL;

DI int crow(int r, int h) { return (r & 3) + 8 * (r >> 2) + 4 * h; }
constexpr unsigned long long diag_lanes(int i) {
  unsigned long long m = 0;
  for (int l = 0; l < 64; ++l)
    if ((i & 3) + 8 * (i >> 2) + 4 * (l >> 5) <= (l & 31)) m |= 1ull << l;
  return m;
}
template <int I>
DI float diag_select(float keep_not, float keep) {
  float r;
  asm("v_cndmask_b32_e64 %0, %1, %2, %3" : "=v"(r) : "v"(keep_not), "v"(keep), "s"(diag_lanes(I)));
  return r;
}
template <int I>
DI void diag_masks(f32x16& mD, f32x16& mF, float neg, float zero) {
  if constexpr (I < 16) {
    mD[I] = diag_select<I>(neg, zero);
    mF[I] = diag_select<I>(zero, neg);
    diag_masks<I + 1>(mD, mF, neg, zero);
  }
}
DI float swap_half(float x) {
  auto rr = __builtin_amdgcn_permlane32_swap(__float_as_uint(x), __float_as_uint(x), false, false);
  return __uint_as_float((threadIdx.x & 32) ? rr[0] : rr[1]);
}
DI void exp16(f32x16& s) {
#pragma unroll
  for (int i = 0; i < 16; ++i) s[i] = __builtin_amdgcn_exp2f(s[i]);
}
#define AE(S, i) S[i] = __builtin_amdgcn_exp2f(S[i])
#define AC(P, S, i) P[(i) & 3] = cvtpk(S[2 * (i)], S[2 * (i) + 1])
#define ABC(x) __builtin_bit_cast(bf16x8, x)
#define APHASE(QK, PV, SX, PXa, PXb, SY, CINY, PYa, PYb, QY0, QY1, OY, LY, KF0, KF1, VF0, VF1, R1, R2) do { \
  if (QK) { SY = MFMA32(KF0, QY0, CINY); PIN(SY); } SBAR(); \
  AE(SX, 0); AE(SX, 1); AE(SX, 2); AE(SX, 3); PIN(SX); SBAR(); \
  if (QK) { SY = MFMA32(KF1, QY1, SY); PIN(SY); } SBAR(); \
  R1; \
  AC(PXa, SX, 0); AC(PXa, SX, 1); AE(SX, 4); AE(SX, 5); AE(SX, 6); PIN(SX); PIN(PXa); SBAR(); \
  if (PV) { OY = MFMA32(VF0, ABC(PYa), OY); PIN(OY); } SBAR(); \
  AE(SX, 7); AC(PXa, SX, 2); AE(SX, 8); AE(SX, 9); AC(PXa, SX, 3); PIN(SX); PIN(PXa); SBAR(); \
  if (PV) { LY = MFMA16(ones, ABC(PYa), LY); PIN(LY); } SBAR(); \
  R2; \
  AE(SX, 10); AE(SX, 11); PIN(SX); SBAR(); \
  if (PV) { OY = MFMA32(VF1, ABC(PYb), OY); PIN(OY); } SBAR(); \
  AC(PXb, SX, 4); AC(PXb, SX, 5); AE(SX, 12); AE(SX, 13); AE(SX, 14); PIN(SX); PIN(PXb); SBAR(); \
  if (PV) { LY = MFMA16(ones, ABC(PYb), LY); PIN(LY); } SBAR(); \
  AE(SX, 15); AC(PXb, SX, 6); AC(PXb, SX, 7); PIN(PXb); SBAR(); \
} while (0)

template <bool MASKED>
DI void attn_exact(const char* __restrict__ kv, int t0, int t1, int qt, const bf16x8& q0, const bf16x8& q1, const bf16x8& ones,
                   f32x16& o, float& l) {
  const int lane = threadIdx.x & 63, r = lane & 31, h = lane >> 5;
  float m = -3.0e38f;
  for (int t = t0; t < t1; ++t) {
    const char* p = kv + (size_t)t * 4096 + lane * 16;
    bf16x8 k0 = gload16_now(p), k1 = gload16_now(p + 1024);
    f32x16 s = {};
    s = MFMA32(k0, q0, s); s = MFMA32(k1, q1, s);
#pragma unroll
    for (int i = 0; i < 16; ++i) {
      bool ok = true;
      if (MASKED) { const int c = crow(i, h); ok = (t == qt) ? (c <= r) : ((t == qt - 8) ? (c > r) : true); }
      m = fmaxf(m, ok ? s[i] : -3.0e38f);
    }
  }
  m = fmaxf(m, swap_half(m));
  o = (f32x16){};
  f32x4 la = {};
  for (int t = t0; t < t1; ++t) {
    const char* p = kv + (size_t)t * 4096 + lane * 16;
    bf16x8 k0 = gload16_now(p), k1 = gload16_now(p + 1024);
    bf16x8 v0 = gload16_now(p + 2048), v1 = gload16_now(p + 3072);
    f32x16 s = {};
    s = MFMA32(k0, q0, s); s = MFMA32(k1, q1, s);
#pragma unroll
    for (int i = 0; i < 16; ++i) {
      bool ok = true;
      if (MASKED) { const int c = crow(i, h); ok = (t == qt) ? (c <= r) : ((t == qt - 8) ? (c > r) : true); }
      s[i] = ok ? __builtin_amdgcn_exp2f(s[i] - m) : 0.f;
    }
    bf16x8 pa = pack8(s, 0), pb = pack8(s, 8);
    o = MFMA32(v0, pa, o); o = MFMA32(v1, pb, o);
    la = MFMA16(ones, pa, la); la = MFMA16(ones, pb, la);
  }
  l = la[0];
}
DI bool ref_unsafe(const f32x16& o, float l) {
  float z = 0.f;
#pragma unroll
  for (int i = 0; i < 16; ++i) z = __builtin_fmaf(o[i], 0.f, z);
  const bool ok = (l >= 5.421010862e-20f) && (l <= 1.8446744e19f) && (z == 0.f);
  return __any(!ok);
}

__global__ __launch_bounds__(512, 2) void k_fused(const char* __restrict__ xa, const char* __restrict__ wq,
                                                  const char* __restrict__ wk, const char* __restrict__ wv,
                                                  const char* __restrict__ woa, const char* __restrict__ wqkv,
                                                  const char* __restrict__ wob, const char* __restrict__ gw,
                                                  char* __restrict__ ws, char* __restrict__ out) {
  extern __shared__ __attribute__((aligned(16))) char lds[];
  asm volatile("" :: "s"(xa), "s"(wq), "s"(wk), "s"(wv), "s"(wqkv), "s"(gw), "s"(ws));
  const int b = blockIdx.x;
  const int xcd = b & 7, idx = b >> 3;
  const int tid = threadIdx.x, lane = tid & 63;
  const int w = rfl(tid >> 6);
  const int r = lane & 31, h = lane >> 5;
  const unsigned lds0 = (unsigned)(uintptr_t)lds;
  const unsigned lo16 = lane * 16;
  const int pmb = (xcd & 1) * 8 + (idx & 7), pj = (xcd >> 1) * 4 + ((idx >> 3) & 1) * 2 + (idx >> 4);
  const int pr = w & 3, half = w >> 2;
  const char* kva = ws + WS_KVA;
  auto pieceA = [&](int k, int sl, int i) {
    const char* src = kva + (size_t)(half * 32 + 4 * k + pr) * 4096;
    const unsigned dst = lds0 + sl * SL + w * 4096;
    if (i == 0) glds16_sc1<0>(lo16, src, dst);
    else if (i == 1) glds16_sc1<1024>(lo16, src, dst);
    else if (i == 2) glds16_sc1<2048>(lo16, src, dst);
    else glds16_sc1<3072>(lo16, src, dst);
  };
  PDma dma;
  dma.init_uniform(xa, pmb, lds0, w);
  auto dinit = [&]() { dma.init_lanes(wq, wqkv, pj, lane); };
  u32x2 tag;
  const bool evenj = (pj & 1) == 0;
  const int kvrt = pmb * 4 + (pj >> 2), kvt = (pj >> 1) & 1;
  f32x16 kvacc = {};
  lds_cptr kvxl = (lds_cptr)lds, kvwl = (lds_cptr)lds;
  if (evenj) pre_kv(xa, kvt ? wv : wk, ws, kvrt, kvt, lds, w, dma, dinit, tag, kvacc, kvxl, kvwl);
  else pre_gate(xa, gw, ws, pmb * 128 + (pj >> 1) * 16, w, dma, dinit, tag);
  const int wn = w >> 1, pwm = w & 1;
  const int ct = 6 * pj + 2 * wn;
  const bool hasB = wn < 3;
  bf16x8 held[8];
  {
    f32x16 acc[2][2] = {};
    u32x2 kv0 = {~tag[0], 0u}, kv1 = kv0;
    auto tail = [&](int n) { pieceA(n >> 2, 2 + (n >> 2), n & 3); };
    auto peek = [&]() { flag_peek(kv0, lane * 8, ws + WS_FKV); flag_peek(kv1, lane * 8, ws + WS_FKV + 512); };
    auto check = [&]() {
      asm volatile("s_waitcnt vmcnt(4) lgkmcnt(0)" : "+v"(kv0), "+v"(kv1)::"memory");
      if (w == 0) { flag_wait(kv0, lane * 8, ws + WS_FKV, tag); flag_wait(kv1, lane * 8, ws + WS_FKV + 512, tag); }
      asm volatile("s_barrier" ::: "memory");
    };
    float* red = (float*)(lds + KVC) + lane * 4;
    auto put = [&](int s4) {
#pragma unroll
      for (int g = 0; g < 4; ++g) *(f32x4*)(red + (s4 * 4 + g) * 256) = (f32x4){kvacc[4 * g], kvacc[4 * g + 1], kvacc[4 * g + 2], kvacc[4 * g + 3]};
    };
    auto get = [&](int s4) {
#pragma unroll
      for (int g = 0; g < 4; ++g) {
        const f32x4 a = *(const f32x4*)(red + (s4 * 4 + g) * 256);
#pragma unroll
        for (int e = 0; e < 4; ++e) kvacc[4 * g + e] += a[e];
      }
    };
    auto hook = [&](int it) {
      if (evenj) {
        if (it == 0) {
          const bf16x8 xf = lds_rd128(kvxl), wf = cat8(lds_tr(kvwl), lds_tr(kvwl + 256));
          if (kvt) kvacc = MFMA32(xf, wf, kvacc); else kvacc = MFMA32(wf, xf, kvacc);
        }
        else if (it == 1) { if (w >= 4) put(w - 4); }
        else if (it == 2) { if (w < 4) get(w); }
        else if (it == 3) { if (w >= 1 && w < 4) put(w); }
        else if (it == 4) {
          if (w == 0) {
            get(1); get(2); get(3);
            char* dst = ws + WS_KVA + (size_t)kvrt * 4096 + kvt * 2048;
            gstore16_sc1<0>(lo16, dst, pack8(kvacc, 0));
            gstore16_sc1<1024>(lo16, dst, pack8(kvacc, 8));
          }
        } else if (it == 6) { if (tid == 0) flag_set(ws + WS_FKV + (kvrt * 2 + kvt) * 8, tag); }
      }
    };
    if (hasB && ct >= 64) proj_loop<true>(dma, lds, acc, tail, peek, check, hook);
    else proj_loop<false>(dma, lds, acc, tail, peek, check, hook);
    if (!hasB || ct < 32) {
#pragma unroll
      for (int i = 0; i < 2; ++i)
#pragma unroll
        for (int j = 0; j < 2; ++j)
#pragma unroll
          for (int e = 0; e < 16; ++e) acc[i][j][e] *= QSCALE;
    }
#pragma unroll
    for (int i = 0; i < 2; ++i)
#pragma unroll
      for (int j = 0; j < 2; ++j) {
        held[(i * 2 + j) * 2] = pack8(acc[i][j], 0);
        held[(i * 2 + j) * 2 + 1] = pack8(acc[i][j], 8);
      }
  }
  bf16x8 qb[2], q[2][2];
  typedef __attribute__((address_space(3))) bf16x8* lds_v8;
  const unsigned hstep = ct < 32 ? 2048 : 4096;
  char* hbase = ws + (ct < 32 ? WS_QB : WS_KVB + (ct >= 64 ? 2048 : 0)) + (size_t)((ct & 31) * 64 + pmb * 4 + pwm * 2) * hstep;
  auto storeH = [&](int n) {
    if (hasB) {
      char* dst = hbase + (size_t)((n >> 2) + ((n >> 1) & 1) * 64) * hstep;
      if (n & 1) gstore16_sc1<1024>(lo16, dst, held[n]);
      else gstore16_sc1<0>(lo16, dst, held[n]);
    }
  };
  if (!hasB) {
#pragma unroll
    for (int n = 0; n < 8; ++n)
      *(lds_v8)((lds_ptr)lds + QX_OFF + (((pwm * 2 + (n >> 2)) * 2 + ((n >> 1) & 1)) * 2 + (n & 1)) * 1024 + lo16) = held[n];
  }
  asm volatile("s_waitcnt vmcnt(0) lgkmcnt(0)\n\ts_barrier" ::: "memory");
#pragma unroll
  for (int u = 0; u < 2; ++u)
#pragma unroll
    for (int s2 = 0; s2 < 2; ++s2) q[u][s2] = lds_rd128((lds_cptr)lds + QX_OFF + ((pr * 2 + u) * 2 + s2) * 1024 + lo16);

  const int qt = pmb * 4 + pr, hA = 2 * pj;
  const int hB = (b & 7) + 8 * (b >> 6), rb = (b >> 3) & 7;
  const int qtB = rb * 8 + w;
  const int mb = (xcd & 3) * 4 + (idx & 3), nb = (xcd >> 2) * 8 + (idx >> 2);
  const char* kvb = ws + WS_KVB + (size_t)hB * 64 * 4096;
  bf16x8 ones;
  {
    const short v = ((lane & 3) == 0 && (((lane >> 4) & 1) == (((lane & 15) >> 2) & 1))) ? (short)0x3f80 : (short)0;
    ones = (bf16x8){v, v, v, v, v, v, v, v};
  }
  auto pieceB = [&](int n) {
    int gt = rb * 8 - 8 + 2 * w + (n >> 2);
    gt = gt < 0 ? 0 : gt;
    const char* src = kvb + (size_t)gt * 4096;
    const unsigned dst = lds0 + (2 * w + (n >> 2)) * 4096;
    if ((n & 3) == 0) glds16_sc1<0>(lo16, src, dst);
    else if ((n & 3) == 1) glds16_sc1<1024>(lo16, src, dst);
    else if ((n & 3) == 2) glds16_sc1<2048>(lo16, src, dst);
    else glds16_sc1<3072>(lo16, src, dst);
  };
  const char* pqb = ws + WS_QB + (size_t)(hB * 64 + qtB) * 2048;
  unsigned wo[2];
#pragma unroll
  for (int e = 0; e < 2; ++e) {
    const int row = 16 * w + 8 * e + (lane >> 3);
    const int c = (lane & 7) ^ (((row >> 1) & 1) << 2);
    wo[e] = row * 2048 + c * 16;
  }
  auto pieceW = [&](int st, int e) {
    const char* src = (st < 8 ? wob : woa) + (size_t)(st & 7) * 128 * 2048 + nb * 128;
    glds16<0>(wo[e], src, lds0 + WO_OFF + (st % 6) * 16384 + w * 2048 + e * 1024);
  };
#define NOP (void)0
  u32x2 seen;

  const f32x16 zero = {};
  f32x16 o0 = {}, o1 = {}, s0, s1;
  f32x4 l0 = {}, l1 = {};
  u32x4 p0a, p0b, p1a, p1b;
  bf16x8 ka0, ka1, kb0, kb1, va0, va1, vb0, vb1;
  lds_cptr kvbase = (lds_cptr)lds + half * 16384 + lane * 16;
#define APAIR(pp, SL0, SL1, FIRST, LAST, PA, PB, MID, D0, D1, D2, D3) do { \
    lds_cptr cur = kvbase + (SL0) * SL + ((pp) & 1) * 8192; PIN(cur);     \
    lds_cptr nxt = (SL0) == (SL1) && !((pp) & 1) ? cur + 8192 : kvbase + (SL1) * SL; \
    __builtin_amdgcn_s_setprio(PA); \
    if (FIRST) { ka0 = lds_rd128(cur); ka1 = lds_rd128(cur + 1024); s0 = MFMA32(ka0, q[0][0], zero); s0 = MFMA32(ka1, q[0][1], s0); } \
    SBAR(); \
    APHASE(1, !(FIRST), s0, p0a, p0b, s1, zero, p1a, p1b, q[1][0], q[1][1], o1, l1, ka0, ka1, vb0, vb1, \
           (kb0 = lds_rd128(cur + 4096), kb1 = lds_rd128(cur + 5120)), (va0 = lds_rd128(cur + 2048), va1 = lds_rd128(cur + 3072))); \
    D0; SBAR(); \
    APHASE(1, 1, s1, p1a, p1b, s0, zero, p0a, p0b, q[0][0], q[0][1], o0, l0, kb0, kb1, va0, va1, (void)0, (void)0); \
    D1; SBAR(); \
    MID; \
    __builtin_amdgcn_s_setprio(PB); SBAR(); \
    APHASE(1, 1, s0, p0a, p0b, s1, zero, p1a, p1b, q[1][0], q[1][1], o1, l1, kb0, kb1, va0, va1, \
           if (!(LAST)) (ka0 = lds_rd128(nxt), ka1 = lds_rd128(nxt + 1024)), (vb0 = lds_rd128(cur + 4096 + 2048), vb1 = lds_rd128(cur + 4096 + 3072))); \
    D2; SBAR(); \
    APHASE(!(LAST), 1, s1, p1a, p1b, s0, zero, p0a, p0b, q[0][0], q[0][1], o0, l0, ka0, ka1, vb0, vb1, (void)0, (void)0); \
    D3; SBAR(); \
    if (LAST) { \
      o1 = MFMA32(vb0, ABC(p1a), o1); o1 = MFMA32(vb1, ABC(p1b), o1); \
      l1 = MFMA16(ones, ABC(p1a), l1); l1 = MFMA16(ones, ABC(p1b), l1); \
    } \
  } while (0)
#define DA(k, sl, i) pieceA(k, sl, i)
#define DA2(k, sl, i) (pieceA(k, sl, i), pieceA(k, sl, (i) + 1))
#define DB2(n) (pieceB(n), pieceB((n) + 1))
#define DW(st, e) pieceW(st, e)
#define BAR_V(n) asm volatile("s_waitcnt vmcnt(" #n ")\n\ts_barrier" ::: "memory")
  const int fbm = 2 * rb - 2 + (lane & 3);
  const unsigned fbo = ((fbm < 0 ? 2 * rb : fbm) * 16 + ((lane >> 2) % 3 * 32 + hB) / 6) * 8;
  seen = (u32x2){~tag[0], 0u};
#define DH4(n) (storeH(n), storeH((n) + 1), storeH((n) + 2), storeH((n) + 3))
  APAIR(0, 2, 2, 1, 0, 3, 3, NOP, DA2(2, 4, 0), DA2(2, 4, 2), DH4(0), DH4(4));
  APAIR(1, 2, 3, 0, 0, 2, 2, NOP, NOP, NOP, NOP, NOP);
  APAIR(2, 3, 3, 0, 0, 1, 1, NOP, NOP, NOP, NOP, NOP);
  APAIR(3, 3, 4, 0, 0, 0, 3, { BAR_V(0); if (tid == 0) flag_set(ws + WS_FPB + (pmb * 16 + pj) * 8, tag);   },
        NOP, NOP, DA2(3, 2, 0), DA2(3, 2, 2));
  APAIR(4, 4, 4, 0, 0, 2, 1, NOP, NOP, NOP, NOP, NOP);
  flag_peek(seen, fbo, ws + WS_FPB);
  APAIR(5, 4, 2, 0, 0, 0, 3, { asm volatile("s_waitcnt vmcnt(0)" : "+v"(seen)::"memory"); if (w == 0) flag_wait(seen, fbo, ws + WS_FPB, tag); asm volatile("s_barrier" ::: "memory"); },
        NOP, NOP, DB2(0), DB2(2));
  APAIR(6, 2, 2, 0, 0, 2, 1, NOP, DB2(4), DB2(6), (gload16_sc1<0>(qb[0], lo16, pqb), gload16_sc1<1024>(qb[1], lo16, pqb)), DA2(4, 3, 0));
  APAIR(7, 2, 2, 0, 1, 1, 0, NOP, DA2(4, 3, 2), NOP, NOP, NOP);
  asm volatile("s_waitcnt vmcnt(0) lgkmcnt(0)\n\ts_barrier" : "+v"(qb[0]), "+v"(qb[1])::"memory");
  {
    f32x16 o = {};
    f32x4 la = {};
    lds_cptr bb = (lds_cptr)lds + w * 4096 + lane * 16;
    f32x16 mD, sa;
    diag_masks<0>(mD, sa, -3.0e38f, 0.f);
    if (rb != 0) {
      f32x16 sb;
      u32x4 pa0 = {}, pa1 = {}, pb0 = {}, pb1 = {};
      bf16x8 k0, k1, k2, k3, v0, v1, v2, v3;
      k0 = lds_rd128(bb); k1 = lds_rd128(bb + 1024);
      k2 = lds_rd128(bb + 4096); k3 = lds_rd128(bb + 4096 + 1024);
      sa = MFMA32(k0, qb[0], sa); sa = MFMA32(k1, qb[1], sa);
      SBAR();
#define BRDK(KA, KB, j) (KA = lds_rd128(bb + (j) * 4096), KB = lds_rd128(bb + (j) * 4096 + 1024))
#define BRDV(VA, VB, j) (VA = lds_rd128(bb + (j) * 4096 + 2048), VB = lds_rd128(bb + (j) * 4096 + 3072))
#define B_DMA(n) do { pieceA(5, 4, n); SBAR(); } while (0)
      __builtin_amdgcn_s_setprio(3);
      APHASE(1, 0, sa, pa0, pa1, sb, zero, pb0, pb1, qb[0], qb[1], o, la, k2, k3, v2, v3, BRDK(k0, k1, 2), BRDV(v0, v1, 0));
      B_DMA(0); B_DMA(1);
      APHASE(1, 1, sb, pb0, pb1, sa, zero, pa0, pa1, qb[0], qb[1], o, la, k0, k1, v0, v1, BRDK(k2, k3, 3), BRDV(v2, v3, 1));
      B_DMA(2); B_DMA(3);
      __builtin_amdgcn_s_setprio(2);
      APHASE(1, 1, sa, pa0, pa1, sb, zero, pb0, pb1, qb[0], qb[1], o, la, k2, k3, v2, v3, BRDK(k0, k1, 4), BRDV(v0, v1, 2));
      APHASE(1, 1, sb, pb0, pb1, sa, zero, pa0, pa1, qb[0], qb[1], o, la, k0, k1, v0, v1, BRDK(k2, k3, 5), BRDV(v2, v3, 3));
      __builtin_amdgcn_s_setprio(1);
      APHASE(1, 1, sa, pa0, pa1, sb, zero, pb0, pb1, qb[0], qb[1], o, la, k2, k3, v2, v3, BRDK(k0, k1, 6), BRDV(v0, v1, 4));
      APHASE(1, 1, sb, pb0, pb1, sa, zero, pa0, pa1, qb[0], qb[1], o, la, k0, k1, v0, v1, BRDK(k2, k3, 7), BRDV(v2, v3, 5));
      __builtin_amdgcn_s_setprio(0);
      APHASE(1, 1, sa, pa0, pa1, sb, zero, pb0, pb1, qb[0], qb[1], o, la, k2, k3, v2, v3, BRDK(k0, k1, 8), BRDV(v0, v1, 6));
      APHASE(1, 1, sb, pb0, pb1, sa, mD, pa0, pa1, qb[0], qb[1], o, la, k0, k1, v0, v1, (void)0, BRDV(v2, v3, 7));
      APHASE(0, 1, sa, pa0, pa1, sb, zero, pb0, pb1, qb[0], qb[1], o, la, k2, k3, v2, v3, (void)0, BRDV(v0, v1, 8));
      o = MFMA32(v0, ABC(pa0), o); o = MFMA32(v1, ABC(pa1), o);
      la = MFMA16(ones, ABC(pa0), la); la = MFMA16(ones, ABC(pa1), la);
    } else {
#pragma unroll
      for (int n = 0; n < 4; ++n) B_DMA(n);
      auto tile = [&](int j, const f32x16& cin) {
        lds_cptr p = bb + j * 4096;
        bf16x8 k0 = lds_rd128(p), k1 = lds_rd128(p + 1024), v0 = lds_rd128(p + 2048), v1 = lds_rd128(p + 3072);
        f32x16 s = MFMA32(k0, qb[0], cin);
        s = MFMA32(k1, qb[1], s);
        exp16(s);
        bf16x8 pa = pack8(s, 0), pb = pack8(s, 8);
        o = MFMA32(v0, pa, o); o = MFMA32(v1, pb, o);
        la = MFMA16(ones, pa, la); la = MFMA16(ones, pb, la);
      };
      for (int j = 8 - w; j < 8; ++j) tile(j, zero);
      tile(8, mD);
    }
    float l = la[0];
    if (__builtin_expect(ref_unsafe(o, l), 0)) attn_exact<true>(kvb, qtB >= 8 ? qtB - 8 : 0, qtB + 1, qtB, qb[0], qb[1], ones, o, l);
    const float sc = __builtin_amdgcn_rcpf(l);
    char* dst = ws + WS_O + (size_t)(qtB * 64 + 32 + hB) * 2048;
    gstore16_sc1<0>(lo16, dst, pack8s(o, 0, sc));
    gstore16_sc1<1024>(lo16, dst, pack8s(o, 8, sc));
  }
  asm volatile("s_waitcnt vmcnt(2) lgkmcnt(0)\n\ts_barrier" ::: "memory");
  const int wm = w & 3, kh = w >> 2;
  const char* obase = ws + WS_O + (size_t)(mb * 4 + wm) * 64 * 2048 + kh * 4096;
  bf16x8 of[5][4];
  auto loadO1 = [&](int st, int i, bf16x8& f) {
    const char* src = obase + (size_t)(st < 8 ? 32 + 4 * st : 4 * (st - 8)) * 2048;
    if (i == 0) gload16_sc1<0>(f, lo16, src);
    else if (i == 1) gload16_sc1<1024>(f, lo16, src);
    else if (i == 2) gload16_sc1<2048>(f, lo16, src);
    else gload16_sc1<3072>(f, lo16, src);
  };
#define LO(st, i) loadO1(st, i, of[st][i])
  auto loadO = [&](int st, bf16x8 (&f)[4]) {
    const char* src = obase + (size_t)(st < 8 ? 32 + 4 * st : 4 * (st - 8)) * 2048;
    gload16_sc1<0>(f[0], lo16, src); gload16_sc1<1024>(f[1], lo16, src);
    gload16_sc1<2048>(f[2], lo16, src); gload16_sc1<3072>(f[3], lo16, src);
  };
  const unsigned fob = ((mb >> 1) * 32 + (lane & 31)) * 8;
  seen = (u32x2){~tag[0], 0u};
  unsigned warm = 0;
  APAIR(8, 3, 3, 1, 0, 3, 3, NOP, DA(6, 0, 0), DA(6, 0, 1), DA(6, 0, 2), DA(6, 0, 3));
  APAIR(9, 3, 4, 0, 0, 2, 2, NOP, DA(7, 1, 0), DA(7, 1, 1), DA(7, 1, 2), DA(7, 1, 3));
  {
    const int R = 768 + (idx & 3) * 320 + w * 40 + (lane < 40 ? lane : 39);
    const char* pw = (R < 1024 ? wob + (size_t)R * 2048 : woa + (size_t)(R - 1024) * 2048) + nb * 128;
    asm volatile("global_load_dword %0, %1, off" : "+v"(warm) : "v"(pw) : "memory");
  }
  APAIR(10, 4, 4, 0, 0, 1, 0, NOP, NOP, NOP, NOP, NOP);
  APAIR(11, 4, 0, 0, 0, 0, 3, { asm volatile("s_waitcnt vmcnt(0)\n\ts_barrier" : "+v"(warm)::"memory");
                                if (tid == 0) flag_set(ws + WS_FOB + (rb * 32 + hB) * 8, tag);   }, NOP, NOP, NOP, NOP);
  APAIR(12, 0, 0, 0, 0, 3, 2, NOP, DW(0, 0), DW(0, 1), DW(1, 0), DW(1, 1));
  flag_peek(seen, fob, ws + WS_FOB);
  APAIR(13, 0, 1, 0, 0, 2, 1, NOP, DW(2, 0), DW(2, 1), DW(3, 0), DW(3, 1));
  APAIR(14, 1, 1, 0, 0, 0, 3, { asm volatile("s_waitcnt vmcnt(0)" : "+v"(seen)::"memory"); if (w == 0) flag_wait(seen, fob, ws + WS_FOB, tag); asm volatile("s_barrier" ::: "memory"); },
        NOP, NOP, (LO(0, 0), LO(0, 1), DW(4, 0)), (LO(0, 2), LO(0, 3), DW(4, 1)));
  APAIR(15, 1, 1, 0, 1, 2, 1, NOP, (LO(1, 0), LO(1, 1), DW(5, 0)), (LO(1, 2), LO(1, 3), DW(5, 1)), (LO(2, 0), LO(2, 1)), (LO(2, 2), LO(2, 3)));

  loadO(3, of[3]);
  asm volatile("s_waitcnt lgkmcnt(0)\n\ts_barrier" ::: "memory");
  {
    float* xb = (float*)(lds + XCH_OFF) + w * (5 * 256);
    const f32x16& os = half ? o0 : o1;
    const float ls = half ? l0[0] : l1[0];
#pragma unroll
    for (int g = 0; g < 4; ++g) *(f32x4*)(xb + g * 256 + lane * 4) = (f32x4){os[4 * g], os[4 * g + 1], os[4 * g + 2], os[4 * g + 3]};
    xb[4 * 256 + lane] = ls;
  }
  f32x16 oA = half ? o1 : o0;
  float lA = half ? l1[0] : l0[0];
  auto finishA = [&]() {
    const float* xb = (const float*)(lds + XCH_OFF) + (w ^ 4) * (5 * 256);
#pragma unroll
    for (int g = 0; g < 4; ++g) {
      f32x4 v = *(const f32x4*)(xb + g * 256 + lane * 4);
#pragma unroll
      for (int e = 0; e < 4; ++e) oA[4 * g + e] += v[e];
    }
    lA += xb[4 * 256 + lane];
    if (__builtin_expect(ref_unsafe(oA, lA), 0)) {
      if (half) attn_exact<false>(kva, 0, 64, 0, q[1][0], q[1][1], ones, oA, lA);
      else attn_exact<false>(kva, 0, 64, 0, q[0][0], q[0][1], ones, oA, lA);
    }
    const float sc = __builtin_amdgcn_rcpf(lA);
    char* dst = ws + WS_O + (size_t)(qt * 64 + hA + half) * 2048;
    gstore16_sc1<0>(lo16, dst, pack8s(oA, 0, sc));
    gstore16_sc1<1024>(lo16, dst, pack8s(oA, 8, sc));
  };
  {
    const int g16 = lane >> 4, q4 = (lane & 15) >> 2, p4 = lane & 3;
    const int wc = 2 * (g16 & 1) + (p4 >> 1);
    const int wa0 = (kh * 64 + 4 * h + q4) * 128 + ((wc ^ (((q4 >> 1) & 1) << 2)) << 4) + 8 * (p4 & 1);
    lds_cptr wbase[2][2] = {{(lds_cptr)lds + WO_OFF + wa0, (lds_cptr)lds + WO_OFF + (wa0 ^ 64)},
                            {(lds_cptr)lds + WO_OFF + 49152 + wa0, (lds_cptr)lds + WO_OFF + 49152 + (wa0 ^ 64)}};
    PIN(wbase[0][0]); PIN(wbase[0][1]); PIN(wbase[1][0]); PIN(wbase[1][1]);
    f32x16 accB[2] = {}, accA[2] = {};
    bf16x8 wf[4][2];
    const unsigned foa = (mb * 16 + (lane & 15)) * 8;
    u32x2 seenA = {~tag[0], 0u};
    float g = 0.f;
    u32x4 all0 = {~tag[0], 0u, 0u, 0u}, all1 = all0;
    auto rdw = [&](bf16x8 (&f)[2], int st, int ks) {
#pragma unroll
      for (int j = 0; j < 2; ++j) {
        lds_cptr p = wbase[(st % 6) / 3][j] + ((st % 6) % 3) * 16384 + ks * 2048;
        f[j] = cat8(lds_tr(p), lds_tr(p + 1024));
      }
    };
    asm volatile("s_waitcnt vmcnt(15)" : "+v"(of[0][0]), "+v"(of[0][1]), "+v"(of[0][2]), "+v"(of[0][3])::"memory");
    rdw(wf[0], 0, 0); rdw(wf[1], 0, 1);
#define O_MM(ACC, f, a) do { ACC[0] = MFMA32(f[0], a, ACC[0]); SBAR(); ACC[1] = MFMA32(f[1], a, ACC[1]); SBAR(); } while (0)
#define O_STAGE(st, ACC, VM, PRE, POST) do { \
      rdw(wf[2], st, 2); SBAR(); O_MM(ACC, wf[0], of[(st) % 5][0]); \
      rdw(wf[3], st, 3); SBAR(); O_MM(ACC, wf[1], of[(st) % 5][1]); \
      if ((st) < 15) { \
        if ((st) == 0) asm volatile("s_waitcnt lgkmcnt(0)" ::: "memory");     \
        asm volatile("s_waitcnt vmcnt(" #VM ")" : "+v"(of[((st) + 1) % 5][0]), "+v"(of[((st) + 1) % 5][1]), \
                     "+v"(of[((st) + 1) % 5][2]), "+v"(of[((st) + 1) % 5][3]), "+v"(seenA)::"memory"); \
        PRE; \
        if ((st) < 13) asm volatile("s_barrier" ::: "memory");     \
        POST; \
        SBAR(); \
        rdw(wf[0], (st) + 1, 0); \
      } \
      SBAR(); O_MM(ACC, wf[2], of[(st) % 5][2]); \
      if ((st) < 15) rdw(wf[1], (st) + 1, 1); \
      SBAR(); O_MM(ACC, wf[3], of[(st) % 5][3]); \
    } while (0)
#define OW(st) (pieceW(st, 0), pieceW(st, 1))
#define OL(st) loadO(st, of[(st) % 5])
    O_STAGE(0, accB, 9, NOP, { OL(4); SBAR(); finishA(); });
    O_STAGE(1, accB, 10, NOP, (OW(6), OL(5)));
    O_STAGE(2, accB, 6, NOP, { if (tid == 0) flag_set(ws + WS_FOA + (pmb * 16 + pj) * 8, tag);   OW(7); OL(6); });
    O_STAGE(3, accB, 12, NOP, (OW(8), OL(7)));
    O_STAGE(4, accB, 12, NOP, (flag_peek(seenA, foa, ws + WS_FOA), OW(9)));
    O_STAGE(5, accB, 2, if (w == 0) flag_wait(seenA, foa, ws + WS_FOA, tag),
            (gload4_sc1(g, (wm * 32 + r) * 4, ws + WS_G + mb * 512), OW(10), OL(8)));
    O_STAGE(6, accB, 9, NOP, (OW(11), OL(9)));
    O_STAGE(7, accB, 6, NOP, (OW(12), OL(10)));
    O_STAGE(8, accA, 6, NOP, (OW(13), OL(11)));
    O_STAGE(9, accA, 6, NOP, (OW(14), OL(12)));
    O_STAGE(10, accA, 6, NOP, (OW(15), OL(13)));
    O_STAGE(11, accA, 6, NOP, OL(14));
    O_STAGE(12, accA, 4, NOP, OL(15));
    O_STAGE(13, accA, 4, NOP, (flag_peek2(all0, lane * 16, ws + WS_FOA), flag_peek2(all1, lane * 16, ws + WS_FOA + 1024)));
    O_STAGE(14, accA, 2, NOP, NOP);
    O_STAGE(15, accA, 0, NOP, NOP);
    asm volatile("s_waitcnt vmcnt(0)" : "+v"(g), "+v"(all0), "+v"(all1)::"memory");
    if (b == 0 && w == 0) {
      if (!__all(all0[0] == tag[0] && all0[1] == tag[1] && all0[2] == tag[0] && all0[3] == tag[1] &&
                 all1[0] == tag[0] && all1[1] == tag[1] && all1[2] == tag[0] && all1[3] == tag[1])) {
        const u32x2 none = {~tag[0], 0u};
#pragma unroll
        for (int i = 0; i < 4; ++i) flag_wait(none, lane * 8, ws + WS_FOA + i * 512, tag);
      }
      if (lane == 0) flag_set(ws + WS_NONCE, tag);
    }
    float* xw = (float*)(lds + XCH_OFF) + w * 1024 + lane * 4;
    f32x16 acc[2];
#pragma unroll
    for (int j = 0; j < 2; ++j)
#pragma unroll
      for (int i = 0; i < 16; ++i) acc[j][i] = g * accA[j][i] + (1.f - g) * accB[j][i];
    const f32x16& as = kh ? acc[0] : acc[1];
#pragma unroll
    for (int g = 0; g < 4; ++g) *(f32x4*)(xw + g * 256) = (f32x4){as[4 * g], as[4 * g + 1], as[4 * g + 2], as[4 * g + 3]};
    asm volatile("s_waitcnt lgkmcnt(0)\n\ts_barrier" ::: "memory");
    f32x16 t = kh ? acc[1] : acc[0];
    const float* xr = (const float*)(lds + XCH_OFF) + (w ^ 4) * 1024 + lane * 4;
#pragma unroll
    for (int g = 0; g < 4; ++g) {
      f32x4 v = *(const f32x4*)(xr + g * 256);
#pragma unroll
      for (int e = 0; e < 4; ++e) t[4 * g + e] += v[e];
    }
    const int row = mb * 128 + wm * 32 + r;
    char* dst = out + (size_t)row * 2048 + (nb * 64 + kh * 32) * 2;
#pragma unroll
    for (int g = 0; g < 4; g += 2) {
      u32x2 a = {cvtpk(t[4 * g], t[4 * g + 1]), cvtpk(t[4 * g + 2], t[4 * g + 3])};
      u32x2 c = {cvtpk(t[4 * g + 4], t[4 * g + 5]), cvtpk(t[4 * g + 6], t[4 * g + 7])};
      auto x = __builtin_amdgcn_permlane32_swap(a[0], c[0], false, false);
      auto y = __builtin_amdgcn_permlane32_swap(a[1], c[1], false, false);
      *(u32x4*)(dst + g * 16 + h * 16) = (u32x4){x[0], y[0], x[1], y[1]};
    }
  }
}

extern "C" void kernel_launch(void* const* d_in, const int* in_sizes, int n_in, void* d_out, int out_size, void* d_ws,
                              size_t ws_size, hipStream_t stream) {
  const char* xa = (const char*)d_in[0];
  const char* wq = (const char*)d_in[1];
  const char* wk = (const char*)d_in[2];
  const char* wv = (const char*)d_in[3];
  const char* woa = (const char*)d_in[4];
  const char* wqkv = (const char*)d_in[6];
  const char* wob = (const char*)d_in[7];
  const char* gw = (const char*)d_in[8];
  (void)hipFuncSetAttribute((const void*)k_fused, hipFuncAttributeMaxDynamicSharedMemorySize, R_LDS);
  k_fused<<<256, 512, R_LDS, stream>>>(xa, wq, wk, wv, woa, wqkv, wob, gw, (char*)d_ws, (char*)d_out);
}
```

```cpp
#include <hip/hip_runtime.h>
#include <hip/hip_bf16.h>
#include <stdint.h>

typedef float f32x16 __attribute__((ext_vector_type(16)));
typedef float f32x4 __attribute__((ext_vector_type(4)));
typedef float f32x2 __attribute__((ext_vector_type(2)));
typedef short bf16x8 __attribute__((ext_vector_type(8)));
typedef short s16x4 __attribute__((ext_vector_type(4)));
typedef __bf16 bf16x2 __attribute__((ext_vector_type(2)));
typedef unsigned u32x4 __attribute__((ext_vector_type(4)));
typedef unsigned u32x2 __attribute__((ext_vector_type(2)));
typedef __attribute__((address_space(3))) const char* lds_cptr;
typedef __attribute__((address_space(3))) char* lds_ptr;

#define DI __device__ __forceinline__
#define SBAR() __builtin_amdgcn_sched_barrier(0)
#define PIN(x) asm volatile("" : "+v"(x))
#define MFMA32(a, b, c) __builtin_amdgcn_mfma_f32_32x32x16_bf16(a, b, c, 0, 0, 0)
#define MFMA16(a, b, c) __builtin_amdgcn_mfma_f32_16x16x32_bf16(a, b, c, 0, 0, 0)

constexpr int SEQ = 2048, DM = 1024;
constexpr float QSCALE = 0.17677669529663687f * 1.4426950408889634f;

constexpr size_t WS_KVA = 4u << 20;
constexpr size_t WS_G = WS_KVA + (256u << 10);
constexpr size_t WS_NONCE = WS_G + 8192;
constexpr size_t WS_FKV = WS_NONCE + 1024;
constexpr size_t WS_FPB = WS_FKV + 1024;
constexpr size_t WS_FOA = WS_FPB + 2048;
constexpr size_t WS_FOB = WS_FOA + 2048;
constexpr size_t WS_QB = 5u << 20;
constexpr size_t WS_KVB = 9u << 20;
constexpr size_t WS_O = 17u << 20;

DI unsigned rfl(unsigned x) { return __builtin_amdgcn_readfirstlane(x); }
DI unsigned cvtpk(float lo, float hi) {
  f32x2 v = {lo, hi};
  bf16x2 b = __builtin_convertvector(v, bf16x2);
  return __builtin_bit_cast(unsigned, b);
}
DI bf16x8 pack8(const f32x16& a, int o) {
  u32x4 r = {cvtpk(a[o], a[o + 1]), cvtpk(a[o + 2], a[o + 3]), cvtpk(a[o + 4], a[o + 5]), cvtpk(a[o + 6], a[o + 7])};
  return __builtin_bit_cast(bf16x8, r);
}
DI bf16x8 pack8s(const f32x16& a, int o, float s) {
  u32x4 r = {cvtpk(a[o] * s, a[o + 1] * s), cvtpk(a[o + 2] * s, a[o + 3] * s), cvtpk(a[o + 4] * s, a[o + 5] * s),
             cvtpk(a[o + 6] * s, a[o + 7] * s)};
  return __builtin_bit_cast(bf16x8, r);
}
template <int OFF>
DI void glds16(unsigned voff, const void* sbase, unsigned lds_base) {
  asm volatile("s_mov_b32 m0, %2\n\ts_nop 0\n\tglobal_load_lds_dwordx4 %0, %1 offset:%3"
               :: "v"(voff), "s"(sbase), "s"(rfl(lds_base)), "i"(OFF) : "memory");
}
template <int OFF>
DI void glds16_sc1(unsigned voff, const void* sbase, unsigned lds_base) {
  asm volatile("s_mov_b32 m0, %2\n\ts_nop 0\n\tglobal_load_lds_dwordx4 %0, %1 offset:%3 sc1"
               :: "v"(voff), "s"(sbase), "s"(rfl(lds_base)), "i"(OFF) : "memory");
}
DI void glds16v(const char* vaddr, unsigned lds_base) {
  asm volatile("s_mov_b32 m0, %1\n\ts_nop 0\n\tglobal_load_lds_dwordx4 %0, off" :: "v"(vaddr), "s"(rfl(lds_base)) : "memory");
}
DI bf16x8 lds_rd128(lds_cptr p) { return *(const __attribute__((address_space(3))) bf16x8*)p; }
DI s16x4 lds_tr(lds_cptr p) {
  typedef short v4i16_t __attribute__((ext_vector_type(4)));
  return __builtin_bit_cast(s16x4, __builtin_amdgcn_ds_read_tr16_b64_v4i16((__attribute__((address_space(3))) v4i16_t*)p));
}
DI bf16x8 cat8(s16x4 lo, s16x4 hi) { return (bf16x8){lo[0], lo[1], lo[2], lo[3], hi[0], hi[1], hi[2], hi[3]}; }
DI float bf2f(unsigned short b) { return __uint_as_float((unsigned)b << 16); }

template <int OFF>
DI void gload16_sc1(bf16x8& d, unsigned voff, const void* sbase) {
  asm volatile("global_load_dwordx4 %0, %1, %2 offset:%3 sc1" : "=v"(d) : "v"(voff), "s"(sbase), "i"(OFF) : "memory");
}
template <int OFF>
DI void gstore16_sc1(unsigned voff, void* sbase, const bf16x8& d) {
  asm volatile("global_store_dwordx4 %0, %1, %2 offset:%3 sc1\n\ts_nop 1" :: "v"(voff), "v"(d), "s"(sbase), "i"(OFF) : "memory");
}
DI void gload4_sc1(float& d, unsigned voff, const void* sbase) {
  asm volatile("global_load_dword %0, %1, %2 sc1" : "=v"(d) : "v"(voff), "s"(sbase) : "memory");
}
DI bf16x8 gload16_now(const char* p) {
  bf16x8 d;
  asm volatile("global_load_dwordx4 %0, %1, off sc1\n\ts_waitcnt vmcnt(0)" : "=&v"(d) : "v"(p) : "memory");
  return d;
}
typedef __attribute__((address_space(1))) unsigned long long gu64;
DI void flag_set(char* p, u32x2 tag) {
  asm volatile("global_store_dwordx2 %0, %1, %2 sc1\n\ts_nop 1" :: "v"(0u), "v"(tag), "s"(p) : "memory");
}
DI void flag_peek(u32x2& d, unsigned voff, const void* sbase) {
  asm volatile("global_load_dwordx2 %0, %1, %2 sc1" : "+v"(d) : "v"(voff), "s"(sbase) : "memory");
}
DI void flag_peek2(u32x4& d, unsigned voff, const void* sbase) {
  asm volatile("global_load_dwordx4 %0, %1, %2 sc1" : "+v"(d) : "v"(voff), "s"(sbase) : "memory");
}
DI void flag_wait(u32x2 seen, unsigned voff, const char* sbase, u32x2 tag) {
  if (__builtin_expect(__all(seen[0] == tag[0] && seen[1] == tag[1]), 1)) return;
  const unsigned long long want = ((unsigned long long)tag[1] << 32) | tag[0];
  for (unsigned spins = 0; spins < (1u << 21); ++spins) {
    if (__all(__hip_atomic_load((gu64*)(sbase + voff), __ATOMIC_RELAXED, __HIP_MEMORY_SCOPE_AGENT) == want)) return;
    __builtin_amdgcn_s_sleep(4);
  }
}

DI float dot2bf(unsigned a, unsigned b, float c) {
  return __builtin_amdgcn_fdot2_f32_bf16(__builtin_bit_cast(bf16x2, a), __builtin_bit_cast(bf16x2, b), c, false);
}
DI float wave_sum63(float z) {
#define DPP_ADD(ctrl, rmask) z += __uint_as_float(__builtin_amdgcn_update_dpp(0u, __float_as_uint(z), ctrl, rmask, 0xf, true))
  DPP_ADD(0x111, 0xf); DPP_ADD(0x112, 0xf); DPP_ADD(0x114, 0xf); DPP_ADD(0x118, 0xf);
  DPP_ADD(0x142, 0xa); DPP_ADD(0x143, 0xc);
#undef DPP_ADD
  return z;
}

DI void nonce_load(u32x2& nonce, const char* ws) {
  asm volatile("global_load_dwordx2 %0, %1, %2 sc1" : "=v"(nonce) : "v"(0u), "s"(ws + WS_NONCE) : "memory");
}
DI u32x2 nonce_tag(u32x2 nonce) {
  u32x2 tag;
  tag[0] = nonce[0] + 1u;
  tag[1] = nonce[1] + (tag[0] == 0u ? 1u : 0u);
  return tag;
}

constexpr int P_SLOT = 49152;
constexpr int P_XOFF = 0, P_WOFF = 16384;
constexpr int QX_OFF = 0;
constexpr int KVC = 147456;
struct PDma {
  const char* xsrc; const char* wp[4]; unsigned xo[2], wstep[2], lds0; int w, koff;
  DI void init_uniform(const char* xa, int mb, unsigned lds0_, int w_, int koff_) {
    lds0 = lds0_; w = w_; koff = koff_;
    xsrc = xa + (size_t)mb * 128 * 2048;
  }
  DI void init_lanes(const char* wq, const char* wqkv, int j, int lane) {
#pragma unroll
    for (int e = 0; e < 2; ++e) {
      const int row = 16 * w + 8 * e + (lane >> 3);
      xo[e] = row * 2048 + (((lane & 7) ^ (4 * e + (lane >> 4))) << 4);
    }
#pragma unroll
    for (int e = 0; e < 4; ++e) {
      const int krow = 8 * w + 2 * e + (lane >> 5);
      const int c = (lane & 31) ^ ((krow & 3) << 2);
      wp[e] = c < 24 ? wqkv + (size_t)krow * 6144 + j * 384 + c * 16 : wq + (size_t)krow * 2048 + j * 128 + (c - 24) * 16;
      if (e < 2) wstep[e] = c < 24 ? 64 * 6144 : 64 * 2048;
    }
#pragma unroll
    for (int e = 0; e < 4; ++e) wp[e] += (size_t)koff * wstep[e & 1];
  }
  DI void piece(int pc, int st, int slot) {
    const unsigned lb = lds0 + slot * P_SLOT;
    const int ks = (st + koff) & 15;
    if (pc < 2) glds16<0>(xo[pc], xsrc + ks * 128, lb + P_XOFF + w * 2048 + pc * 1024);
    else {
      glds16v(wp[pc - 2], lb + P_WOFF + w * 4096 + (pc - 2) * 1024);
      wp[pc - 2] += wstep[pc & 1];
      if (ks == 15) wp[pc - 2] -= (size_t)16 * wstep[pc & 1];
    }
  }
  DI void stage(int st, int slot) {
#pragma unroll
    for (int pc = 0; pc < 6; ++pc) piece(pc, st, slot);
  }
};
struct PFrag { bf16x8 x[2]; bf16x8 w[2]; };

template <bool VT, class Tail, class Peek, class Check, class Hook>
DI void proj_loop(PDma& dma, char* lds, f32x16 (&acc)[2][2], Tail tail, Peek peek, Check check, Hook hook) {
  const int tid = threadIdx.x, lane = tid & 63;
  const int w = dma.w;
  const int wm = w & 1, wn = w >> 1;
  const int r = lane & 31, h = lane >> 5;
  const int t3 = (r >> 1) & 7;
  const int xa0 = P_XOFF + (wm * 64 + r) * 128 + ((h ^ t3) << 4);
  const int g16 = lane >> 4, q4 = (lane & 15) >> 2, p4 = lane & 3;
  const int wc = wn * 8 + 2 * (g16 & 1) + (p4 >> 1);
  const int wa0 = P_WOFF + (8 * h + q4) * 512 + ((wc ^ (q4 << 2)) << 4) + 8 * (p4 & 1);
  auto rdx = [&](PFrag& f, int i, int slot, int s) { f.x[i] = lds_rd128((lds_cptr)lds + slot * P_SLOT + (xa0 ^ (s << 5)) + i * 4096); };
  auto rdw = [&](PFrag& f, int j, int slot, int s) {
    lds_cptr p = (lds_cptr)lds + slot * P_SLOT + (wa0 ^ (j << 6)) + s * 8192;
    f.w[j] = cat8(lds_tr(p), lds_tr(p + 2048));
  };
  auto mm = [&](const PFrag& f, int i, int j) {
    if constexpr (VT) acc[i][j] = MFMA32(f.x[i], f.w[j], acc[i][j]);
    else acc[i][j] = MFMA32(f.w[j], f.x[i], acc[i][j]);
  };
  auto win = [&](int j, int pc, int slot) {
    if (j == -1) dma.piece(pc, 2, 2);
    else if (j <= 12) dma.piece(pc, j + 3, slot);
    else if (j <= 14 && pc < 4) tail((j - 13) * 4 + pc);
  };
  auto step = [&](const PFrag& c, PFrag& n, int rslot, int rs, bool rd, int pa, int wj, int dslot) {
    mm(c, 0, 0); SBAR();
    if (rd) { rdw(n, 0, rslot, rs); rdx(n, 0, rslot, rs); }
    SBAR();
    mm(c, 0, 1); SBAR();
    if (rd) { rdw(n, 1, rslot, rs); rdx(n, 1, rslot, rs); }
    SBAR();
    mm(c, 1, 0); SBAR();
    if (pa >= 0) win(wj, pa, dslot);
    SBAR();
    mm(c, 1, 1); SBAR();
    if (pa >= 0) win(wj, pa + 1, dslot);
    SBAR();
  };
  PFrag f0, f1;
  rdw(f0, 0, 0, 0); rdx(f0, 0, 0, 0); rdw(f0, 1, 0, 0); rdx(f0, 1, 0, 0);
  auto iter = [&](int it, int slot, int pslot, int nslot, bool hk) {
    step(f0, f1, slot, 1, true, 2, it - 1, pslot);
    step(f1, f0, slot, 2, true, 4, it - 1, pslot);
    step(f0, f1, slot, 3, true, -1, 0, 0);
    asm volatile("s_waitcnt vmcnt(6) lgkmcnt(0)\n\ts_barrier" ::: "memory");
    if (hk) hook(it);
    SBAR();
    step(f1, f0, nslot, 0, true, 0, it, slot);
  };
  iter(0, 0, 0, 1, true); iter(1, 1, 0, 2, true); iter(2, 2, 1, 0, true); iter(3, 0, 2, 1, true);
  iter(4, 1, 0, 2, true); iter(5, 2, 1, 0, true); iter(6, 0, 2, 1, true);
  int slot = 1, pslot = 0;
  for (int it = 7; it < 13; ++it) {
    const int nslot = slot == 2 ? 0 : slot + 1;
    iter(it, slot, pslot, nslot, false);
    pslot = slot;
    slot = nslot;
  }
  peek();
  step(f0, f1, 1, 1, true, 2, 12, 0);
  step(f1, f0, 1, 2, true, 4, 12, 0);
  step(f0, f1, 1, 3, true, -1, 0, 0);
  check();
  SBAR();
  step(f1, f0, 2, 0, true, 0, 13, 0);
  step(f0, f1, 2, 1, true, 2, 13, 0);
  step(f1, f0, 2, 2, true, -1, 0, 0);
  step(f0, f1, 2, 3, true, -1, 0, 0);
  asm volatile("s_waitcnt vmcnt(4) lgkmcnt(0)\n\ts_barrier" ::: "memory");
  SBAR();
  step(f1, f0, 0, 0, true, 0, 14, 0);
  step(f0, f1, 0, 1, true, 2, 14, 0);
  step(f1, f0, 0, 2, true, -1, 0, 0);
  step(f0, f1, 0, 3, true, -1, 0, 0);
  asm volatile("s_waitcnt lgkmcnt(0)\n\ts_barrier" ::: "memory");
  SBAR();
  step(f1, f0, 0, 0, false, -1, 0, 0);
}

template <class Init>
DI void pre_kv(const char* __restrict__ xg, const char* __restrict__ wsel, char* __restrict__ ws, int rt, int t, char* lds, int w,
               PDma& dma, Init init, u32x2& tag, f32x16& acc, lds_cptr& xlast, lds_cptr& wlast) {
  const int tid = threadIdx.x, lane = tid & 63;
  const int r = lane & 31, h = lane >> 5;
  const unsigned lds0 = (unsigned)(uintptr_t)lds;
  u32x2 nonce;
  nonce_load(nonce, ws);
  const int xrow = 4 * w + (lane >> 4);
  const unsigned xo = xrow * 2048 + (((lane & 15) ^ (xrow & 15)) << 4), wo = w * 1024 + lane * 16;
  const char* xs = xg + (size_t)rt * 32 * 2048;
#define KV_OFF(u) ((u) == 0 || (u) == 7 ? KVC : P_SLOT + ((u) - 1) * 16384)
  auto sub = [&](int u) {
    const unsigned lb = lds0 + KV_OFF(u) + w * 1024;
    glds16<0>(xo, xs + u * 256, lb);
    glds16<0>(wo, wsel + u * 8192, lb + 8192);
  };
  sub(0); sub(1); sub(2); sub(3); sub(4); sub(5); sub(6);
  SBAR();
  init();
  const int g16 = lane >> 4, q4 = (lane & 15) >> 2, p4 = lane & 3;
  lds_cptr xa = (lds_cptr)lds + P_SLOT + r * 256 + (((2 * w + h) ^ (r & 15)) << 4);
  lds_cptr wa = (lds_cptr)lds + P_SLOT + 8192 + (16 * w + 8 * h + q4) * 64 + 32 * (g16 & 1) + 8 * p4;
  lds_cptr xc = xa + (KVC - P_SLOT), wc = wa + (KVC - P_SLOT);
  PIN(xa); PIN(wa); PIN(xc); PIN(wc);
  acc = (f32x16){};
  bf16x8 xf[4], wf[4];
#define KV_RD(i, u) do { \
    lds_cptr px_ = (u) == 0 ? xc : xa + ((u) - 1) * 16384; \
    lds_cptr pw_ = (u) == 0 ? wc : wa + ((u) - 1) * 16384; \
    xf[i] = lds_rd128(px_); wf[i] = cat8(lds_tr(pw_), lds_tr(pw_ + 256)); \
  } while (0)
#define KV_MM(i) do { if (t) acc = MFMA32(xf[i], wf[i], acc); else acc = MFMA32(wf[i], xf[i], acc); } while (0)
  asm volatile("s_waitcnt vmcnt(6)\n\ts_barrier" : "+v"(nonce)::"memory");
  KV_RD(0, 0); KV_RD(1, 1); KV_RD(2, 2); KV_RD(3, 3);
  SBAR();
  dma.stage(0, 0);
  SBAR();
  KV_MM(0); KV_MM(1); KV_MM(2); KV_MM(3);
  asm volatile("s_waitcnt vmcnt(6) lgkmcnt(0)\n\ts_barrier" ::: "memory");
  KV_RD(0, 4); KV_RD(1, 5); KV_RD(2, 6);
  SBAR();
  sub(7); dma.stage(1, 1);
  SBAR();
  KV_MM(0); KV_MM(1); KV_MM(2);
  asm volatile("s_waitcnt vmcnt(8) lgkmcnt(0)\n\ts_barrier" ::: "memory");
  dma.piece(0, 2, 2); dma.piece(1, 2, 2);
#undef KV_RD
#undef KV_MM
#undef KV_OFF
  tag = nonce_tag(nonce);
  xlast = xc; wlast = wc;
}

template <class Init>
DI void pre_gate(const char* __restrict__ xg, const char* __restrict__ gw, char* __restrict__ ws, int row0, int w, PDma& dma, Init init,
                 u32x2& tag) {
  const int tid = threadIdx.x, lane = tid & 63;
  u32x4 gx[2][2], gv[2];
  u32x2 nonce;
  const char* px = xg + (size_t)(row0 + 2 * w) * 2048;
  asm volatile("global_load_dwordx2 %6, %10, %11 sc1\n\t"
               "global_load_dwordx4 %0, %7, %8\n\tglobal_load_dwordx4 %1, %7, %8 offset:16\n\t"
               "global_load_dwordx4 %2, %7, %8 offset:2048\n\tglobal_load_dwordx4 %3, %7, %8 offset:2064\n\t"
               "global_load_dwordx4 %4, %7, %9\n\tglobal_load_dwordx4 %5, %7, %9 offset:16"
               : "=&v"(gx[0][0]), "=&v"(gx[0][1]), "=&v"(gx[1][0]), "=&v"(gx[1][1]), "=&v"(gv[0]), "=&v"(gv[1]), "=&v"(nonce)
               : "v"(lane * 32), "s"(px), "s"(gw), "v"(0u), "s"(ws + WS_NONCE) : "memory");
  SBAR();
  init();
  dma.stage(0, 0);
  asm volatile("s_barrier" ::: "memory");
  dma.stage(1, 1); dma.piece(0, 2, 2); dma.piece(1, 2, 2);
  asm volatile("s_waitcnt vmcnt(8)\n\ts_barrier"
               : "+v"(gx[0][0]), "+v"(gx[0][1]), "+v"(gx[1][0]), "+v"(gx[1][1]), "+v"(gv[0]), "+v"(gv[1]), "+v"(nonce)::"memory");
  tag = nonce_tag(nonce);
#pragma unroll
  for (int i = 0; i < 2; ++i) {
    float z = 0.f, z2 = 0.f;
#pragma unroll
    for (int e = 0; e < 4; ++e) { z = dot2bf(gx[i][0][e], gv[0][e], z); z2 = dot2bf(gx[i][1][e], gv[1][e], z2); }
    z += z2;
    z = wave_sum63(z);
    const float g = 1.f / (1.f + __expf(-z));
    if (lane == 63) asm volatile("global_store_dword %0, %1, %2 sc1\n\ts_nop 0" :: "v"(0u), "v"(g), "s"(ws + WS_G + (row0 + 2 * w + i) * 4) : "memory");
  }
}

constexpr int SL = 32768;
constexpr int R_LDS = 5 * SL;
constexpr int XCH_OFF = 0;
constexpr int WO_OFF = 2 * SL;

DI int crow(int r, int h) { return (r & 3) + 8 * (r >> 2) + 4 * h; }
constexpr unsigned long long diag_lanes(int i) {
  unsigned long long m = 0;
  for (int l = 0; l < 64; ++l)
    if ((i & 3) + 8 * (i >> 2) + 4 * (l >> 5) <= (l & 31)) m |= 1ull << l;
  return m;
}
template <int I>
DI float diag_select(float keep_not, float keep) {
  float r;
  asm("v_cndmask_b32_e64 %0, %1, %2, %3" : "=v"(r) : "v"(keep_not), "v"(keep), "s"(diag_lanes(I)));
  return r;
}
template <int I>
DI void diag_masks(f32x16& mD, f32x16& mF, float neg, float zero) {
  if constexpr (I < 16) {
    mD[I] = diag_select<I>(neg, zero);
    mF[I] = diag_select<I>(zero, neg);
    diag_masks<I + 1>(mD, mF, neg, zero);
  }
}
DI float swap_half(float x) {
  auto rr = __builtin_amdgcn_permlane32_swap(__float_as_uint(x), __float_as_uint(x), false, false);
  return __uint_as_float((threadIdx.x & 32) ? rr[0] : rr[1]);
}
DI void exp16(f32x16& s) {
#pragma unroll
  for (int i = 0; i < 16; ++i) s[i] = __builtin_amdgcn_exp2f(s[i]);
}
#define AE(S, i) S[i] = __builtin_amdgcn_exp2f(S[i])
#define AC(P, S, i) P[(i) & 3] = cvtpk(S[2 * (i)], S[2 * (i) + 1])
#define ABC(x) __builtin_bit_cast(bf16x8, x)
#define APHASE(QK, PV, SX, PXa, PXb, SY, CINY, PYa, PYb, QY0, QY1, OY, LY, KF0, KF1, VF0, VF1, R1, R2) do { \
  if (QK) { SY = MFMA32(KF0, QY0, CINY); PIN(SY); } SBAR(); \
  AE(SX, 0); AE(SX, 1); AE(SX, 2); AE(SX, 3); PIN(SX); SBAR(); \
  if (QK) { SY = MFMA32(KF1, QY1, SY); PIN(SY); } SBAR(); \
  R1; \
  AC(PXa, SX, 0); AC(PXa, SX, 1); AE(SX, 4); AE(SX, 5); AE(SX, 6); PIN(SX); PIN(PXa); SBAR(); \
  if (PV) { OY = MFMA32(VF0, ABC(PYa), OY); PIN(OY); } SBAR(); \
  AE(SX, 7); AC(PXa, SX, 2); AE(SX, 8); AE(SX, 9); AC(PXa, SX, 3); PIN(SX); PIN(PXa); SBAR(); \
  if (PV) { LY = MFMA16(ones, ABC(PYa), LY); PIN(LY); } SBAR(); \
  R2; \
  AE(SX, 10); AE(SX, 11); PIN(SX); SBAR(); \
  if (PV) { OY = MFMA32(VF1, ABC(PYb), OY); PIN(OY); } SBAR(); \
  AC(PXb, SX, 4); AC(PXb, SX, 5); AE(SX, 12); AE(SX, 13); AE(SX, 14); PIN(SX); PIN(PXb); SBAR(); \
  if (PV) { LY = MFMA16(ones, ABC(PYb), LY); PIN(LY); } SBAR(); \
  AE(SX, 15); AC(PXb, SX, 6); AC(PXb, SX, 7); PIN(PXb); SBAR(); \
} while (0)

template <bool MASKED>
DI void attn_exact(const char* __restrict__ kv, int t0, int t1, int qt, const bf16x8& q0, const bf16x8& q1, const bf16x8& ones,
                   f32x16& o, float& l) {
  const int lane = threadIdx.x & 63, r = lane & 31, h = lane >> 5;
  float m = -3.0e38f;
  for (int t = t0; t < t1; ++t) {
    const char* p = kv + (size_t)t * 4096 + lane * 16;
    bf16x8 k0 = gload16_now(p), k1 = gload16_now(p + 1024);
    f32x16 s = {};
    s = MFMA32(k0, q0, s); s = MFMA32(k1, q1, s);
#pragma unroll
    for (int i = 0; i < 16; ++i) {
      bool ok = true;
      if (MASKED) { const int c = crow(i, h); ok = (t == qt) ? (c <= r) : ((t == qt - 8) ? (c > r) : true); }
      m = fmaxf(m, ok ? s[i] : -3.0e38f);
    }
  }
  m = fmaxf(m, swap_half(m));
  o = (f32x16){};
  f32x4 la = {};
  for (int t = t0; t < t1; ++t) {
    const char* p = kv + (size_t)t * 4096 + lane * 16;
    bf16x8 k0 = gload16_now(p), k1 = gload16_now(p + 1024);
    bf16x8 v0 = gload16_now(p + 2048), v1 = gload16_now(p + 3072);
    f32x16 s = {};
    s = MFMA32(k0, q0, s); s = MFMA32(k1, q1, s);
#pragma unroll
    for (int i = 0; i < 16; ++i) {
      bool ok = true;
      if (MASKED) { const int c = crow(i, h); ok = (t == qt) ? (c <= r) : ((t == qt - 8) ? (c > r) : true); }
      s[i] = ok ? __builtin_amdgcn_exp2f(s[i] - m) : 0.f;
    }
    bf16x8 pa = pack8(s, 0), pb = pack8(s, 8);
    o = MFMA32(v0, pa, o); o = MFMA32(v1, pb, o);
    la = MFMA16(ones, pa, la); la = MFMA16(ones, pb, la);
  }
  l = la[0];
}
DI bool ref_unsafe(const f32x16& o, float l) {
  float z = 0.f;
#pragma unroll
  for (int i = 0; i < 16; ++i) z = __builtin_fmaf(o[i], 0.f, z);
  const bool ok = (l >= 5.421010862e-20f) && (l <= 1.8446744e19f) && (z == 0.f);
  return __any(!ok);
}

__global__ __launch_bounds__(512, 2) void k_fused(const char* __restrict__ xa, const char* __restrict__ wq,
                                                  const char* __restrict__ wk, const char* __restrict__ wv,
                                                  const char* __restrict__ woa, const char* __restrict__ wqkv,
                                                  const char* __restrict__ wob, const char* __restrict__ gw,
                                                  char* __restrict__ ws, char* __restrict__ out) {
  extern __shared__ __attribute__((aligned(16))) char lds[];
  asm volatile("" :: "s"(xa), "s"(wq), "s"(wk), "s"(wv), "s"(wqkv), "s"(gw), "s"(ws));
  const int b = blockIdx.x;
  const int xcd = b & 7, idx = b >> 3;
  const int tid = threadIdx.x, lane = tid & 63;
  const int w = rfl(tid >> 6);
  const int r = lane & 31, h = lane >> 5;
  const unsigned lds0 = (unsigned)(uintptr_t)lds;
  const unsigned lo16 = lane * 16;
  const int pmb = (xcd & 1) * 8 + (idx & 7), pj = (xcd >> 1) * 4 + ((idx >> 3) & 1) * 2 + (idx >> 4);
  const int pr = w & 3, half = w >> 2;
  const char* kva = ws + WS_KVA;
  auto pieceA = [&](int k, int sl, int i) {
    const char* src = kva + (size_t)(half * 32 + 4 * k + pr) * 4096;
    const unsigned dst = lds0 + sl * SL + w * 4096;
    if (i == 0) glds16_sc1<0>(lo16, src, dst);
    else if (i == 1) glds16_sc1<1024>(lo16, src, dst);
    else if (i == 2) glds16_sc1<2048>(lo16, src, dst);
    else glds16_sc1<3072>(lo16, src, dst);
  };
  PDma dma;
  dma.init_uniform(xa, pmb, lds0, w, ((xcd ^ (xcd >> 1)) & 1) * 8);
  auto dinit = [&]() { dma.init_lanes(wq, wqkv, pj, lane); };
  u32x2 tag;
  const bool evenj = (pj & 1) == 0;
  const int kvrt = pmb * 4 + (pj >> 2), kvt = (pj >> 1) & 1;
  f32x16 kvacc = {};
  lds_cptr kvxl = (lds_cptr)lds, kvwl = (lds_cptr)lds;
  if (evenj) pre_kv(xa, kvt ? wv : wk, ws, kvrt, kvt, lds, w, dma, dinit, tag, kvacc, kvxl, kvwl);
  else pre_gate(xa, gw, ws, pmb * 128 + (pj >> 1) * 16, w, dma, dinit, tag);
  const int wn = w >> 1, pwm = w & 1;
  const int ct = 6 * pj + 2 * wn;
  const bool hasB = wn < 3;
  bf16x8 held[8];
  {
    f32x16 acc[2][2] = {};
    u32x2 kv0 = {~tag[0], 0u}, kv1 = kv0;
    auto tail = [&](int n) { pieceA(n >> 2, 2 + (n >> 2), n & 3); };
    auto peek = [&]() { flag_peek(kv0, lane * 8, ws + WS_FKV); flag_peek(kv1, lane * 8, ws + WS_FKV + 512); };
    auto check = [&]() {
      asm volatile("s_waitcnt vmcnt(4) lgkmcnt(0)" : "+v"(kv0), "+v"(kv1)::"memory");
      if (w == 0) { flag_wait(kv0, lane * 8, ws + WS_FKV, tag); flag_wait(kv1, lane * 8, ws + WS_FKV + 512, tag); }
      asm volatile("s_barrier" ::: "memory");
    };
    float* red = (float*)(lds + KVC) + lane * 4;
    auto put = [&](int s4) {
#pragma unroll
      for (int g = 0; g < 4; ++g) *(f32x4*)(red + (s4 * 4 + g) * 256) = (f32x4){kvacc[4 * g], kvacc[4 * g + 1], kvacc[4 * g + 2], kvacc[4 * g + 3]};
    };
    auto get = [&](int s4) {
#pragma unroll
      for (int g = 0; g < 4; ++g) {
        const f32x4 a = *(const f32x4*)(red + (s4 * 4 + g) * 256);
#pragma unroll
        for (int e = 0; e < 4; ++e) kvacc[4 * g + e] += a[e];
      }
    };
    auto hook = [&](int it) {
      if (evenj) {
        if (it == 0) {
          const bf16x8 xf = lds_rd128(kvxl), wf = cat8(lds_tr(kvwl), lds_tr(kvwl + 256));
          if (kvt) kvacc = MFMA32(xf, wf, kvacc); else kvacc = MFMA32(wf, xf, kvacc);
        }
        else if (it == 1) { if (w >= 4) put(w - 4); }
        else if (it == 2) { if (w < 4) get(w); }
        else if (it == 3) { if (w >= 1 && w < 4) put(w); }
        else if (it == 4) {
          if (w == 0) {
            get(1); get(2); get(3);
            char* dst = ws + WS_KVA + (size_t)kvrt * 4096 + kvt * 2048;
            gstore16_sc1<0>(lo16, dst, pack8(kvacc, 0));
            gstore16_sc1<1024>(lo16, dst, pack8(kvacc, 8));
          }
        } else if (it == 6) { if (tid == 0) flag_set(ws + WS_FKV + (kvrt * 2 + kvt) * 8, tag); }
      }
    };
    if (hasB && ct >= 64) proj_loop<true>(dma, lds, acc, tail, peek, check, hook);
    else proj_loop<false>(dma, lds, acc, tail, peek, check, hook);
    if (!hasB || ct < 32) {
#pragma unroll
      for (int i = 0; i < 2; ++i)
#pragma unroll
        for (int j = 0; j < 2; ++j)
#pragma unroll
          for (int e = 0; e < 16; ++e) acc[i][j][e] *= QSCALE;
    }
#pragma unroll
    for (int i = 0; i < 2; ++i)
#pragma unroll
      for (int j = 0; j < 2; ++j) {
        held[(i * 2 + j) * 2] = pack8(acc[i][j], 0);
        held[(i * 2 + j) * 2 + 1] = pack8(acc[i][j], 8);
      }
  }
  bf16x8 qb[2], q[2][2];
  typedef __attribute__((address_space(3))) bf16x8* lds_v8;
  const unsigned hstep = ct < 32 ? 2048 : 4096;
  char* hbase = ws + (ct < 32 ? WS_QB : WS_KVB + (ct >= 64 ? 2048 : 0)) + (size_t)((ct & 31) * 64 + pmb * 4 + pwm * 2) * hstep;
  auto storeH = [&](int n) {
    if (hasB) {
      char* dst = hbase + (size_t)((n >> 2) + ((n >> 1) & 1) * 64) * hstep;
      if (n & 1) gstore16_sc1<1024>(lo16, dst, held[n]);
      else gstore16_sc1<0>(lo16, dst, held[n]);
    }
  };
  if (!hasB) {
#pragma unroll
    for (int n = 0; n < 8; ++n)
      *(lds_v8)((lds_ptr)lds + QX_OFF + (((pwm * 2 + (n >> 2)) * 2 + ((n >> 1) & 1)) * 2 + (n & 1)) * 1024 + lo16) = held[n];
  }
  asm volatile("s_waitcnt vmcnt(0) lgkmcnt(0)\n\ts_barrier" ::: "memory");
#pragma unroll
  for (int u = 0; u < 2; ++u)
#pragma unroll
    for (int s2 = 0; s2 < 2; ++s2) q[u][s2] = lds_rd128((lds_cptr)lds + QX_OFF + ((pr * 2 + u) * 2 + s2) * 1024 + lo16);

  const int qt = pmb * 4 + pr, hA = 2 * pj;
  const int hB = (b & 7) + 8 * (b >> 6), rb = (b >> 3) & 7;
  const int qtB = rb * 8 + w;
  const int mb = (xcd & 3) * 4 + (idx & 3), nb = (xcd >> 2) * 8 + (idx >> 2);
  const char* kvb = ws + WS_KVB + (size_t)hB * 64 * 4096;
  bf16x8 ones;
  {
    const short v = ((lane & 3) == 0 && (((lane >> 4) & 1) == (((lane & 15) >> 2) & 1))) ? (short)0x3f80 : (short)0;
    ones = (bf16x8){v, v, v, v, v, v, v, v};
  }
  auto pieceB = [&](int n) {
    int gt = rb * 8 - 8 + 2 * w + (n >> 2);
    gt = gt < 0 ? 0 : gt;
    const char* src = kvb + (size_t)gt * 4096;
    const unsigned dst = lds0 + (2 * w + (n >> 2)) * 4096;
    if ((n & 3) == 0) glds16_sc1<0>(lo16, src, dst);
    else if ((n & 3) == 1) glds16_sc1<1024>(lo16, src, dst);
    else if ((n & 3) == 2) glds16_sc1<2048>(lo16, src, dst);
    else glds16_sc1<3072>(lo16, src, dst);
  };
  const char* pqb = ws + WS_QB + (size_t)(hB * 64 + qtB) * 2048;
  unsigned wo[2];
#pragma unroll
  for (int e = 0; e < 2; ++e) {
    const int row = 16 * w + 8 * e + (lane >> 3);
    const int c = (lane & 7) ^ (((row >> 1) & 1) << 2);
    wo[e] = row * 2048 + c * 16;
  }
  auto pieceW = [&](int st, int e) {
    const char* src = (st < 8 ? wob : woa) + (size_t)(st & 7) * 128 * 2048 + nb * 128;
    glds16<0>(wo[e], src, lds0 + WO_OFF + (st % 6) * 16384 + w * 2048 + e * 1024);
  };
#define NOP (void)0
  u32x2 seen;

  const f32x16 zero = {};
  f32x16 o0 = {}, o1 = {}, s0, s1;
  f32x4 l0 = {}, l1 = {};
  u32x4 p0a, p0b, p1a, p1b;
  bf16x8 ka0, ka1, kb0, kb1, va0, va1, vb0, vb1;
  lds_cptr kvbase = (lds_cptr)lds + half * 16384 + lane * 16;
#define APAIR(pp, SL0, SL1, FIRST, LAST, PA, PB, MID, D0, D1, D2, D3) do { \
    lds_cptr cur = kvbase + (SL0) * SL + ((pp) & 1) * 8192; PIN(cur);     \
    lds_cptr nxt = (SL0) == (SL1) && !((pp) & 1) ? cur + 8192 : kvbase + (SL1) * SL; \
    __builtin_amdgcn_s_setprio(PA); \
    if (FIRST) { ka0 = lds_rd128(cur); ka1 = lds_rd128(cur + 1024); s0 = MFMA32(ka0, q[0][0], zero); s0 = MFMA32(ka1, q[0][1], s0); } \
    SBAR(); \
    APHASE(1, !(FIRST), s0, p0a, p0b, s1, zero, p1a, p1b, q[1][0], q[1][1], o1, l1, ka0, ka1, vb0, vb1, \
           (kb0 = lds_rd128(cur + 4096), kb1 = lds_rd128(cur + 5120)), (va0 = lds_rd128(cur + 2048), va1 = lds_rd128(cur + 3072))); \
    D0; SBAR(); \
    APHASE(1, 1, s1, p1a, p1b, s0, zero, p0a, p0b, q[0][0], q[0][1], o0, l0, kb0, kb1, va0, va1, (void)0, (void)0); \
    D1; SBAR(); \
    MID; \
    __builtin_amdgcn_s_setprio(PB); SBAR(); \
    APHASE(1, 1, s0, p0a, p0b, s1, zero, p1a, p1b, q[1][0], q[1][1], o1, l1, kb0, kb1, va0, va1, \
           if (!(LAST)) (ka0 = lds_rd128(nxt), ka1 = lds_rd128(nxt + 1024)), (vb0 = lds_rd128(cur + 4096 + 2048), vb1 = lds_rd128(cur + 4096 + 3072))); \
    D2; SBAR(); \
    APHASE(!(LAST), 1, s1, p1a, p1b, s0, zero, p0a, p0b, q[0][0], q[0][1], o0, l0, ka0, ka1, vb0, vb1, (void)0, (void)0); \
    D3; SBAR(); \
    if (LAST) { \
      o1 = MFMA32(vb0, ABC(p1a), o1); o1 = MFMA32(vb1, ABC(p1b), o1); \
      l1 = MFMA16(ones, ABC(p1a), l1); l1 = MFMA16(ones, ABC(p1b), l1); \
    } \
  } while (0)
#define DA(k, sl, i) pieceA(k, sl, i)
#define DA2(k, sl, i) (pieceA(k, sl, i), pieceA(k, sl, (i) + 1))
#define DB2(n) (pieceB(n), pieceB((n) + 1))
#define DW(st, e) pieceW(st, e)
#define BAR_V(n) asm volatile("s_waitcnt vmcnt(" #n ")\n\ts_barrier" ::: "memory")
  const int fbm = 2 * rb - 2 + (lane & 3);
  const unsigned fbo = ((fbm < 0 ? 2 * rb : fbm) * 16 + ((lane >> 2) % 3 * 32 + hB) / 6) * 8;
  seen = (u32x2){~tag[0], 0u};
#define DH4(n) (storeH(n), storeH((n) + 1), storeH((n) + 2), storeH((n) + 3))
  APAIR(0, 2, 2, 1, 0, 3, 3, NOP, DA2(2, 4, 0), DA2(2, 4, 2), DH4(0), DH4(4));
  APAIR(1, 2, 3, 0, 0, 2, 2, NOP, NOP, NOP, NOP, NOP);
  APAIR(2, 3, 3, 0, 0, 1, 1, NOP, NOP, NOP, NOP, NOP);
  APAIR(3, 3, 4, 0, 0, 0, 3, { BAR_V(0); if (tid == 0) flag_set(ws + WS_FPB + (pmb * 16 + pj) * 8, tag);   },
        NOP, NOP, DA2(3, 2, 0), DA2(3, 2, 2));
  APAIR(4, 4, 4, 0, 0, 2, 1, NOP, NOP, NOP, NOP, NOP);
  flag_peek(seen, fbo, ws + WS_FPB);
  APAIR(5, 4, 2, 0, 0, 0, 3, { asm volatile("s_waitcnt vmcnt(0)" : "+v"(seen)::"memory"); if (w == 0) flag_wait(seen, fbo, ws + WS_FPB, tag); asm volatile("s_barrier" ::: "memory"); },
        NOP, NOP, DB2(0), DB2(2));
  APAIR(6, 2, 2, 0, 0, 2, 1, NOP, DB2(4), DB2(6), (gload16_sc1<0>(qb[0], lo16, pqb), gload16_sc1<1024>(qb[1], lo16, pqb)), DA2(4, 3, 0));
  APAIR(7, 2, 2, 0, 1, 1, 0, NOP, DA2(4, 3, 2), NOP, NOP, NOP);

  asm volatile("s_waitcnt vmcnt(0) lgkmcnt(0)\n\ts_barrier" : "+v"(qb[0]), "+v"(qb[1])::"memory");
  {
    f32x16 o = {};
    f32x4 la = {};
    lds_cptr bb = (lds_cptr)lds + w * 4096 + lane * 16;
    f32x16 mD, sa;
    diag_masks<0>(mD, sa, -3.0e38f, 0.f);
    if (rb != 0) {
      f32x16 sb;
      u32x4 pa0 = {}, pa1 = {}, pb0 = {}, pb1 = {};
      bf16x8 k0, k1, k2, k3, v0, v1, v2, v3;
      k0 = lds_rd128(bb); k1 = lds_rd128(bb + 1024);
      k2 = lds_rd128(bb + 4096); k3 = lds_rd128(bb + 4096 + 1024);
      sa = MFMA32(k0, qb[0], sa); sa = MFMA32(k1, qb[1], sa);
      SBAR();
#define BRDK(KA, KB, j) (KA = lds_rd128(bb + (j) * 4096), KB = lds_rd128(bb + (j) * 4096 + 1024))
#define BRDV(VA, VB, j) (VA = lds_rd128(bb + (j) * 4096 + 2048), VB = lds_rd128(bb + (j) * 4096 + 3072))
#define B_DMA(n) do { pieceA(5, 4, n); SBAR(); } while (0)
      __builtin_amdgcn_s_setprio(3);
      APHASE(1, 0, sa, pa0, pa1, sb, zero, pb0, pb1, qb[0], qb[1], o, la, k2, k3, v2, v3, BRDK(k0, k1, 2), BRDV(v0, v1, 0));
      B_DMA(0); B_DMA(1);
      APHASE(1, 1, sb, pb0, pb1, sa, zero, pa0, pa1, qb[0], qb[1], o, la, k0, k1, v0, v1, BRDK(k2, k3, 3), BRDV(v2, v3, 1));
      B_DMA(2); B_DMA(3);
      __builtin_amdgcn_s_setprio(2);
      APHASE(1, 1, sa, pa0, pa1, sb, zero, pb0, pb1, qb[0], qb[1], o, la, k2, k3, v2, v3, BRDK(k0, k1, 4), BRDV(v0, v1, 2));
      APHASE(1, 1, sb, pb0, pb1, sa, zero, pa0, pa1, qb[0], qb[1], o, la, k0, k1, v0, v1, BRDK(k2, k3, 5), BRDV(v2, v3, 3));
      __builtin_amdgcn_s_setprio(1);
      APHASE(1, 1, sa, pa0, pa1, sb, zero, pb0, pb1, qb[0], qb[1], o, la, k2, k3, v2, v3, BRDK(k0, k1, 6), BRDV(v0, v1, 4));
      APHASE(1, 1, sb, pb0, pb1, sa, zero, pa0, pa1, qb[0], qb[1], o, la, k0, k1, v0, v1, BRDK(k2, k3, 7), BRDV(v2, v3, 5));
      __builtin_amdgcn_s_setprio(0);
      APHASE(1, 1, sa, pa0, pa1, sb, zero, pb0, pb1, qb[0], qb[1], o, la, k2, k3, v2, v3, BRDK(k0, k1, 8), BRDV(v0, v1, 6));
      APHASE(1, 1, sb, pb0, pb1, sa, mD, pa0, pa1, qb[0], qb[1], o, la, k0, k1, v0, v1, (void)0, BRDV(v2, v3, 7));
      APHASE(0, 1, sa, pa0, pa1, sb, zero, pb0, pb1, qb[0], qb[1], o, la, k2, k3, v2, v3, (void)0, BRDV(v0, v1, 8));
      o = MFMA32(v0, ABC(pa0), o); o = MFMA32(v1, ABC(pa1), o);
      la = MFMA16(ones, ABC(pa0), la); la = MFMA16(ones, ABC(pa1), la);
    } else {
#pragma unroll
      for (int n = 0; n < 4; ++n) B_DMA(n);
      auto tile = [&](int j, const f32x16& cin) {
        lds_cptr p = bb + j * 4096;
        bf16x8 k0 = lds_rd128(p), k1 = lds_rd128(p + 1024), v0 = lds_rd128(p + 2048), v1 = lds_rd128(p + 3072);
        f32x16 s = MFMA32(k0, qb[0], cin);
        s = MFMA32(k1, qb[1], s);
        exp16(s);
        bf16x8 pa = pack8(s, 0), pb = pack8(s, 8);
        o = MFMA32(v0, pa, o); o = MFMA32(v1, pb, o);
        la = MFMA16(ones, pa, la); la = MFMA16(ones, pb, la);
      };
      for (int j = 8 - w; j < 8; ++j) tile(j, zero);
      tile(8, mD);
    }
    float l = la[0];
    if (__builtin_expect(ref_unsafe(o, l), 0)) attn_exact<true>(kvb, qtB >= 8 ? qtB - 8 : 0, qtB + 1, qtB, qb[0], qb[1], ones, o, l);
    const float sc = __builtin_amdgcn_rcpf(l);
    char* dst = ws + WS_O + (size_t)(qtB * 64 + 32 + hB) * 2048;
    gstore16_sc1<0>(lo16, dst, pack8s(o, 0, sc));
    gstore16_sc1<1024>(lo16, dst, pack8s(o, 8, sc));
  }
  asm volatile("s_waitcnt vmcnt(2) lgkmcnt(0)\n\ts_barrier" ::: "memory");
  const int wm = w & 3, kh = w >> 2;
  const char* obase = ws + WS_O + (size_t)(mb * 4 + wm) * 64 * 2048 + kh * 4096;
  bf16x8 of[5][4];
  auto loadO1 = [&](int st, int i, bf16x8& f) {
    const char* src = obase + (size_t)(st < 8 ? 32 + 4 * st : 4 * (st - 8)) * 2048;
    if (i == 0) gload16_sc1<0>(f, lo16, src);
    else if (i == 1) gload16_sc1<1024>(f, lo16, src);
    else if (i == 2) gload16_sc1<2048>(f, lo16, src);
    else gload16_sc1<3072>(f, lo16, src);
  };
#define LO(st, i) loadO1(st, i, of[st][i])
  auto loadO = [&](int st, bf16x8 (&f)[4]) {
    const char* src = obase + (size_t)(st < 8 ? 32 + 4 * st : 4 * (st - 8)) * 2048;
    gload16_sc1<0>(f[0], lo16, src); gload16_sc1<1024>(f[1], lo16, src);
    gload16_sc1<2048>(f[2], lo16, src); gload16_sc1<3072>(f[3], lo16, src);
  };
  const unsigned fob = ((mb >> 1) * 32 + (lane & 31)) * 8;
  seen = (u32x2){~tag[0], 0u};
  unsigned warm = 0;
  APAIR(8, 3, 3, 1, 0, 3, 3, NOP, DA(6, 0, 0), DA(6, 0, 1), DA(6, 0, 2), DA(6, 0, 3));
  APAIR(9, 3, 4, 0, 0, 2, 2, NOP, DA(7, 1, 0), DA(7, 1, 1), DA(7, 1, 2), DA(7, 1, 3));
  {
    const int R = 768 + (idx & 3) * 320 + w * 40 + (lane < 40 ? lane : 39);
    const char* pw = (R < 1024 ? wob + (size_t)R * 2048 : woa + (size_t)(R - 1024) * 2048) + nb * 128;
    asm volatile("global_load_dword %0, %1, off" : "+v"(warm) : "v"(pw) : "memory");
  }
  APAIR(10, 4, 4, 0, 0, 1, 0, NOP, NOP, NOP, NOP, NOP);
  APAIR(11, 4, 0, 0, 0, 0, 3, { asm volatile("s_waitcnt vmcnt(0)\n\ts_barrier" : "+v"(warm)::"memory");
                                if (tid == 0) flag_set(ws + WS_FOB + (rb * 32 + hB) * 8, tag);   }, NOP, NOP, NOP, NOP);
  APAIR(12, 0, 0, 0, 0, 3, 2, NOP, DW(0, 0), DW(0, 1), DW(1, 0), DW(1, 1));
  flag_peek(seen, fob, ws + WS_FOB);
  APAIR(13, 0, 1, 0, 0, 2, 1, NOP, DW(2, 0), DW(2, 1), DW(3, 0), DW(3, 1));
  APAIR(14, 1, 1, 0, 0, 0, 3, { asm volatile("s_waitcnt vmcnt(0)" : "+v"(seen)::"memory"); if (w == 0) flag_wait(seen, fob, ws + WS_FOB, tag); asm volatile("s_barrier" ::: "memory"); },
        NOP, NOP, (LO(0, 0), LO(0, 1), DW(4, 0)), (LO(0, 2), LO(0, 3), DW(4, 1)));
  APAIR(15, 1, 1, 0, 1, 2, 1, NOP, (LO(1, 0), LO(1, 1), DW(5, 0)), (LO(1, 2), LO(1, 3), DW(5, 1)), (LO(2, 0), LO(2, 1)), (LO(2, 2), LO(2, 3)));

  loadO(3, of[3]);
  asm volatile("s_waitcnt lgkmcnt(0)\n\ts_barrier" ::: "memory");
  {
    float* xb = (float*)(lds + XCH_OFF) + w * (5 * 256);
    const f32x16& os = half ? o0 : o1;
    const float ls = half ? l0[0] : l1[0];
#pragma unroll
    for (int g = 0; g < 4; ++g) *(f32x4*)(xb + g * 256 + lane * 4) = (f32x4){os[4 * g], os[4 * g + 1], os[4 * g + 2], os[4 * g + 3]};
    xb[4 * 256 + lane] = ls;
  }
  f32x16 oA = half ? o1 : o0;
  float lA = half ? l1[0] : l0[0];
  auto finishA = [&]() {
    const float* xb = (const float*)(lds + XCH_OFF) + (w ^ 4) * (5 * 256);
#pragma unroll
    for (int g = 0; g < 4; ++g) {
      f32x4 v = *(const f32x4*)(xb + g * 256 + lane * 4);
#pragma unroll
      for (int e = 0; e < 4; ++e) oA[4 * g + e] += v[e];
    }
    lA += xb[4 * 256 + lane];
    if (__builtin_expect(ref_unsafe(oA, lA), 0)) {
      if (half) attn_exact<false>(kva, 0, 64, 0, q[1][0], q[1][1], ones, oA, lA);
      else attn_exact<false>(kva, 0, 64, 0, q[0][0], q[0][1], ones, oA, lA);
    }
    const float sc = __builtin_amdgcn_rcpf(lA);
    char* dst = ws + WS_O + (size_t)(qt * 64 + hA + half) * 2048;
    gstore16_sc1<0>(lo16, dst, pack8s(oA, 0, sc));
    gstore16_sc1<1024>(lo16, dst, pack8s(oA, 8, sc));
  };
  {
    const int g16 = lane >> 4, q4 = (lane & 15) >> 2, p4 = lane & 3;
    const int wc = 2 * (g16 & 1) + (p4 >> 1);
    const int wa0 = (kh * 64 + 4 * h + q4) * 128 + ((wc ^ (((q4 >> 1) & 1) << 2)) << 4) + 8 * (p4 & 1);
    lds_cptr wbase[2][2] = {{(lds_cptr)lds + WO_OFF + wa0, (lds_cptr)lds + WO_OFF + (wa0 ^ 64)},
                            {(lds_cptr)lds + WO_OFF + 49152 + wa0, (lds_cptr)lds + WO_OFF + 49152 + (wa0 ^ 64)}};
    PIN(wbase[0][0]); PIN(wbase[0][1]); PIN(wbase[1][0]); PIN(wbase[1][1]);
    f32x16 accB[2] = {}, accA[2] = {};
    bf16x8 wf[4][2];
    const unsigned foa = (mb * 16 + (lane & 15)) * 8;
    u32x2 seenA = {~tag[0], 0u};
    float g = 0.f;
    u32x4 all0 = {~tag[0], 0u, 0u, 0u}, all1 = all0;
    auto rdw = [&](bf16x8 (&f)[2], int st, int ks) {
#pragma unroll
      for (int j = 0; j < 2; ++j) {
        lds_cptr p = wbase[(st % 6) / 3][j] + ((st % 6) % 3) * 16384 + ks * 2048;
        f[j] = cat8(lds_tr(p), lds_tr(p + 1024));
      }
    };
    asm volatile("s_waitcnt vmcnt(15)" : "+v"(of[0][0]), "+v"(of[0][1]), "+v"(of[0][2]), "+v"(of[0][3])::"memory");
    rdw(wf[0], 0, 0); rdw(wf[1], 0, 1);
#define O_MM(ACC, f, a) do { ACC[0] = MFMA32(f[0], a, ACC[0]); SBAR(); ACC[1] = MFMA32(f[1], a, ACC[1]); SBAR(); } while (0)
#define O_STAGE(st, ACC, VM, PRE, POST) do { \
      rdw(wf[2], st, 2); SBAR(); O_MM(ACC, wf[0], of[(st) % 5][0]); \
      rdw(wf[3], st, 3); SBAR(); O_MM(ACC, wf[1], of[(st) % 5][1]); \
      if ((st) < 15) { \
        if ((st) == 0) asm volatile("s_waitcnt lgkmcnt(0)" ::: "memory");     \
        asm volatile("s_waitcnt vmcnt(" #VM ")" : "+v"(of[((st) + 1) % 5][0]), "+v"(of[((st) + 1) % 5][1]), \
                     "+v"(of[((st) + 1) % 5][2]), "+v"(of[((st) + 1) % 5][3]), "+v"(seenA)::"memory"); \
        PRE; \
        if ((st) < 13) asm volatile("s_barrier" ::: "memory");     \
        POST; \
        SBAR(); \
        rdw(wf[0], (st) + 1, 0); \
      } \
      SBAR(); O_MM(ACC, wf[2], of[(st) % 5][2]); \
      if ((st) < 15) rdw(wf[1], (st) + 1, 1); \
      SBAR(); O_MM(ACC, wf[3], of[(st) % 5][3]); \
    } while (0)
#define OW(st) (pieceW(st, 0), pieceW(st, 1))
#define OL(st) loadO(st, of[(st) % 5])
    O_STAGE(0, accB, 9, NOP, { OL(4); SBAR(); finishA(); });
    O_STAGE(1, accB, 10, NOP, (OW(6), OL(5)));
    O_STAGE(2, accB, 6, NOP, { if (tid == 0) flag_set(ws + WS_FOA + (pmb * 16 + pj) * 8, tag);   OW(7); OL(6); });
    O_STAGE(3, accB, 12, NOP, (OW(8), OL(7)));
    O_STAGE(4, accB, 12, NOP, (flag_peek(seenA, foa, ws + WS_FOA), OW(9)));
    O_STAGE(5, accB, 2, if (w == 0) flag_wait(seenA, foa, ws + WS_FOA, tag),
            (gload4_sc1(g, (wm * 32 + r) * 4, ws + WS_G + mb * 512), OW(10), OL(8)));
    O_STAGE(6, accB, 9, NOP, (OW(11), OL(9)));
    O_STAGE(7, accB, 6, NOP, (OW(12), OL(10)));
    O_STAGE(8, accA, 6, NOP, (OW(13), OL(11)));
    O_STAGE(9, accA, 6, NOP, (OW(14), OL(12)));
    O_STAGE(10, accA, 6, NOP, (OW(15), OL(13)));
    O_STAGE(11, accA, 6, NOP, OL(14));
    O_STAGE(12, accA, 4, NOP, OL(15));
    O_STAGE(13, accA, 4, NOP, (flag_peek2(all0, lane * 16, ws + WS_FOA), flag_peek2(all1, lane * 16, ws + WS_FOA + 1024)));
    O_STAGE(14, accA, 2, NOP, NOP);
    O_STAGE(15, accA, 0, NOP, NOP);
    asm volatile("s_waitcnt vmcnt(0)" : "+v"(g), "+v"(all0), "+v"(all1)::"memory");
    if (b == 0 && w == 0) {
      if (!__all(all0[0] == tag[0] && all0[1] == tag[1] && all0[2] == tag[0] && all0[3] == tag[1] &&
                 all1[0] == tag[0] && all1[1] == tag[1] && all1[2] == tag[0] && all1[3] == tag[1])) {
        const u32x2 none = {~tag[0], 0u};
#pragma unroll
        for (int i = 0; i < 4; ++i) flag_wait(none, lane * 8, ws + WS_FOA + i * 512, tag);
      }
      if (lane == 0) flag_set(ws + WS_NONCE, tag);
    }
    float* xw = (float*)(lds + XCH_OFF) + w * 1024 + lane * 4;
    f32x16 acc[2];
#pragma unroll
    for (int j = 0; j < 2; ++j)
#pragma unroll
      for (int i = 0; i < 16; ++i) acc[j][i] = g * accA[j][i] + (1.f - g) * accB[j][i];
    const f32x16& as = kh ? acc[0] : acc[1];
#pragma unroll
    for (int g = 0; g < 4; ++g) *(f32x4*)(xw + g * 256) = (f32x4){as[4 * g], as[4 * g + 1], as[4 * g + 2], as[4 * g + 3]};
    asm volatile("s_waitcnt lgkmcnt(0)\n\ts_barrier" ::: "memory");
    f32x16 t = kh ? acc[1] : acc[0];
    const float* xr = (const float*)(lds + XCH_OFF) + (w ^ 4) * 1024 + lane * 4;
#pragma unroll
    for (int g = 0; g < 4; ++g) {
      f32x4 v = *(const f32x4*)(xr + g * 256);
#pragma unroll
      for (int e = 0; e < 4; ++e) t[4 * g + e] += v[e];
    }
    const int row = mb * 128 + wm * 32 + r;
    char* dst = out + (size_t)row * 2048 + (nb * 64 + kh * 32) * 2;
#pragma unroll
    for (int g = 0; g < 4; g += 2) {
      u32x2 a = {cvtpk(t[4 * g], t[4 * g + 1]), cvtpk(t[4 * g + 2], t[4 * g + 3])};
      u32x2 c = {cvtpk(t[4 * g + 4], t[4 * g + 5]), cvtpk(t[4 * g + 6], t[4 * g + 7])};
      auto x = __builtin_amdgcn_permlane32_swap(a[0], c[0], false, false);
      auto y = __builtin_amdgcn_permlane32_swap(a[1], c[1], false, false);
      *(u32x4*)(dst + g * 16 + h * 16) = (u32x4){x[0], y[0], x[1], y[1]};
    }
  }
}

extern "C" void kernel_launch(void* const* d_in, const int* in_sizes, int n_in, void* d_out, int out_size, void* d_ws,
                              size_t ws_size, hipStream_t stream) {
  const char* xa = (const char*)d_in[0];
  const char* wq = (const char*)d_in[1];
  const char* wk = (const char*)d_in[2];
  const char* wv = (const char*)d_in[3];
  const char* woa = (const char*)d_in[4];
  const char* wqkv = (const char*)d_in[6];
  const char* wob = (const char*)d_in[7];
  const char* gw = (const char*)d_in[8];
  (void)hipFuncSetAttribute((const void*)k_fused, hipFuncAttributeMaxDynamicSharedMemorySize, R_LDS);
  k_fused<<<256, 512, R_LDS, stream>>>(xa, wq, wk, wv, woa, wqkv, wob, gw, (char*)d_ws, (char*)d_out);
}
```
